```python
import torch

def _softmax(x, axis=-1):
    return torch.softmax(x, dim=axis)
def _gelu(x):
    return torch.nn.functional.gelu(x, approximate="tanh")
def _silu(x):
    return torch.nn.functional.silu(x)
def _sigmoid(x):
    return torch.sigmoid(x)
def _layernorm(x, axis=-1, eps=1e-5):
    m = x.mean(dim=axis, keepdim=True)
    v = x.var(dim=axis, keepdim=True, unbiased=False)
    return (x - m) / torch.sqrt(v + eps)
def _scatter_add(tgt, idx, src, axis=0):
    return tgt.clone().index_add_(axis, idx, src)
def _segment_sum(src, seg, n):
    out = torch.zeros((n,) + src.shape[1:], dtype=src.dtype)
    return out.index_add_(0, seg, src)
def _pad(x, pw):
    flat = tuple(v for pair in reversed(pw) for v in pair)
    return torch.nn.functional.pad(x, flat)

kernel_name = 'interleave_layers__spectral_fnet__dense_activation_relu__prompt'

def setup_inputs():

    _w = (lambda a: (lambda t: t.to(torch.bfloat16) if t.is_floating_point() else t)(torch.as_tensor(a)))
    def _A_setup():
        B, S, D = 1, 2048, 576
        ns = torch.arange(S)[:, None] * torch.arange(S)[None, :]
        nd = torch.arange(D)[:, None] * torch.arange(D)[None, :]
        Ws_re = torch.cos(-2.0 * torch.pi * ns / S).to(float)
        Ws_im = torch.sin(-2.0 * torch.pi * ns / S).to(float)
        Wd_re = torch.cos(-2.0 * torch.pi * nd / D).to(float)
        Wd_im = torch.sin(-2.0 * torch.pi * nd / D).to(float)
        return {
            "x": _w((lambda *s: torch.randn(*s, dtype=torch.bfloat16) * 0.05)(B, S, D)),
            "dft_seq_re": _w(Ws_re),
            "dft_seq_im": _w(Ws_im),
            "dft_hid_re": _w(Wd_re),
            "dft_hid_im": _w(Wd_im),
        }
    _A_in = _A_setup()

    def _B_setup():
        B, S, D, Dff = 1, 2048, 576, 4608
        return {
            "x": _w((lambda *s: torch.randn(*s, dtype=torch.bfloat16) * 0.05)(B, S, D)),
            "w1": _w((lambda *s: torch.randn(*s, dtype=torch.bfloat16) * 1.6)(D, Dff)),
            "w2": _w((lambda *s: torch.randn(*s, dtype=torch.bfloat16) * 1.6)(Dff, D)),
        }
    _B_in = _B_setup()

    return {
        "A_x": _A_in["x"],
        "A_dft_seq_re": _A_in["dft_seq_re"],
        "A_dft_seq_im": _A_in["dft_seq_im"],
        "A_dft_hid_re": _A_in["dft_hid_re"],
        "A_dft_hid_im": _A_in["dft_hid_im"],
        "B_x": _B_in["x"],
        "B_w1": _B_in["w1"],
        "B_w2": _B_in["w2"],
    }

def reference(A_x, A_dft_seq_re, A_dft_seq_im, A_dft_hid_re, A_dft_hid_im, B_x, B_w1, B_w2):
    def _A_ref(x, dft_seq_re, dft_seq_im, dft_hid_re, dft_hid_im):
        a = torch.einsum("bsd,ed->bse", x, dft_hid_re)
        b = torch.einsum("bsd,ed->bse", x, dft_hid_im)
        y = torch.einsum("ts,bse->bte", dft_seq_re, a) - torch.einsum("ts,bse->bte", dft_seq_im, b)
        return y

    def _B_ref(x, w1, w2):
        h = torch.einsum("bsd,df->bsf", x, w1)
        zero = torch.zeros_like(h)
        h = torch.maximum(h, zero)
        return torch.einsum("bsf,fd->bsd", h, w2)

    x = A_x
    x = x + _A_ref(_layernorm(x, axis=-1), A_dft_seq_re, A_dft_seq_im, A_dft_hid_re, A_dft_hid_im)
    x = x + _B_ref(_layernorm(x, axis=-1), B_w1, B_w2)
    x = x + _A_ref(_layernorm(x, axis=-1), A_dft_seq_re, A_dft_seq_im, A_dft_hid_re, A_dft_hid_im)
    x = x + _B_ref(_layernorm(x, axis=-1), B_w1, B_w2)
    return x
```

```cpp
#include <hip/hip_runtime.h>
#include <hip/hip_bf16.h>
#include <stdint.h>
typedef unsigned short u16;
using bf16x8 = __attribute__((ext_vector_type(8))) short;
using f32x4 = __attribute__((ext_vector_type(4))) float;
constexpr int S = 2048, D = 576, F = 4608;

__device__ __forceinline__ float bf2f(u16 v) { return __uint_as_float(((unsigned)v) << 16); }
__device__ __forceinline__ u16 f2bf(float f) { __hip_bfloat16 h = __float2bfloat16(f); return *(u16*)&h; }

__global__ void ln_kernel(const u16* __restrict__ x, u16* __restrict__ y) {
  int row = blockIdx.x * 4 + (threadIdx.x >> 6), lane = threadIdx.x & 63;
  float v[9]; float s = 0;
  for (int i = 0; i < 9; ++i) { v[i] = bf2f(x[row * D + i * 64 + lane]); s += v[i]; }
  for (int o = 32; o; o >>= 1) s += __shfl_xor(s, o);
  float m = s / D, q = 0;
  for (int i = 0; i < 9; ++i) { float d = v[i] - m; q += d * d; }
  for (int o = 32; o; o >>= 1) q += __shfl_xor(q, o);
  float r = rsqrtf(q / D + 1e-5f);
  for (int i = 0; i < 9; ++i) y[row * D + i * 64 + lane] = f2bf((v[i] - m) * r);
}

template <bool BT, int MODE>
__global__ void gemm_kernel(const u16* __restrict__ A, int lda, const u16* __restrict__ B, int ldb, u16* C, int ldc,
                            const u16* res, int K, const u16* __restrict__ A2, const u16* __restrict__ B2) {
  __shared__ u16 As[64][40], Bs[64][40];
  int tid = threadIdx.x, w = tid >> 6, lane = tid & 63;
  int m0 = blockIdx.y * 64, n0 = blockIdx.x * 64;
  int wm = (w >> 1) * 32, wn = (w & 1) * 32;
  f32x4 acc[2][2] = {};
  int npass = (MODE == 3) ? 2 : 1;
  for (int pass = 0; pass < npass; ++pass) {
    const u16* Ap = pass ? A2 : A; const u16* Bp = pass ? B2 : B;
    if (pass) for (int i = 0; i < 2; ++i) for (int j = 0; j < 2; ++j) acc[i][j] = -acc[i][j];
    for (int k0 = 0; k0 < K; k0 += 32) {
      { int r = tid >> 2, c = (tid & 3) * 8;
        *(bf16x8*)&As[r][c] = *(const bf16x8*)&Ap[(size_t)(m0 + r) * lda + k0 + c];
        if (BT) *(bf16x8*)&Bs[r][c] = *(const bf16x8*)&Bp[(size_t)(n0 + r) * ldb + k0 + c];
        else { int kr = tid >> 3, nc = (tid & 7) * 8; bf16x8 v = *(const bf16x8*)&Bp[(size_t)(k0 + kr) * ldb + n0 + nc];
          for (int j = 0; j < 8; ++j) Bs[nc + j][kr] = v[j]; }
      }
      __syncthreads();
      bf16x8 a[2], b[2];
      for (int i = 0; i < 2; ++i) { a[i] = *(bf16x8*)&As[wm + i * 16 + (lane & 15)][(lane >> 4) * 8];
                                    b[i] = *(bf16x8*)&Bs[wn + i * 16 + (lane & 15)][(lane >> 4) * 8]; }
      for (int i = 0; i < 2; ++i) for (int j = 0; j < 2; ++j)
        acc[i][j] = __builtin_amdgcn_mfma_f32_16x16x32_bf16(a[i], b[j], acc[i][j], 0, 0, 0);
      __syncthreads();
    }
  }
  for (int i = 0; i < 2; ++i) for (int j = 0; j < 2; ++j) for (int r = 0; r < 4; ++r) {
    int row = m0 + wm + i * 16 + (lane >> 4) * 4 + r, col = n0 + wn + j * 16 + (lane & 15);
    float v = acc[i][j][r];
    if (MODE == 3) v = -v;
    if (MODE == 1) v = fmaxf(v, 0.f);
    if (MODE >= 2) v += bf2f(res[(size_t)row * ldc + col]);
    C[(size_t)row * ldc + col] = f2bf(v);
  }
}

extern "C" void kernel_launch(void* const* d_in, const int* in_sizes, int n_in, void* d_out, int out_size, void* d_ws,
                              size_t ws_size, hipStream_t stream) {
  const u16* x0 = (const u16*)d_in[0]; const u16* Cr = (const u16*)d_in[1]; const u16* Ci = (const u16*)d_in[2];
  const u16* Wr = (const u16*)d_in[3]; const u16* Wi = (const u16*)d_in[4];
  const u16* w1 = (const u16*)d_in[6]; const u16* w2 = (const u16*)d_in[7];
  u16* ws = (u16*)d_ws;
  u16* ln = ws; u16* a = ln + S * D; u16* b = a + S * D; u16* x1 = b + S * D; u16* x2 = x1 + S * D; u16* h = x2 + S * D;
  u16* out = (u16*)d_out;
  const u16* x = x0;
  for (int l = 0; l < 2; ++l) {
    ln_kernel<<<S / 4, 256, 0, stream>>>(x, ln);
    gemm_kernel<true, 0><<<dim3(D / 64, S / 64), 256, 0, stream>>>(ln, D, Wr, D, a, D, nullptr, D, nullptr, nullptr);
    gemm_kernel<true, 0><<<dim3(D / 64, S / 64), 256, 0, stream>>>(ln, D, Wi, D, b, D, nullptr, D, nullptr, nullptr);
    gemm_kernel<false, 3><<<dim3(D / 64, S / 64), 256, 0, stream>>>(Cr, S, a, D, x1, D, x, S, Ci, b);
    ln_kernel<<<S / 4, 256, 0, stream>>>(x1, ln);
    gemm_kernel<false, 1><<<dim3(F / 64, S / 64), 256, 0, stream>>>(ln, D, w1, F, h, F, nullptr, D, nullptr, nullptr);
    u16* xo = l == 0 ? x2 : out;
    gemm_kernel<false, 2><<<dim3(D / 64, S / 64), 256, 0, stream>>>(h, F, w2, D, xo, D, x1, F, nullptr, nullptr);
    x = x2;
  }
}
```

```cpp
#include <hip/hip_runtime.h>
#include <stdint.h>

namespace fk {
typedef unsigned short u16;

__global__ __launch_bounds__(256, 1) __attribute__((aligned(4096))) void k_main(char* ws, const u16* x0, const u16* Cr, const u16* Ci, const u16* Wr, const u16* Wi, u16* out, const u16* w1, const u16* w2) {
  unsigned tid = threadIdx.x;
  asm volatile(
"s_load_dwordx16 s[4:19], %2, 0x0\n"
"s_load_dwordx2 s[20:21], %2, 0x40\n"
"s_mov_b32 s29, %1\n"
"v_lshrrev_b32 v1, 6, v0\n"
"s_nop 0\n"
"v_readfirstlane_b32 s28, v1\n"
"v_and_b32 v0, 63, v0\n"
"v_lshlrev_b32 v1, 4, v0\n"
"s_mov_b32 s30, 0\n"
"v_lshlrev_b32 v21, 7, v0\n"
"s_getpc_b64 s[48:49]\n"
".Lpc%=:\n"
"s_mov_b32 s50, .Lend%=-.Lpc%=\n"
"s_mov_b32 s51, 0x20000\n"
"s_lshr_b32 s35, s29, 3\n"
"s_lshl2_add_u32 s35, s35, s28\n"
"s_lshl_b32 s35, s35, 13\n"
"s_cmp_lt_u32 s35, s50\n"
"s_cbranch_scc0 31f\n"
"buffer_load_dword v23, v21, s[48:51], s35 offen\n"
"31:\n"
"s_bfe_u32 s76, s29, 0x20001\n"
"s_lshr_b32 s33, s29, 5\n"
"s_lshl3_add_u32 s76, s76, s33\n"
"s_lshl_b32 s76, s76, 1\n"
"v_and_b32 v4, 15, v0\n"
"v_lshrrev_b32 v5, 4, v0\n"
"v_bfe_u32 v8, v0, 3, 2\n"
"v_lshrrev_b32 v9, 5, v0\n"
"v_lshl_add_u32 v8, v9, 3, v8\n"
"v_mul_u32_u24 v8, 1152, v8\n"
"v_and_b32 v20, 7, v0\n"
"v_xor_b32 v9, v20, v5\n"
"v_lshl_add_u32 v8, v9, 4, v8\n"
"v_xor_b32 v9, 64, v8\n"
"v_add_u32 v9, 18432, v9\n"
"s_lshr_b32 s33, s28, 1\n"
"s_add_u32 s33, s33, s76\n"
"s_lshl_b32 s33, s33, 3\n"
"s_and_b32 s34, s28, 1\n"
"s_add_u32 s33, s33, s34\n"
"s_mul_i32 s33, s33, 4608\n"
"s_mul_i32 s34, s28, 18432\n"
"s_mov_b32 m0, s34\n"
"s_mov_b32 s38, 2359296\n"
"s_mov_b32 s39, 0x20000\n"
"s_waitcnt lgkmcnt(0)\n"
"s_mov_b64 s[36:37], s[6:7]\n"
"s_add_u32 s34, s33, 384\n"
"32:\n"
"buffer_load_dwordx4 v8, s[36:39], s33 offen lds\n"
"s_add_u32 m0, m0, 1024\n"
"s_nop 0\n"
"buffer_load_dwordx4 v9, s[36:39], s33 offen lds\n"
"s_add_u32 m0, m0, 1024\n"
"s_add_u32 s33, s33, 128\n"
"s_cmp_lt_u32 s33, s34\n"
"s_cbranch_scc1 32b\n"
"s_mov_b64 s[24:25], s[4:5]\n"
"s_mov_b32 s26, 79740928\n"
"s_mov_b32 s27, 0x20000\n"
"buffer_load_dword v221, off, s[24:27], 0 offset:0 sc1\n"
"s_lshl2_add_u32 s35, s29, s28\n"
"s_cmp_lt_u32 s35, 512\n"
"s_cselect_b64 s[48:49], s[12:13], s[14:15]\n"
"s_and_b32 s35, s35, 511\n"
"s_mul_i32 s35, s35, 1296\n"
"s_add_u32 s50, s35, 1408\n"
"s_min_u32 s50, s50, 663552\n"
"buffer_load_dword v22, v21, s[48:51], s35 offen\n"
"s_add_u32 s34, s33, 768\n"
"33:\n"
"buffer_load_dwordx4 v8, s[36:39], s33 offen lds\n"
"s_add_u32 m0, m0, 1024\n"
"s_nop 0\n"
"buffer_load_dwordx4 v9, s[36:39], s33 offen lds\n"
"s_add_u32 m0, m0, 1024\n"
"s_add_u32 s33, s33, 128\n"
"s_cmp_lt_u32 s33, s34\n"
"s_cbranch_scc1 33b\n"
"s_bfe_u32 s76, s29, 0x20001\n"
"s_lshl_b32 s76, s76, 3\n"
"s_lshr_b32 s33, s29, 5\n"
"s_add_u32 s76, s76, s33\n"
"s_lshl_b32 s76, s76, 1\n"
"s_and_b32 s77, s29, 1\n"
"s_lshl_b32 s77, s77, 1\n"
"s_mov_b32 s33, 0\n"
"s_lshl_b32 s34, s28, 1\n"
"s_bfe_u32 s35, s29, 0x20003\n"
"s_mul_i32 s35, s35, 9\n"
"s_cmp_lt_u32 s28, 1\n"
"s_cselect_b32 s80, s33, s34\n"
"s_add_u32 s80, s80, s35\n"
"s_cmp_lt_u32 s77, 2\n"
"s_cselect_b64 s[68:69], s[12:13], s[14:15]\n"
"s_mov_b32 s70, 663552\n"
"s_mov_b32 s71, 0x20000\n"
"s_mul_i32 s78, s80, 18432\n"
"s_mul_i32 s79, s28, 16384\n"
"s_add_u32 s79, s79, 93184\n"
"v_lshrrev_b32 v2, 3, v0\n"
"v_mul_u32_u24 v2, 1152, v2\n"
"v_and_b32 v3, 7, v0\n"
"v_lshrrev_b32 v21, 4, v0\n"
"v_xor_b32 v3, v3, v21\n"
"v_xor_b32 v21, 4, v3\n"
"v_lshl_add_u32 v21, v21, 4, v2\n"
"v_lshl_add_u32 v2, v3, 4, v2\n"
"v_add_u32 v3, 9216, v21\n"
"s_bfe_u32 s33, s29, 0x20003\n"
"s_mul_i32 s33, s33, 9\n"
"s_add_u32 s33, s33, 8\n"
"s_mul_i32 s33, s33, 18432\n"
"s_lshr_b32 s34, s28, 1\n"
"s_lshl_b32 s34, s34, 7\n"
"s_add_u32 s83, s33, s34\n"
"s_bitcmp1_b32 s28, 0\n"
"s_cselect_b64 vcc, -1, 0\n"
"v_cndmask_b32 v21, v2, v3, vcc\n"
"s_cmp_lt_u32 s28, 2\n"
"s_cbranch_scc0 34f\n"
"s_lshl_b32 s81, s28, 10\n"
"s_add_u32 m0, s81, 91136\n"
"s_add_u32 s81, s83, 1024\n"
"buffer_load_dwordx4 v21, s[68:71], s81 offen lds\n"
"34:\n"
"s_lshl_b32 s81, s28, 10\n"
"s_add_u32 m0, s81, 74752\n"
"s_add_u32 s81, s83, 0\n"
"buffer_load_dwordx4 v21, s[68:71], s81 offen lds\n"
"s_lshl_b32 s81, s28, 10\n"
"s_add_u32 m0, s81, 78848\n"
"s_add_u32 s81, s83, 256\n"
"buffer_load_dwordx4 v21, s[68:71], s81 offen lds\n"
"s_lshl_b32 s81, s28, 10\n"
"s_add_u32 m0, s81, 82944\n"
"s_add_u32 s81, s83, 512\n"
"buffer_load_dwordx4 v21, s[68:71], s81 offen lds\n"
"s_lshl_b32 s81, s28, 10\n"
"s_add_u32 m0, s81, 87040\n"
"s_add_u32 s81, s83, 768\n"
"buffer_load_dwordx4 v21, s[68:71], s81 offen lds\n"
"s_lshl2_add_u32 s33, s29, s28\n"
"s_mov_b64 s[40:41], s[24:25]\n"
"s_mov_b32 s43, 0x20000\n"
"s_mov_b32 s51, 0x20000\n"
"s_cmp_lt_u32 s33, 864\n"
"s_cselect_b32 s42, 79740928, 0\n"
"s_cselect_b32 s50, 5308416, 0\n"
"s_sub_u32 s34, s33, 432\n"
"s_cmp_lt_u32 s33, 432\n"
"s_cselect_b32 s85, 1, 0\n"
"s_cselect_b64 s[48:49], s[18:19], s[20:21]\n"
"s_cselect_b32 s33, s33, s34\n"
"s_mov_b32 s34, 1152\n"
"s_cselect_b32 s54, 9216, s34\n"
"s_mul_i32 s52, s33, 683\n"
"s_lshr_b32 s52, s52, 11\n"
"s_mul_i32 s34, s52, 3\n"
"s_sub_u32 s53, s33, s34\n"
"s_mul_i32 s34, s29, 171\n"
"s_lshr_b32 s34, s34, 10\n"
"s_mul_i32 s33, s34, 6\n"
"s_sub_u32 s33, s29, s33\n"
"s_lshl2_add_u32 s34, s34, s28\n"
"s_cmp_eq_u32 s85, 1\n"
"s_cselect_b32 s52, s34, s52\n"
"s_cselect_b32 s53, s33, s53\n"
"v_lshrrev_b32 v56, 3, v0\n"
"v_mul_lo_u32 v56, s54, v56\n"
"v_lshl_add_u32 v56, v20, 4, v56\n"
"s_mul_i32 s33, s53, 3\n"
"s_add_u32 s33, s33, 0\n"
"s_lshr_b32 s34, s33, 2\n"
"s_mul_i32 s34, s34, 9\n"
"s_and_b32 s35, s33, 3\n"
"s_lshl_b32 s35, s35, 1\n"
"s_add_u32 s34, s34, s35\n"
"s_mul_i32 s35, s33, 18\n"
"s_sub_u32 s35, s35, 280\n"
"s_cmp_lt_u32 s33, 16\n"
"s_cselect_b32 s34, s34, s35\n"
"s_cselect_b32 s31, 1, 9\n"
"s_lshl_b32 s35, s52, 1\n"
"s_cmp_eq_u32 s85, 1\n"
"s_cselect_b32 s34, s34, s35\n"
"s_cselect_b32 s31, s31, 1\n"
"s_cselect_b32 s33, s52, s33\n"
"s_lshl_b32 s33, s33, 7\n"
"s_lshl_b32 s35, s54, 4\n"
"s_mul_i32 s34, s34, s35\n"
"s_add_u32 s73, s33, s34\n"
"s_mul_i32 s31, s31, s35\n"
"s_add_u32 s74, s73, s31\n"
"buffer_load_dwordx4 v[92:95], v56, s[48:51], s73 offen\n"
"s_add_u32 m0, s79, 0\n"
"s_add_u32 s81, s78, 0\n"
"buffer_load_dwordx4 v2, s[68:71], s81 offen lds\n"
"s_lshl3_add_u32 s73, s54, s73\n"
"buffer_load_dwordx4 v[96:99], v56, s[48:51], s73 offen\n"
"s_add_u32 m0, s79, 1024\n"
"s_add_u32 s81, s78, 0\n"
"buffer_load_dwordx4 v3, s[68:71], s81 offen lds\n"
"buffer_load_dwordx4 v[100:103], v56, s[48:51], s74 offen\n"
"s_add_u32 m0, s79, 2048\n"
"s_add_u32 s81, s78, 128\n"
"buffer_load_dwordx4 v2, s[68:71], s81 offen lds\n"
"v_lshrrev_b32 v7, 1, v4\n"
"v_xor_b32 v7, v7, v5\n"
"v_lshlrev_b32 v7, 4, v7\n"
"v_lshl_add_u32 v7, v4, 7, v7\n"
"v_mov_b32 v8, v7\n"
"v_xor_b32 v9, 64, v7\n"
"v_add_u32 v10, 55296, v8\n"
"v_add_u32 v11, 55296, v9\n"
"s_mul_i32 s33, s28, 18432\n"
"v_add_u32 v12, s33, v8\n"
"v_add_u32 v13, s33, v9\n"
"v_add_u32 v6, s79, v7\n"
"v_xor_b32 v7, 64, v6\n"
"v_add_u32 v14, 74752, v8\n"
"v_add_u32 v15, 74752, v9\n"
"s_waitcnt vmcnt(23)\n"
"v_readfirstlane_b32 s96, v221\n"
"s_nop 0\n"
"s_add_u32 s96, s96, 1\n"
"s_xor_b32 s97, s96, 0x9e3779b9\n"
"v_mov_b32 v16, 0x3f803f80\n"
"v_mov_b32 v17, 0x3f803f80\n"
"v_mov_b32 v18, 0x3f803f80\n"
"v_mov_b32 v19, 0x3f803f80\n"
"s_and_b32 s55, s29, 3\n"
"s_bfe_u32 s56, s29, 0x10002\n"
"s_lshl_b32 s56, s56, 3\n"
"s_lshr_b32 s33, s29, 5\n"
"s_add_u32 s56, s56, s33\n"
"s_bfe_u32 s57, s29, 0x20003\n"
"s_and_b32 s58, s28, 1\n"
"s_lshr_b32 s59, s28, 1\n"
"s_and_b32 s72, s56, 3\n"
"s_xor_b32 s72, s72, s55\n"
"s_cmp_lt_u32 s55, 2\n"
"s_cselect_b64 s[36:37], s[8:9], s[10:11]\n"
"s_mov_b32 s38, 8388608\n"
"s_mov_b32 s39, 0x20000\n"
"s_lshl_b32 s44, s56, 19\n"
"s_lshl_b32 s33, s72, 17\n"
"s_add_u32 s44, s44, s33\n"
"s_and_b32 s33, s55, 1\n"
"s_lshl_b32 s33, s33, 11\n"
"s_add_u32 s44, s44, s33\n"
"s_lshl2_add_u32 s33, s57, s28\n"
"s_lshl_b32 s33, s33, 13\n"
"s_add_u32 s82, s44, s33\n"
"s_lshl_b32 s33, s28, 15\n"
"s_add_u32 s44, s44, s33\n"
"s_lshl_b32 s46, s28, 11\n"
"s_and_b32 s33, s29, 7\n"
"s_lshr_b32 s34, s29, 3\n"
"s_lshr_b32 s60, s33, 1\n"
"s_lshl_b32 s60, s60, 1\n"
"s_and_b32 s35, s34, 1\n"
"s_add_u32 s60, s60, s35\n"
"s_and_b32 s61, s33, 1\n"
"s_lshl_b32 s61, s61, 4\n"
"s_lshr_b32 s35, s34, 1\n"
"s_add_u32 s61, s61, s35\n"
"v_xor_b32 v58, v4, v5\n"
"v_lshlrev_b32 v58, 4, v58\n"
"v_lshl_add_u32 v58, v5, 12, v58\n"
"s_lshl_b32 s33, s58, 7\n"
"v_xor_b32 v58, s33, v58\n"
"v_xor_b32 v59, 64, v58\n"
"s_and_b32 s33, s77, 2\n"
"s_lshr_b32 s35, s76, 5\n"
"s_add_u32 s33, s33, s35\n"
"s_lshl_b32 s33, s33, 7\n"
"s_bfe_u32 s35, s76, 0x30002\n"
"s_add_u32 s33, s33, s35\n"
"s_mul_i32 s66, s33, 9216\n"
"s_add_u32 s66, s66, 11321344\n"
"s_lshl2_add_u32 s35, s57, s28\n"
"s_mul_i32 s35, s35, 73728\n"
"s_add_u32 s35, s35, s66\n"
"s_and_b32 s33, s76, 3\n"
"s_add_u32 s33, s33, 0\n"
"s_xor_b32 s33, s33, s28\n"
"s_lshl_b32 s33, s33, 1\n"
"s_addc_u32 s33, s33, 0\n"
"s_lshl_b32 s33, s33, 10\n"
"s_add_u32 s62, s33, s35\n"
"s_and_b32 s33, s76, 3\n"
"s_add_u32 s33, s33, 1\n"
"s_xor_b32 s33, s33, s28\n"
"s_lshl_b32 s33, s33, 1\n"
"s_addc_u32 s33, s33, 0\n"
"s_lshl_b32 s33, s33, 10\n"
"s_add_u32 s63, s33, s35\n"
"v_lshlrev_b32 v57, 7, v4\n"
"v_lshl_add_u32 v57, v5, 12, v57\n"
"s_mul_i32 s33, s52, 57\n"
"s_lshr_b32 s33, s33, 9\n"
"s_lshr_b32 s34, s33, 1\n"
"s_cmp_eq_u32 s85, 1\n"
"s_cselect_b32 s75, s33, s34\n"
"s_cselect_b32 s34, 9, 18\n"
"s_mul_i32 s34, s34, s75\n"
"s_sub_u32 s93, s52, s34\n"
"s_cmp_lt_u32 s28, 2\n"
"s_cbranch_scc1 35f\n"
"s_mov_b64 exec, 0\n"
"s_cmp_eq_u32 s28, 2\n"
"s_cbranch_scc1 .Lq0_%=\n"
"s_branch .Lq1_%=\n"
".Lqret%=:\n"
"s_mov_b64 exec, -1\n"
"35:\n"
"s_waitcnt vmcnt(6)\n"
"s_barrier\n"
"ds_read_b128 a[0:3], v12 offset:0\n"
"ds_read_b128 a[4:7], v14 offset:0\n"
"ds_read_b128 a[8:11], v13 offset:0\n"
"ds_read_b128 a[12:15], v15 offset:0\n"
"ds_read_b128 a[16:19], v12 offset:2048\n"
"ds_read_b128 a[20:23], v14 offset:2048\n"
"s_waitcnt lgkmcnt(5)\n"
"v_mfma_f32_16x16x32_bf16 v[196:199], v[16:19], a[0:3], 0\n"
"ds_read_b128 a[24:27], v13 offset:2048\n"
"ds_read_b128 a[28:31], v15 offset:2048\n"
"v_mfma_f32_16x16x32_bf16 v[200:203], a[0:3], a[0:3], 0\n"
"s_waitcnt lgkmcnt(6)\n"
"v_mfma_f32_16x16x32_bf16 v[212:215], a[0:3], a[4:7], 0\n"
"s_lshl3_add_u32 s74, s54, s74\n"
"buffer_load_dwordx4 v[104:107], v56, s[48:51], s74 offen\n"
"v_mfma_f32_16x16x32_bf16 v[216:219], v[16:19], a[4:7], 0\n"
"s_waitcnt lgkmcnt(5)\n"
"v_mfma_f32_16x16x32_bf16 v[196:199], v[16:19], a[8:11], v[196:199]\n"
"ds_read_b128 a[32:35], v12 offset:4096\n"
"ds_read_b128 a[36:39], v14 offset:4096\n"
"v_mfma_f32_16x16x32_bf16 v[200:203], a[8:11], a[8:11], v[200:203]\n"
"s_waitcnt lgkmcnt(6)\n"
"v_mfma_f32_16x16x32_bf16 v[212:215], a[8:11], a[12:15], v[212:215]\n"
"s_add_u32 m0, s79, 3072\n"
"s_add_u32 s81, s78, 128\n"
"buffer_load_dwordx4 v3, s[68:71], s81 offen lds\n"
"v_mfma_f32_16x16x32_bf16 v[216:219], v[16:19], a[12:15], v[216:219]\n"
"s_waitcnt lgkmcnt(5)\n"
"v_mfma_f32_16x16x32_bf16 v[196:199], v[16:19], a[16:19], v[196:199]\n"
"ds_read_b128 a[40:43], v13 offset:4096\n"
"ds_read_b128 a[44:47], v15 offset:4096\n"
"v_mfma_f32_16x16x32_bf16 v[200:203], a[16:19], a[16:19], v[200:203]\n"
"s_waitcnt lgkmcnt(6)\n"
"v_mfma_f32_16x16x32_bf16 v[212:215], a[16:19], a[20:23], v[212:215]\n"
"s_mul_i32 s33, s53, 3\n"
"s_add_u32 s33, s33, 1\n"
"s_lshr_b32 s34, s33, 2\n"
"s_mul_i32 s34, s34, 9\n"
"s_and_b32 s35, s33, 3\n"
"s_lshl_b32 s35, s35, 1\n"
"s_add_u32 s34, s34, s35\n"
"s_mul_i32 s35, s33, 18\n"
"s_sub_u32 s35, s35, 280\n"
"s_cmp_lt_u32 s33, 16\n"
"s_cselect_b32 s34, s34, s35\n"
"s_cselect_b32 s31, 1, 9\n"
"s_lshl_b32 s35, s52, 1\n"
"s_cmp_eq_u32 s85, 1\n"
"s_cselect_b32 s34, s34, s35\n"
"s_cselect_b32 s31, s31, 1\n"
"s_cselect_b32 s33, s52, s33\n"
"s_lshl_b32 s33, s33, 7\n"
"s_lshl_b32 s35, s54, 4\n"
"s_mul_i32 s34, s34, s35\n"
"s_add_u32 s73, s33, s34\n"
"s_mul_i32 s31, s31, s35\n"
"s_add_u32 s74, s73, s31\n"
"buffer_load_dwordx4 v[108:111], v56, s[48:51], s73 offen\n"
"v_mfma_f32_16x16x32_bf16 v[216:219], v[16:19], a[20:23], v[216:219]\n"
"s_waitcnt lgkmcnt(5)\n"
"v_mfma_f32_16x16x32_bf16 v[196:199], v[16:19], a[24:27], v[196:199]\n"
"ds_read_b128 a[48:51], v12 offset:6144\n"
"ds_read_b128 a[52:55], v14 offset:6144\n"
"v_mfma_f32_16x16x32_bf16 v[200:203], a[24:27], a[24:27], v[200:203]\n"
"s_waitcnt lgkmcnt(6)\n"
"v_mfma_f32_16x16x32_bf16 v[212:215], a[24:27], a[28:31], v[212:215]\n"
"s_add_u32 m0, s79, 4096\n"
"s_add_u32 s81, s78, 256\n"
"buffer_load_dwordx4 v2, s[68:71], s81 offen lds\n"
"v_mfma_f32_16x16x32_bf16 v[216:219], v[16:19], a[28:31], v[216:219]\n"
"s_waitcnt lgkmcnt(5)\n"
"v_mfma_f32_16x16x32_bf16 v[196:199], v[16:19], a[32:35], v[196:199]\n"
"ds_read_b128 a[56:59], v13 offset:6144\n"
"ds_read_b128 a[60:63], v15 offset:6144\n"
"v_mfma_f32_16x16x32_bf16 v[200:203], a[32:35], a[32:35], v[200:203]\n"
"s_waitcnt lgkmcnt(6)\n"
"v_mfma_f32_16x16x32_bf16 v[212:215], a[32:35], a[36:39], v[212:215]\n"
"s_lshl3_add_u32 s73, s54, s73\n"
"buffer_load_dwordx4 v[112:115], v56, s[48:51], s73 offen\n"
"v_mfma_f32_16x16x32_bf16 v[216:219], v[16:19], a[36:39], v[216:219]\n"
"s_waitcnt lgkmcnt(5)\n"
"v_mfma_f32_16x16x32_bf16 v[196:199], v[16:19], a[40:43], v[196:199]\n"
"ds_read_b128 a[64:67], v12 offset:8192\n"
"ds_read_b128 a[68:71], v14 offset:8192\n"
"v_mfma_f32_16x16x32_bf16 v[200:203], a[40:43], a[40:43], v[200:203]\n"
"s_waitcnt lgkmcnt(6)\n"
"v_mfma_f32_16x16x32_bf16 v[212:215], a[40:43], a[44:47], v[212:215]\n"
"s_add_u32 m0, s79, 5120\n"
"s_add_u32 s81, s78, 256\n"
"buffer_load_dwordx4 v3, s[68:71], s81 offen lds\n"
"v_mfma_f32_16x16x32_bf16 v[216:219], v[16:19], a[44:47], v[216:219]\n"
"s_waitcnt lgkmcnt(5)\n"
"v_mfma_f32_16x16x32_bf16 v[196:199], v[16:19], a[48:51], v[196:199]\n"
"ds_read_b128 a[72:75], v13 offset:8192\n"
"ds_read_b128 a[76:79], v15 offset:8192\n"
"v_mfma_f32_16x16x32_bf16 v[200:203], a[48:51], a[48:51], v[200:203]\n"
"s_waitcnt lgkmcnt(6)\n"
"v_mfma_f32_16x16x32_bf16 v[212:215], a[48:51], a[52:55], v[212:215]\n"
"buffer_load_dwordx4 v[116:119], v56, s[48:51], s74 offen\n"
"v_mfma_f32_16x16x32_bf16 v[216:219], v[16:19], a[52:55], v[216:219]\n"
"s_waitcnt lgkmcnt(5)\n"
"v_mfma_f32_16x16x32_bf16 v[196:199], v[16:19], a[56:59], v[196:199]\n"
"ds_read_b128 a[80:83], v12 offset:10240\n"
"ds_read_b128 a[84:87], v14 offset:10240\n"
"v_mfma_f32_16x16x32_bf16 v[200:203], a[56:59], a[56:59], v[200:203]\n"
"s_waitcnt lgkmcnt(6)\n"
"v_mfma_f32_16x16x32_bf16 v[212:215], a[56:59], a[60:63], v[212:215]\n"
"s_add_u32 m0, s79, 6144\n"
"s_add_u32 s81, s78, 384\n"
"buffer_load_dwordx4 v2, s[68:71], s81 offen lds\n"
"v_mfma_f32_16x16x32_bf16 v[216:219], v[16:19], a[60:63], v[216:219]\n"
"s_waitcnt lgkmcnt(5)\n"
"v_mfma_f32_16x16x32_bf16 v[196:199], v[16:19], a[64:67], v[196:199]\n"
"ds_read_b128 a[88:91], v13 offset:10240\n"
"ds_read_b128 a[92:95], v15 offset:10240\n"
"v_mfma_f32_16x16x32_bf16 v[200:203], a[64:67], a[64:67], v[200:203]\n"
"s_waitcnt lgkmcnt(6)\n"
"v_mfma_f32_16x16x32_bf16 v[212:215], a[64:67], a[68:71], v[212:215]\n"
"s_lshl3_add_u32 s74, s54, s74\n"
"buffer_load_dwordx4 v[120:123], v56, s[48:51], s74 offen\n"
"v_mfma_f32_16x16x32_bf16 v[216:219], v[16:19], a[68:71], v[216:219]\n"
"s_waitcnt lgkmcnt(5)\n"
"v_mfma_f32_16x16x32_bf16 v[196:199], v[16:19], a[72:75], v[196:199]\n"
"ds_read_b128 a[96:99], v12 offset:12288\n"
"ds_read_b128 a[100:103], v14 offset:12288\n"
"v_mfma_f32_16x16x32_bf16 v[200:203], a[72:75], a[72:75], v[200:203]\n"
"s_waitcnt lgkmcnt(6)\n"
"v_mfma_f32_16x16x32_bf16 v[212:215], a[72:75], a[76:79], v[212:215]\n"
"s_add_u32 m0, s79, 7168\n"
"s_add_u32 s81, s78, 384\n"
"buffer_load_dwordx4 v3, s[68:71], s81 offen lds\n"
"v_mfma_f32_16x16x32_bf16 v[216:219], v[16:19], a[76:79], v[216:219]\n"
"s_waitcnt lgkmcnt(5)\n"
"v_mfma_f32_16x16x32_bf16 v[196:199], v[16:19], a[80:83], v[196:199]\n"
"ds_read_b128 a[104:107], v13 offset:12288\n"
"ds_read_b128 a[108:111], v15 offset:12288\n"
"v_mfma_f32_16x16x32_bf16 v[200:203], a[80:83], a[80:83], v[200:203]\n"
"s_waitcnt lgkmcnt(6)\n"
"v_mfma_f32_16x16x32_bf16 v[212:215], a[80:83], a[84:87], v[212:215]\n"
"s_mul_i32 s33, s53, 3\n"
"s_add_u32 s33, s33, 2\n"
"s_lshr_b32 s34, s33, 2\n"
"s_mul_i32 s34, s34, 9\n"
"s_and_b32 s35, s33, 3\n"
"s_lshl_b32 s35, s35, 1\n"
"s_add_u32 s34, s34, s35\n"
"s_mul_i32 s35, s33, 18\n"
"s_sub_u32 s35, s35, 280\n"
"s_cmp_lt_u32 s33, 16\n"
"s_cselect_b32 s34, s34, s35\n"
"s_cselect_b32 s31, 1, 9\n"
"s_lshl_b32 s35, s52, 1\n"
"s_cmp_eq_u32 s85, 1\n"
"s_cselect_b32 s34, s34, s35\n"
"s_cselect_b32 s31, s31, 1\n"
"s_cselect_b32 s33, s52, s33\n"
"s_lshl_b32 s33, s33, 7\n"
"s_lshl_b32 s35, s54, 4\n"
"s_mul_i32 s34, s34, s35\n"
"s_add_u32 s73, s33, s34\n"
"s_mul_i32 s31, s31, s35\n"
"s_add_u32 s74, s73, s31\n"
"buffer_load_dwordx4 v[124:127], v56, s[48:51], s73 offen\n"
"v_mfma_f32_16x16x32_bf16 v[216:219], v[16:19], a[84:87], v[216:219]\n"
"s_waitcnt lgkmcnt(5)\n"
"v_mfma_f32_16x16x32_bf16 v[196:199], v[16:19], a[88:91], v[196:199]\n"
"ds_read_b128 a[112:115], v12 offset:14336\n"
"ds_read_b128 a[116:119], v14 offset:14336\n"
"v_mfma_f32_16x16x32_bf16 v[200:203], a[88:91], a[88:91], v[200:203]\n"
"s_waitcnt lgkmcnt(6)\n"
"v_mfma_f32_16x16x32_bf16 v[212:215], a[88:91], a[92:95], v[212:215]\n"
"s_add_u32 m0, s79, 8192\n"
"s_add_u32 s81, s78, 512\n"
"buffer_load_dwordx4 v2, s[68:71], s81 offen lds\n"
"v_mfma_f32_16x16x32_bf16 v[216:219], v[16:19], a[92:95], v[216:219]\n"
"s_waitcnt lgkmcnt(5)\n"
"v_mfma_f32_16x16x32_bf16 v[196:199], v[16:19], a[96:99], v[196:199]\n"
"ds_read_b128 a[120:123], v13 offset:14336\n"
"ds_read_b128 a[124:127], v15 offset:14336\n"
"v_mfma_f32_16x16x32_bf16 v[200:203], a[96:99], a[96:99], v[200:203]\n"
"s_waitcnt lgkmcnt(6)\n"
"v_mfma_f32_16x16x32_bf16 v[212:215], a[96:99], a[100:103], v[212:215]\n"
"s_lshl3_add_u32 s73, s54, s73\n"
"buffer_load_dwordx4 v[128:131], v56, s[48:51], s73 offen\n"
"v_mfma_f32_16x16x32_bf16 v[216:219], v[16:19], a[100:103], v[216:219]\n"
"s_waitcnt lgkmcnt(5)\n"
"v_mfma_f32_16x16x32_bf16 v[196:199], v[16:19], a[104:107], v[196:199]\n"
"ds_read_b128 a[128:131], v12 offset:16384\n"
"ds_read_b128 a[132:135], v14 offset:16384\n"
"v_mfma_f32_16x16x32_bf16 v[200:203], a[104:107], a[104:107], v[200:203]\n"
"s_waitcnt lgkmcnt(6)\n"
"v_mfma_f32_16x16x32_bf16 v[212:215], a[104:107], a[108:111], v[212:215]\n"
"s_add_u32 m0, s79, 9216\n"
"s_add_u32 s81, s78, 512\n"
"buffer_load_dwordx4 v3, s[68:71], s81 offen lds\n"
"v_mfma_f32_16x16x32_bf16 v[216:219], v[16:19], a[108:111], v[216:219]\n"
"s_waitcnt lgkmcnt(5)\n"
"v_mfma_f32_16x16x32_bf16 v[196:199], v[16:19], a[112:115], v[196:199]\n"
"ds_read_b128 a[136:139], v13 offset:16384\n"
"ds_read_b128 a[140:143], v15 offset:16384\n"
"v_mfma_f32_16x16x32_bf16 v[200:203], a[112:115], a[112:115], v[200:203]\n"
"s_waitcnt lgkmcnt(6)\n"
"v_mfma_f32_16x16x32_bf16 v[212:215], a[112:115], a[116:119], v[212:215]\n"
"buffer_load_dwordx4 v[132:135], v56, s[48:51], s74 offen\n"
"v_mfma_f32_16x16x32_bf16 v[216:219], v[16:19], a[116:119], v[216:219]\n"
"s_waitcnt lgkmcnt(5)\n"
"v_mfma_f32_16x16x32_bf16 v[196:199], v[16:19], a[120:123], v[196:199]\n"
"v_mfma_f32_16x16x32_bf16 v[200:203], a[120:123], a[120:123], v[200:203]\n"
"s_waitcnt lgkmcnt(4)\n"
"v_mfma_f32_16x16x32_bf16 v[212:215], a[120:123], a[124:127], v[212:215]\n"
"s_add_u32 m0, s79, 10240\n"
"s_add_u32 s81, s78, 640\n"
"buffer_load_dwordx4 v2, s[68:71], s81 offen lds\n"
"v_mfma_f32_16x16x32_bf16 v[216:219], v[16:19], a[124:127], v[216:219]\n"
"s_waitcnt lgkmcnt(3)\n"
"v_mfma_f32_16x16x32_bf16 v[196:199], v[16:19], a[128:131], v[196:199]\n"
"v_mfma_f32_16x16x32_bf16 v[200:203], a[128:131], a[128:131], v[200:203]\n"
"s_waitcnt lgkmcnt(2)\n"
"v_mfma_f32_16x16x32_bf16 v[212:215], a[128:131], a[132:135], v[212:215]\n"
"s_lshl3_add_u32 s74, s54, s74\n"
"buffer_load_dwordx4 v[136:139], v56, s[48:51], s74 offen\n"
"v_mfma_f32_16x16x32_bf16 v[216:219], v[16:19], a[132:135], v[216:219]\n"
"s_waitcnt lgkmcnt(1)\n"
"v_mfma_f32_16x16x32_bf16 v[196:199], v[16:19], a[136:139], v[196:199]\n"
"v_mfma_f32_16x16x32_bf16 v[200:203], a[136:139], a[136:139], v[200:203]\n"
"s_waitcnt lgkmcnt(0)\n"
"v_mfma_f32_16x16x32_bf16 v[212:215], a[136:139], a[140:143], v[212:215]\n"
"s_add_u32 m0, s79, 11264\n"
"s_add_u32 s81, s78, 640\n"
"buffer_load_dwordx4 v3, s[68:71], s81 offen lds\n"
"v_mfma_f32_16x16x32_bf16 v[216:219], v[16:19], a[140:143], v[216:219]\n"
"s_add_u32 m0, s79, 12288\n"
"s_add_u32 s81, s78, 768\n"
"buffer_load_dwordx4 v2, s[68:71], s81 offen lds\n"
"s_add_u32 m0, s79, 13312\n"
"s_add_u32 s81, s78, 768\n"
"buffer_load_dwordx4 v3, s[68:71], s81 offen lds\n"
"s_add_u32 m0, s79, 14336\n"
"s_add_u32 s81, s78, 896\n"
"buffer_load_dwordx4 v2, s[68:71], s81 offen lds\n"
"s_add_u32 m0, s79, 15360\n"
"s_add_u32 s81, s78, 896\n"
"buffer_load_dwordx4 v3, s[68:71], s81 offen lds\n"
"v_mov_b32 v20, v196\n"
"v_mov_b32 v21, v200\n"
"s_mov_b32 exec_lo, 0x200002\n"
"s_mov_b32 exec_hi, 0x20000200\n"
"v_mov_b32 v21, v201\n"
"s_mov_b32 exec_lo, 0x400004\n"
"s_mov_b32 exec_hi, 0x40000400\n"
"v_mov_b32 v21, v202\n"
"s_mov_b32 exec_lo, 0x800008\n"
"s_mov_b32 exec_hi, 0x80000800\n"
"v_mov_b32 v21, v203\n"
"s_mov_b64 exec, -1\n"
"v_mul_f32 v20, 0.001736111111111111, v20\n"
"v_mul_f32 v22, v20, v20\n"
"v_mul_f32 v21, 0.001736111111111111, v21\n"
"v_sub_f32 v21, v21, v22\n"
"v_max_f32 v21, 0, v21\n"
"v_add_f32 v21, 0x3727c5ac, v21\n"
"v_rsq_f32 v21, v21\n"
"s_nop 0\n"
"v_mul_f32 v20, -v20, v21\n"
"s_lshl_b32 s33, s77, 30\n"
"v_xor_b32 v20, s33, v20\n"
"v_xor_b32 v21, s33, v21\n"
"s_lshl_b32 s33, s28, 7\n"
"s_add_u32 s33, s33, 73728\n"
"v_lshl_add_u32 v22, v4, 3, s33\n"
"s_mov_b32 exec_lo, 0xf0000f\n"
"s_mov_b32 exec_hi, 0xf0000f00\n"
"ds_write_b64 v22, v[20:21]\n"
"s_mov_b64 exec, -1\n"
"v_lshl_add_u32 v23, v5, 5, s33\n"
"v_lshlrev_b32 v22, 5, v5\n"
"v_add_u32 v22, 73728, v22\n"
"v_lshlrev_b32 v20, 2, v0\n"
"s_mul_i32 s33, s28, 192\n"
"s_add_u32 s33, s33, 158720\n"
"v_lshl_add_u32 v14, v4, 2, s33\n"
"s_lshl_b32 s33, s28, 12\n"
"s_add_u32 s33, s33, 74752\n"
"v_add_u32 v12, s33, v1\n"
"v_lshrrev_b32 v15, 2, v0\n"
"v_lshlrev_b32 v15, 7, v15\n"
"v_and_b32 v13, 3, v0\n"
"v_lshl_add_u32 v15, v13, 3, v15\n"
"v_add_u32 v15, s33, v15\n"
"s_waitcnt lgkmcnt(0)\n"
"s_barrier\n"
"ds_read_b128 a[0:3], v8 offset:0\n"
"ds_read_b128 a[4:7], v8 offset:18432\n"
"ds_read_b128 a[8:11], v8 offset:36864\n"
"ds_read_b128 a[12:15], v10 offset:0\n"
"ds_read_b128 a[16:19], v9 offset:0\n"
"ds_read_b128 a[20:23], v9 offset:18432\n"
"ds_read_b128 a[24:27], v9 offset:36864\n"
"ds_read_b128 a[28:31], v11 offset:0\n"
"s_waitcnt vmcnt(24)\n"
"ds_read_b128 v[60:63], v6 offset:0\n"
"ds_read_b128 v[64:67], v7 offset:0\n"
"ds_read_b128 a[32:35], v8 offset:2048\n"
"ds_read_b128 a[36:39], v8 offset:20480\n"
"ds_read_b128 a[40:43], v8 offset:38912\n"
"ds_read_b128 a[44:47], v10 offset:2048\n"
"ds_read_b128 a[48:51], v9 offset:2048\n"
"s_waitcnt lgkmcnt(14)\n"
"ds_read_b128 a[52:55], v9 offset:20480\n"
"s_waitcnt lgkmcnt(14)\n"
"ds_read_b128 a[56:59], v9 offset:38912\n"
"s_waitcnt lgkmcnt(14)\n"
"ds_read_b128 a[60:63], v11 offset:2048\n"
"s_waitcnt vmcnt(20)\n"
"s_waitcnt lgkmcnt(14)\n"
"ds_read_b128 v[68:71], v6 offset:2048\n"
"s_waitcnt lgkmcnt(14)\n"
"ds_read_b128 v[72:75], v7 offset:2048\n"
"s_waitcnt lgkmcnt(11)\n"
"v_mfma_f32_16x16x32_bf16 v[156:159], a[0:3], v[60:63], 0\n"
"s_waitcnt lgkmcnt(10)\n"
"s_add_u32 m0, s79, 0\n"
"s_add_u32 s81, s78, 1024\n"
"buffer_load_dwordx4 v2, s[68:71], s81 offen lds\n"
"v_mfma_f32_16x16x32_bf16 v[156:159], a[16:19], v[64:67], v[156:159]\n"
"s_add_u32 m0, s79, 1024\n"
"s_add_u32 s81, s78, 1024\n"
"buffer_load_dwordx4 v3, s[68:71], s81 offen lds\n"
"v_mfma_f32_16x16x32_bf16 v[160:163], a[20:23], v[64:67], 0\n"
"s_waitcnt vmcnt(18)\n"
"ds_read_b128 v[76:79], v6 offset:4096\n"
"ds_read_b128 v[80:83], v7 offset:4096\n"
"v_mfma_f32_16x16x32_bf16 v[160:163], a[4:7], v[60:63], v[160:163]\n"
"ds_read_b128 a[64:67], v8 offset:4096\n"
"ds_read_b128 a[68:71], v8 offset:22528\n"
"v_mfma_f32_16x16x32_bf16 v[164:167], a[8:11], v[60:63], 0\n"
"ds_read_b128 a[72:75], v8 offset:40960\n"
"s_waitcnt lgkmcnt(14)\n"
"ds_read_b128 a[76:79], v10 offset:4096\n"
"v_mfma_f32_16x16x32_bf16 v[164:167], a[24:27], v[64:67], v[164:167]\n"
"s_waitcnt lgkmcnt(14)\n"
"ds_read_b128 a[80:83], v9 offset:4096\n"
"s_waitcnt lgkmcnt(14)\n"
"ds_read_b128 a[84:87], v9 offset:22528\n"
"v_mfma_f32_16x16x32_bf16 v[168:171], a[28:31], v[64:67], 0\n"
"s_waitcnt lgkmcnt(14)\n"
"ds_read_b128 a[88:91], v9 offset:40960\n"
"s_waitcnt lgkmcnt(14)\n"
"ds_read_b128 a[92:95], v11 offset:4096\n"
"v_mfma_f32_16x16x32_bf16 v[168:171], a[12:15], v[60:63], v[168:171]\n"
"s_waitcnt lgkmcnt(14)\n"
"ds_write_b128 v12, v[92:95] offset:0\n"
"s_waitcnt lgkmcnt(14)\n"
"ds_write_b128 v12, v[96:99] offset:1024\n"
"s_waitcnt lgkmcnt(14)\n"
"ds_write_b128 v12, v[100:103] offset:2048\n"
"s_waitcnt lgkmcnt(14)\n"
"ds_write_b128 v12, v[104:107] offset:3072\n"
"s_nop 0\n"
"s_waitcnt lgkmcnt(14)\n"
"ds_read_b64_tr_b16 v[92:93], v15 offset:0\n"
"s_waitcnt lgkmcnt(14)\n"
"ds_read_b64_tr_b16 v[94:95], v15 offset:2048\n"
"s_waitcnt lgkmcnt(14)\n"
"ds_read_b64_tr_b16 v[96:97], v15 offset:32\n"
"s_waitcnt lgkmcnt(14)\n"
"ds_read_b64_tr_b16 v[98:99], v15 offset:2080\n"
"s_waitcnt lgkmcnt(14)\n"
"ds_read_b64_tr_b16 v[100:101], v15 offset:64\n"
"s_waitcnt lgkmcnt(14)\n"
"ds_read_b64_tr_b16 v[102:103], v15 offset:2112\n"
"s_waitcnt lgkmcnt(14)\n"
"ds_read_b64_tr_b16 v[104:105], v15 offset:96\n"
"s_waitcnt lgkmcnt(14)\n"
"ds_read_b64_tr_b16 v[106:107], v15 offset:2144\n"
"v_mfma_f32_16x16x32_bf16 v[172:175], v[16:19], v[60:63], 0\n"
".Lq0_%=:\n"
"s_cbranch_execz .Lqret%=\n"
"s_waitcnt lgkmcnt(14)\n"
"ds_read_b128 v[196:199], v23 offset:0\n"
"s_waitcnt lgkmcnt(14)\n"
"ds_read_b128 v[200:203], v23 offset:16\n"
"v_mfma_f32_16x16x32_bf16 v[172:175], v[16:19], v[64:67], v[172:175]\n"
"s_waitcnt lgkmcnt(14)\n"
"ds_read_b128 v[24:27], v22 offset:0\n"
"s_waitcnt lgkmcnt(14)\n"
"ds_read_b128 v[28:31], v22 offset:16\n"
"v_mfma_f32_16x16x32_bf16 v[172:175], v[16:19], v[68:71], v[172:175]\n"
"s_add_u32 m0, s79, 2048\n"
"s_add_u32 s81, s78, 18432\n"
"buffer_load_dwordx4 v2, s[68:71], s81 offen lds\n"
"v_mfma_f32_16x16x32_bf16 v[172:175], v[16:19], v[72:75], v[172:175]\n"
"s_add_u32 m0, s79, 3072\n"
"s_add_u32 s81, s78, 18432\n"
"buffer_load_dwordx4 v3, s[68:71], s81 offen lds\n"
"v_mfma_f32_16x16x32_bf16 v[168:171], a[60:63], v[72:75], v[168:171]\n"
"s_waitcnt vmcnt(16)\n"
"s_waitcnt lgkmcnt(14)\n"
"ds_read_b128 v[84:87], v6 offset:6144\n"
"s_waitcnt lgkmcnt(14)\n"
"ds_read_b128 v[88:91], v7 offset:6144\n"
"v_mfma_f32_16x16x32_bf16 v[168:171], a[44:47], v[68:71], v[168:171]\n"
"s_waitcnt lgkmcnt(14)\n"
"ds_read_b128 a[96:99], v8 offset:6144\n"
"s_waitcnt lgkmcnt(14)\n"
"ds_read_b128 a[100:103], v8 offset:24576\n"
"v_mfma_f32_16x16x32_bf16 v[164:167], a[40:43], v[68:71], v[164:167]\n"
"s_waitcnt lgkmcnt(14)\n"
"ds_read_b128 a[104:107], v8 offset:43008\n"
"s_waitcnt lgkmcnt(14)\n"
"ds_read_b128 a[108:111], v10 offset:6144\n"
"v_mfma_f32_16x16x32_bf16 v[164:167], a[56:59], v[72:75], v[164:167]\n"
"s_waitcnt lgkmcnt(14)\n"
"ds_read_b128 a[112:115], v9 offset:6144\n"
"s_waitcnt lgkmcnt(14)\n"
"ds_read_b128 a[116:119], v9 offset:24576\n"
"v_mfma_f32_16x16x32_bf16 v[160:163], a[52:55], v[72:75], v[160:163]\n"
"s_waitcnt lgkmcnt(14)\n"
"ds_read_b128 a[120:123], v9 offset:43008\n"
"s_waitcnt lgkmcnt(14)\n"
"ds_read_b128 a[124:127], v11 offset:6144\n"
"v_mfma_f32_16x16x32_bf16 v[160:163], a[36:39], v[68:71], v[160:163]\n"
"s_mul_i32 s33, s53, 3\n"
"s_add_u32 s33, s33, 0\n"
"s_add_u32 s34, s33, 15\n"
"s_sub_u32 s34, s34, s75\n"
"s_and_b32 s34, s34, 7\n"
"s_cmp_gt_u32 s34, 1\n"
"s_addc_u32 s34, s34, 9\n"
"s_cmp_eq_u32 s33, 8\n"
"s_cselect_b32 s34, 11, s34\n"
"s_cmp_eq_u32 s85, 1\n"
"s_cselect_b32 s34, s93, s34\n"
"s_cselect_b32 s33, s33, s93\n"
"s_mul_i32 s34, s34, 18\n"
"s_add_u32 s33, s33, s34\n"
"s_mul_i32 s34, s75, 1296\n"
"s_add_u32 s33, s33, s34\n"
"s_lshl_b32 s33, s33, 10\n"
"s_add_u32 s45, s33, 589824\n"
"buffer_store_dwordx4 v[92:95], v1, s[40:43], s45 offen sc1\n"
"v_mfma_f32_16x16x32_bf16 v[140:143], v[16:19], v[92:95], 0\n"
"s_waitcnt lgkmcnt(14)\n"
"ds_write_b128 v12, v[108:111] offset:0\n"
"s_waitcnt lgkmcnt(14)\n"
"ds_write_b128 v12, v[112:115] offset:1024\n"
"s_waitcnt lgkmcnt(14)\n"
"ds_write_b128 v12, v[116:119] offset:2048\n"
"s_waitcnt lgkmcnt(14)\n"
"ds_write_b128 v12, v[120:123] offset:3072\n"
"s_nop 0\n"
"s_waitcnt lgkmcnt(14)\n"
"ds_read_b64_tr_b16 v[108:109], v15 offset:0\n"
"s_waitcnt lgkmcnt(14)\n"
"ds_read_b64_tr_b16 v[110:111], v15 offset:2048\n"
"s_waitcnt lgkmcnt(14)\n"
"ds_read_b64_tr_b16 v[112:113], v15 offset:32\n"
"s_waitcnt lgkmcnt(14)\n"
"ds_read_b64_tr_b16 v[114:115], v15 offset:2080\n"
"s_waitcnt lgkmcnt(14)\n"
"ds_read_b64_tr_b16 v[116:117], v15 offset:64\n"
"s_waitcnt lgkmcnt(14)\n"
"ds_read_b64_tr_b16 v[118:119], v15 offset:2112\n"
"s_waitcnt lgkmcnt(14)\n"
"ds_read_b64_tr_b16 v[120:121], v15 offset:96\n"
"s_waitcnt lgkmcnt(14)\n"
"ds_read_b64_tr_b16 v[122:123], v15 offset:2144\n"
"v_mfma_f32_16x16x32_bf16 v[156:159], a[32:35], v[68:71], v[156:159]\n"
"s_waitcnt lgkmcnt(14)\n"
"ds_read_b128 v[32:35], v22 offset:128\n"
"s_waitcnt lgkmcnt(14)\n"
"ds_read_b128 v[36:39], v22 offset:144\n"
"v_mfma_f32_16x16x32_bf16 v[156:159], a[48:51], v[72:75], v[156:159]\n"
"s_waitcnt lgkmcnt(14)\n"
"ds_read_b128 v[40:43], v22 offset:256\n"
"s_waitcnt lgkmcnt(14)\n"
"ds_read_b128 v[44:47], v22 offset:272\n"
"v_mfma_f32_16x16x32_bf16 v[156:159], a[64:67], v[76:79], v[156:159]\n"
"s_add_u32 m0, s79, 4096\n"
"s_add_u32 s81, s78, 18560\n"
"buffer_load_dwordx4 v2, s[68:71], s81 offen lds\n"
"v_mfma_f32_16x16x32_bf16 v[156:159], a[80:83], v[80:83], v[156:159]\n"
"s_add_u32 m0, s79, 5120\n"
"s_add_u32 s81, s78, 18560\n"
"buffer_load_dwordx4 v3, s[68:71], s81 offen lds\n"
"v_mfma_f32_16x16x32_bf16 v[160:163], a[84:87], v[80:83], v[160:163]\n"
"s_waitcnt vmcnt(15)\n"
"s_waitcnt lgkmcnt(14)\n"
"ds_read_b128 v[60:63], v6 offset:8192\n"
"s_waitcnt lgkmcnt(14)\n"
"ds_read_b128 v[64:67], v7 offset:8192\n"
"v_mfma_f32_16x16x32_bf16 v[160:163], a[68:71], v[76:79], v[160:163]\n"
"s_waitcnt lgkmcnt(14)\n"
"ds_read_b128 a[128:131], v8 offset:8192\n"
"s_waitcnt lgkmcnt(14)\n"
"ds_read_b128 a[132:135], v8 offset:26624\n"
"v_mfma_f32_16x16x32_bf16 v[164:167], a[72:75], v[76:79], v[164:167]\n"
"s_waitcnt lgkmcnt(14)\n"
"ds_read_b128 a[136:139], v8 offset:45056\n"
"s_waitcnt lgkmcnt(14)\n"
"ds_read_b128 a[140:143], v10 offset:8192\n"
"v_mfma_f32_16x16x32_bf16 v[164:167], a[88:91], v[80:83], v[164:167]\n"
"s_waitcnt lgkmcnt(14)\n"
"ds_read_b128 a[144:147], v9 offset:8192\n"
"s_waitcnt lgkmcnt(14)\n"
"ds_read_b128 a[148:151], v9 offset:26624\n"
"v_mfma_f32_16x16x32_bf16 v[168:171], a[92:95], v[80:83], v[168:171]\n"
"s_waitcnt lgkmcnt(14)\n"
"ds_read_b128 a[152:155], v9 offset:45056\n"
"s_waitcnt lgkmcnt(14)\n"
"ds_read_b128 a[156:159], v11 offset:8192\n"
"v_mfma_f32_16x16x32_bf16 v[168:171], a[76:79], v[76:79], v[168:171]\n"
"s_add_u32 s45, s45, 331776\n"
"buffer_store_dwordx4 v[96:99], v1, s[40:43], s45 offen sc1\n"
"v_mfma_f32_16x16x32_bf16 v[144:147], v[16:19], v[96:99], 0\n"
"v_mfma_f32_16x16x32_bf16 v[172:175], v[16:19], v[76:79], v[172:175]\n"
"s_waitcnt lgkmcnt(14)\n"
"ds_read_b128 v[48:51], v22 offset:384\n"
"s_waitcnt lgkmcnt(14)\n"
"ds_read_b128 v[52:55], v22 offset:400\n"
"v_mfma_f32_16x16x32_bf16 v[172:175], v[16:19], v[80:83], v[172:175]\n"
"v_mul_f32 v196, v196, v216\n"
"v_mul_f32 v198, v198, v216\n"
"v_mfma_f32_16x16x32_bf16 v[172:175], v[16:19], v[84:87], v[172:175]\n"
"s_add_u32 m0, s79, 6144\n"
"s_add_u32 s81, s78, 18688\n"
"buffer_load_dwordx4 v2, s[68:71], s81 offen lds\n"
"v_mfma_f32_16x16x32_bf16 v[172:175], v[16:19], v[88:91], v[172:175]\n"
"s_add_u32 m0, s79, 7168\n"
"s_add_u32 s81, s78, 18688\n"
"buffer_load_dwordx4 v3, s[68:71], s81 offen lds\n"
"v_mfma_f32_16x16x32_bf16 v[168:171], a[124:127], v[88:91], v[168:171]\n"
"s_waitcnt vmcnt(14)\n"
"s_waitcnt lgkmcnt(14)\n"
"ds_read_b128 v[68:71], v6 offset:10240\n"
"s_waitcnt lgkmcnt(14)\n"
"ds_read_b128 v[72:75], v7 offset:10240\n"
"v_mfma_f32_16x16x32_bf16 v[168:171], a[108:111], v[84:87], v[168:171]\n"
"s_waitcnt lgkmcnt(14)\n"
"ds_read_b128 a[160:163], v8 offset:10240\n"
"s_waitcnt lgkmcnt(14)\n"
"ds_read_b128 a[164:167], v8 offset:28672\n"
"v_mfma_f32_16x16x32_bf16 v[164:167], a[104:107], v[84:87], v[164:167]\n"
"s_waitcnt lgkmcnt(14)\n"
"ds_read_b128 a[168:171], v8 offset:47104\n"
"s_waitcnt lgkmcnt(14)\n"
"ds_read_b128 a[172:175], v10 offset:10240\n"
"v_mfma_f32_16x16x32_bf16 v[164:167], a[120:123], v[88:91], v[164:167]\n"
"s_waitcnt lgkmcnt(14)\n"
"ds_read_b128 a[176:179], v9 offset:10240\n"
"s_waitcnt lgkmcnt(14)\n"
"ds_read_b128 a[180:183], v9 offset:28672\n"
"v_mfma_f32_16x16x32_bf16 v[160:163], a[116:119], v[88:91], v[160:163]\n"
"s_waitcnt lgkmcnt(14)\n"
"ds_read_b128 a[184:187], v9 offset:47104\n"
"s_waitcnt lgkmcnt(14)\n"
"ds_read_b128 a[188:191], v11 offset:10240\n"
"v_mfma_f32_16x16x32_bf16 v[160:163], a[100:103], v[84:87], v[160:163]\n"
"s_add_u32 s45, s45, 331776\n"
"buffer_store_dwordx4 v[100:103], v1, s[40:43], s45 offen sc1\n"
"v_mfma_f32_16x16x32_bf16 v[148:151], v[16:19], v[100:103], 0\n"
"v_mfma_f32_16x16x32_bf16 v[156:159], a[96:99], v[84:87], v[156:159]\n"
"v_mul_f32 v200, v200, v216\n"
"v_mul_f32 v202, v202, v216\n"
"v_mfma_f32_16x16x32_bf16 v[156:159], a[112:115], v[88:91], v[156:159]\n"
"v_fmac_f32 v196, v197, v212\n"
"v_fmac_f32 v198, v199, v213\n"
"v_mfma_f32_16x16x32_bf16 v[156:159], a[128:131], v[60:63], v[156:159]\n"
"s_add_u32 m0, s79, 8192\n"
"s_add_u32 s81, s78, 18816\n"
"buffer_load_dwordx4 v2, s[68:71], s81 offen lds\n"
"v_mfma_f32_16x16x32_bf16 v[156:159], a[144:147], v[64:67], v[156:159]\n"
"s_waitcnt lgkmcnt(14)\n"
"s_add_u32 m0, s79, 9216\n"
"s_add_u32 s81, s78, 18816\n"
"buffer_load_dwordx4 v3, s[68:71], s81 offen lds\n"
"v_mfma_f32_16x16x32_bf16 v[160:163], a[148:151], v[64:67], v[160:163]\n"
"s_waitcnt vmcnt(15)\n"
"ds_read_b128 v[76:79], v6 offset:12288\n"
"s_waitcnt lgkmcnt(14)\n"
"ds_read_b128 v[80:83], v7 offset:12288\n"
"v_mfma_f32_16x16x32_bf16 v[160:163], a[132:135], v[60:63], v[160:163]\n"
"s_waitcnt lgkmcnt(14)\n"
"ds_read_b128 a[192:195], v8 offset:12288\n"
"s_waitcnt lgkmcnt(14)\n"
"ds_read_b128 a[196:199], v8 offset:30720\n"
"v_mfma_f32_16x16x32_bf16 v[164:167], a[136:139], v[60:63], v[164:167]\n"
"s_waitcnt lgkmcnt(14)\n"
"ds_read_b128 a[200:203], v8 offset:49152\n"
"s_waitcnt lgkmcnt(14)\n"
"ds_read_b128 a[204:207], v10 offset:12288\n"
"v_mfma_f32_16x16x32_bf16 v[164:167], a[152:155], v[64:67], v[164:167]\n"
"s_waitcnt lgkmcnt(14)\n"
"ds_read_b128 a[208:211], v9 offset:12288\n"
"s_waitcnt lgkmcnt(14)\n"
"ds_read_b128 a[212:215], v9 offset:30720\n"
"v_mfma_f32_16x16x32_bf16 v[168:171], a[156:159], v[64:67], v[168:171]\n"
"s_waitcnt lgkmcnt(14)\n"
"ds_read_b128 a[216:219], v9 offset:49152\n"
"s_waitcnt lgkmcnt(14)\n"
"ds_read_b128 a[220:223], v11 offset:12288\n"
"v_mfma_f32_16x16x32_bf16 v[168:171], a[140:143], v[60:63], v[168:171]\n"
"s_add_u32 s45, s45, 331776\n"
"buffer_store_dwordx4 v[104:107], v1, s[40:43], s45 offen sc1\n"
"v_mfma_f32_16x16x32_bf16 v[152:155], v[16:19], v[104:107], 0\n"
"v_mfma_f32_16x16x32_bf16 v[172:175], v[16:19], v[60:63], v[172:175]\n"
"v_fmac_f32 v200, v201, v214\n"
"v_fmac_f32 v202, v203, v215\n"
"v_mfma_f32_16x16x32_bf16 v[172:175], v[16:19], v[64:67], v[172:175]\n"
"v_cvt_pk_bf16_f32 v220, v196, v198\n"
"v_cvt_pk_bf16_f32 v221, v200, v202\n"
"v_mfma_f32_16x16x32_bf16 v[172:175], v[16:19], v[68:71], v[172:175]\n"
"s_add_u32 m0, s79, 10240\n"
"s_add_u32 s81, s78, 18944\n"
"buffer_load_dwordx4 v2, s[68:71], s81 offen lds\n"
"v_mfma_f32_16x16x32_bf16 v[172:175], v[16:19], v[72:75], v[172:175]\n"
"s_waitcnt lgkmcnt(10)\n"
"s_add_u32 m0, s79, 11264\n"
"s_add_u32 s81, s78, 18944\n"
"buffer_load_dwordx4 v3, s[68:71], s81 offen lds\n"
"v_mfma_f32_16x16x32_bf16 v[168:171], a[188:191], v[72:75], v[168:171]\n"
"s_waitcnt vmcnt(16)\n"
"ds_read_b128 v[84:87], v6 offset:14336\n"
"ds_read_b128 v[88:91], v7 offset:14336\n"
"v_mfma_f32_16x16x32_bf16 v[168:171], a[172:175], v[68:71], v[168:171]\n"
"ds_read_b128 a[224:227], v8 offset:14336\n"
"ds_read_b128 a[228:231], v8 offset:32768\n"
"v_mfma_f32_16x16x32_bf16 v[164:167], a[168:171], v[68:71], v[164:167]\n"
"ds_read_b128 a[232:235], v8 offset:51200\n"
"s_waitcnt lgkmcnt(14)\n"
"ds_read_b128 a[236:239], v10 offset:14336\n"
"v_mfma_f32_16x16x32_bf16 v[164:167], a[184:187], v[72:75], v[164:167]\n"
"s_waitcnt lgkmcnt(14)\n"
"ds_read_b128 a[240:243], v9 offset:14336\n"
"s_waitcnt lgkmcnt(14)\n"
"ds_read_b128 a[244:247], v9 offset:32768\n"
"v_mfma_f32_16x16x32_bf16 v[160:163], a[180:183], v[72:75], v[160:163]\n"
"s_waitcnt lgkmcnt(14)\n"
"ds_read_b128 a[248:251], v9 offset:51200\n"
"s_waitcnt lgkmcnt(14)\n"
"ds_read_b128 a[252:255], v11 offset:14336\n"
"v_mfma_f32_16x16x32_bf16 v[160:163], a[164:167], v[68:71], v[160:163]\n"
"s_mul_i32 s33, s53, 3\n"
"s_add_u32 s33, s33, 1\n"
"s_add_u32 s34, s33, 15\n"
"s_sub_u32 s34, s34, s75\n"
"s_and_b32 s34, s34, 7\n"
"s_cmp_gt_u32 s34, 1\n"
"s_addc_u32 s34, s34, 9\n"
"s_cmp_eq_u32 s33, 8\n"
"s_cselect_b32 s34, 11, s34\n"
"s_cmp_eq_u32 s85, 1\n"
"s_cselect_b32 s34, s93, s34\n"
"s_cselect_b32 s33, s33, s93\n"
"s_mul_i32 s34, s34, 18\n"
"s_add_u32 s33, s33, s34\n"
"s_mul_i32 s34, s75, 1296\n"
"s_add_u32 s33, s33, s34\n"
"s_lshl_b32 s33, s33, 10\n"
"s_add_u32 s45, s33, 589824\n"
"buffer_store_dwordx4 v[108:111], v1, s[40:43], s45 offen sc1\n"
"v_mfma_f32_16x16x32_bf16 v[140:143], v[16:19], v[108:111], v[140:143]\n"
"v_mfma_f32_16x16x32_bf16 v[156:159], a[160:163], v[68:71], v[156:159]\n"
"s_add_u32 s33, s76, s59\n"
"s_and_b32 s33, s33, 3\n"
"s_lshl2_add_u32 s33, s57, s33\n"
"s_mul_i32 s33, s33, 73728\n"
"s_add_u32 s33, s33, s66\n"
"s_lshl3_add_u32 s33, s58, s33\n"
"buffer_store_dwordx2 v[220:221], v1, s[24:27], s33 offen offset:2048 sc1\n"
"v_mfma_f32_16x16x32_bf16 v[156:159], a[176:179], v[72:75], v[156:159]\n"
"s_waitcnt lgkmcnt(14)\n"
"ds_write_b32 v14, v216 offset:128\n"
"v_mfma_f32_16x16x32_bf16 v[156:159], a[192:195], v[76:79], v[156:159]\n"
"s_waitcnt lgkmcnt(14)\n"
"s_add_u32 m0, s79, 12288\n"
"s_add_u32 s81, s78, 19072\n"
"buffer_load_dwordx4 v2, s[68:71], s81 offen lds\n"
"v_mfma_f32_16x16x32_bf16 v[156:159], a[208:211], v[80:83], v[156:159]\n"
"s_waitcnt lgkmcnt(13)\n"
"s_add_u32 m0, s79, 13312\n"
"s_add_u32 s81, s78, 19072\n"
"buffer_load_dwordx4 v3, s[68:71], s81 offen lds\n"
"v_mfma_f32_16x16x32_bf16 v[160:163], a[212:215], v[80:83], v[160:163]\n"
"s_waitcnt vmcnt(18)\n"
"ds_read_b128 v[60:63], v6 offset:0\n"
"ds_read_b128 v[64:67], v7 offset:0\n"
"v_mfma_f32_16x16x32_bf16 v[160:163], a[196:199], v[76:79], v[160:163]\n"
"s_waitcnt lgkmcnt(14)\n"
"ds_read_b128 v[224:227], v8 offset:16384\n"
"s_waitcnt lgkmcnt(14)\n"
"ds_read_b128 v[228:231], v8 offset:34816\n"
"v_mfma_f32_16x16x32_bf16 v[164:167], a[200:203], v[76:79], v[164:167]\n"
"s_waitcnt lgkmcnt(14)\n"
"ds_read_b128 v[232:235], v8 offset:53248\n"
"s_waitcnt lgkmcnt(14)\n"
"ds_read_b128 v[236:239], v10 offset:16384\n"
"v_mfma_f32_16x16x32_bf16 v[164:167], a[216:219], v[80:83], v[164:167]\n"
"s_waitcnt lgkmcnt(14)\n"
"ds_read_b128 v[240:243], v9 offset:16384\n"
"s_waitcnt lgkmcnt(14)\n"
"ds_read_b128 v[244:247], v9 offset:34816\n"
"v_mfma_f32_16x16x32_bf16 v[168:171], a[220:223], v[80:83], v[168:171]\n"
"s_waitcnt lgkmcnt(14)\n"
"ds_read_b128 v[248:251], v9 offset:53248\n"
"s_waitcnt lgkmcnt(14)\n"
"ds_read_b128 v[252:255], v11 offset:16384\n"
"v_mfma_f32_16x16x32_bf16 v[168:171], a[204:207], v[76:79], v[168:171]\n"
"s_add_u32 s45, s45, 331776\n"
"buffer_store_dwordx4 v[112:115], v1, s[40:43], s45 offen sc1\n"
"v_mfma_f32_16x16x32_bf16 v[144:147], v[16:19], v[112:115], v[144:147]\n"
"s_waitcnt lgkmcnt(14)\n"
"ds_write_b128 v12, v[124:127] offset:0\n"
"s_waitcnt lgkmcnt(14)\n"
"ds_write_b128 v12, v[128:131] offset:1024\n"
"s_waitcnt lgkmcnt(14)\n"
"ds_write_b128 v12, v[132:135] offset:2048\n"
"s_waitcnt lgkmcnt(14)\n"
"ds_write_b128 v12, v[136:139] offset:3072\n"
"s_nop 0\n"
"s_waitcnt lgkmcnt(14)\n"
"ds_read_b64_tr_b16 v[124:125], v15 offset:0\n"
"s_waitcnt lgkmcnt(14)\n"
"ds_read_b64_tr_b16 v[126:127], v15 offset:2048\n"
"s_waitcnt lgkmcnt(14)\n"
"ds_read_b64_tr_b16 v[128:129], v15 offset:32\n"
"s_waitcnt lgkmcnt(14)\n"
"ds_read_b64_tr_b16 v[130:131], v15 offset:2080\n"
"s_waitcnt lgkmcnt(14)\n"
"ds_read_b64_tr_b16 v[132:133], v15 offset:64\n"
"s_waitcnt lgkmcnt(14)\n"
"ds_read_b64_tr_b16 v[134:135], v15 offset:2112\n"
"s_waitcnt lgkmcnt(14)\n"
"ds_read_b64_tr_b16 v[136:137], v15 offset:96\n"
"s_waitcnt lgkmcnt(14)\n"
"ds_read_b64_tr_b16 v[138:139], v15 offset:2144\n"
"v_mfma_f32_16x16x32_bf16 v[172:175], v[16:19], v[76:79], v[172:175]\n"
"v_mfma_f32_16x16x32_bf16 v[172:175], v[16:19], v[80:83], v[172:175]\n"
"v_mfma_f32_16x16x32_bf16 v[172:175], v[16:19], v[84:87], v[172:175]\n"
"s_add_u32 m0, s79, 14336\n"
"s_add_u32 s81, s78, 19200\n"
"buffer_load_dwordx4 v2, s[68:71], s81 offen lds\n"
"v_mfma_f32_16x16x32_bf16 v[172:175], v[16:19], v[88:91], v[172:175]\n"
"s_add_u32 m0, s79, 15360\n"
"s_add_u32 s81, s78, 19200\n"
"buffer_load_dwordx4 v3, s[68:71], s81 offen lds\n"
"v_mfma_f32_16x16x32_bf16 v[168:171], a[252:255], v[88:91], v[168:171]\n"
"s_waitcnt vmcnt(19)\n"
"s_waitcnt lgkmcnt(14)\n"
"ds_read_b128 v[68:71], v6 offset:2048\n"
"s_waitcnt lgkmcnt(14)\n"
"ds_read_b128 v[72:75], v7 offset:2048\n"
"v_mfma_f32_16x16x32_bf16 v[168:171], a[236:239], v[84:87], v[168:171]\n"
"s_add_u32 s45, s45, 331776\n"
"buffer_store_dwordx4 v[116:119], v1, s[40:43], s45 offen sc1\n"
"v_mfma_f32_16x16x32_bf16 v[148:151], v[16:19], v[116:119], v[148:151]\n"
"v_mfma_f32_16x16x32_bf16 v[164:167], a[232:235], v[84:87], v[164:167]\n"
"v_mfma_f32_16x16x32_bf16 v[164:167], a[248:251], v[88:91], v[164:167]\n"
"v_mfma_f32_16x16x32_bf16 v[160:163], a[244:247], v[88:91], v[160:163]\n"
"v_mfma_f32_16x16x32_bf16 v[160:163], a[228:231], v[84:87], v[160:163]\n"
"v_mfma_f32_16x16x32_bf16 v[156:159], a[224:227], v[84:87], v[156:159]\n"
"v_mfma_f32_16x16x32_bf16 v[156:159], a[240:243], v[88:91], v[156:159]\n"
"v_mfma_f32_16x16x32_bf16 v[156:159], v[224:227], v[60:63], v[156:159]\n"
"s_add_u32 m0, s79, 0\n"
"s_add_u32 s81, s78, 19328\n"
"buffer_load_dwordx4 v2, s[68:71], s81 offen lds\n"
"v_mfma_f32_16x16x32_bf16 v[156:159], v[240:243], v[64:67], v[156:159]\n"
"s_add_u32 m0, s79, 1024\n"
"s_add_u32 s81, s78, 19328\n"
"buffer_load_dwordx4 v3, s[68:71], s81 offen lds\n"
"v_mfma_f32_16x16x32_bf16 v[160:163], v[244:247], v[64:67], v[160:163]\n"
"s_waitcnt vmcnt(19)\n"
"s_waitcnt lgkmcnt(14)\n"
"ds_read_b128 v[76:79], v6 offset:4096\n"
"s_waitcnt lgkmcnt(14)\n"
"ds_read_b128 v[80:83], v7 offset:4096\n"
"v_mfma_f32_16x16x32_bf16 v[160:163], v[228:231], v[60:63], v[160:163]\n"
"s_add_u32 s45, s45, 331776\n"
"buffer_store_dwordx4 v[120:123], v1, s[40:43], s45 offen sc1\n"
"v_mfma_f32_16x16x32_bf16 v[152:155], v[16:19], v[120:123], v[152:155]\n"
"v_mfma_f32_16x16x32_bf16 v[164:167], v[232:235], v[60:63], v[164:167]\n"
"v_mfma_f32_16x16x32_bf16 v[164:167], v[248:251], v[64:67], v[164:167]\n"
"v_mfma_f32_16x16x32_bf16 v[168:171], v[252:255], v[64:67], v[168:171]\n"
"v_mfma_f32_16x16x32_bf16 v[168:171], v[236:239], v[60:63], v[168:171]\n"
"v_mfma_f32_16x16x32_bf16 v[172:175], v[16:19], v[60:63], v[172:175]\n"
"v_mfma_f32_16x16x32_bf16 v[172:175], v[16:19], v[64:67], v[172:175]\n"
"s_waitcnt lgkmcnt(3)\n"
"v_mfma_f32_16x16x32_bf16 v[176:179], a[0:3], v[68:71], 0\n"
"s_waitcnt lgkmcnt(2)\n"
"s_add_u32 m0, s79, 2048\n"
"s_add_u32 s81, s78, 19456\n"
"buffer_load_dwordx4 v2, s[68:71], s81 offen lds\n"
"v_mfma_f32_16x16x32_bf16 v[176:179], a[16:19], v[72:75], v[176:179]\n"
"s_add_u32 m0, s79, 3072\n"
"s_add_u32 s81, s78, 19456\n"
"buffer_load_dwordx4 v3, s[68:71], s81 offen lds\n"
"v_mfma_f32_16x16x32_bf16 v[180:183], a[20:23], v[72:75], 0\n"
"s_waitcnt vmcnt(19)\n"
"ds_read_b128 v[84:87], v6 offset:6144\n"
"ds_read_b128 v[88:91], v7 offset:6144\n"
"v_mfma_f32_16x16x32_bf16 v[180:183], a[4:7], v[68:71], v[180:183]\n"
"s_mul_i32 s33, s53, 3\n"
"s_add_u32 s33, s33, 2\n"
"s_add_u32 s34, s33, 15\n"
"s_sub_u32 s34, s34, s75\n"
"s_and_b32 s34, s34, 7\n"
"s_cmp_gt_u32 s34, 1\n"
"s_addc_u32 s34, s34, 9\n"
"s_cmp_eq_u32 s33, 8\n"
"s_cselect_b32 s34, 11, s34\n"
"s_cmp_eq_u32 s85, 1\n"
"s_cselect_b32 s34, s93, s34\n"
"s_cselect_b32 s33, s33, s93\n"
"s_mul_i32 s34, s34, 18\n"
"s_add_u32 s33, s33, s34\n"
"s_mul_i32 s34, s75, 1296\n"
"s_add_u32 s33, s33, s34\n"
"s_lshl_b32 s33, s33, 10\n"
"s_add_u32 s45, s33, 589824\n"
"buffer_store_dwordx4 v[124:127], v1, s[40:43], s45 offen sc1\n"
"v_mfma_f32_16x16x32_bf16 v[140:143], v[16:19], v[124:127], v[140:143]\n"
"v_mfma_f32_16x16x32_bf16 v[184:187], a[8:11], v[68:71], 0\n"
"v_mfma_f32_16x16x32_bf16 v[184:187], a[24:27], v[72:75], v[184:187]\n"
"v_mfma_f32_16x16x32_bf16 v[188:191], a[28:31], v[72:75], 0\n"
"v_mfma_f32_16x16x32_bf16 v[188:191], a[12:15], v[68:71], v[188:191]\n"
"v_mul_f32 v196, v24, v172\n"
"v_mul_f32 v197, v26, v172\n"
"v_mfma_f32_16x16x32_bf16 v[192:195], v[16:19], v[68:71], 0\n"
"v_fmac_f32 v196, v25, v156\n"
"v_fmac_f32 v197, v27, v157\n"
"v_mfma_f32_16x16x32_bf16 v[192:195], v[16:19], v[72:75], v[192:195]\n"
"s_waitcnt lgkmcnt(3)\n"
"v_mul_f32 v198, v28, v172\n"
"v_mul_f32 v199, v30, v172\n"
"s_barrier\n"
"v_mfma_f32_16x16x32_bf16 v[192:195], v[16:19], v[76:79], v[192:195]\n"
"s_waitcnt lgkmcnt(2)\n"
"s_waitcnt vmcnt(17)\n"
"ds_read_b128 v[60:63], v6 offset:8192\n"
"ds_read_b128 v[64:67], v7 offset:8192\n"
"v_mfma_f32_16x16x32_bf16 v[192:195], v[16:19], v[80:83], v[192:195]\n"
"s_add_u32 s45, s45, 331776\n"
"buffer_store_dwordx4 v[128:131], v1, s[40:43], s45 offen sc1\n"
"v_mfma_f32_16x16x32_bf16 v[144:147], v[16:19], v[128:131], v[144:147]\n"
"v_mfma_f32_16x16x32_bf16 v[188:191], a[60:63], v[80:83], v[188:191]\n"
"s_add_u32 m0, s46, 0\n"
"s_xor_b32 s47, s44, 262144\n"
"buffer_load_dwordx4 v58, s[36:39], s47 offen  lds\n"
"v_mfma_f32_16x16x32_bf16 v[188:191], a[44:47], v[76:79], v[188:191]\n"
"v_fmac_f32 v198, v29, v158\n"
"v_fmac_f32 v199, v31, v159\n"
"v_mfma_f32_16x16x32_bf16 v[184:187], a[40:43], v[76:79], v[184:187]\n"
"v_mul_f32 v200, v32, v172\n"
"v_mul_f32 v201, v34, v172\n"
"v_mfma_f32_16x16x32_bf16 v[184:187], a[56:59], v[80:83], v[184:187]\n"
"v_fmac_f32 v200, v33, v160\n"
"v_fmac_f32 v201, v35, v161\n"
"v_mfma_f32_16x16x32_bf16 v[180:183], a[52:55], v[80:83], v[180:183]\n"
"v_mul_f32 v202, v36, v172\n"
"v_mul_f32 v203, v38, v172\n"
"v_mfma_f32_16x16x32_bf16 v[180:183], a[36:39], v[76:79], v[180:183]\n"
"v_fmac_f32 v202, v37, v162\n"
"v_fmac_f32 v203, v39, v163\n"
"v_mfma_f32_16x16x32_bf16 v[176:179], a[32:35], v[76:79], v[176:179]\n"
"v_cvt_pk_bf16_f32 v204, v196, v197\n"
"v_cvt_pk_bf16_f32 v205, v198, v199\n"
"v_mfma_f32_16x16x32_bf16 v[176:179], a[48:51], v[80:83], v[176:179]\n"
"s_waitcnt lgkmcnt(3)\n"
"v_cvt_pk_bf16_f32 v206, v200, v201\n"
"v_cvt_pk_bf16_f32 v207, v202, v203\n"
"v_mfma_f32_16x16x32_bf16 v[176:179], a[64:67], v[84:87], v[176:179]\n"
"s_waitcnt lgkmcnt(2)\n"
"s_waitcnt vmcnt(16)\n"
"ds_read_b128 v[68:71], v6 offset:10240\n"
"ds_read_b128 v[72:75], v7 offset:10240\n"
"v_mfma_f32_16x16x32_bf16 v[176:179], a[80:83], v[88:91], v[176:179]\n"
"s_add_u32 s45, s45, 331776\n"
"buffer_store_dwordx4 v[132:135], v1, s[40:43], s45 offen sc1\n"
"v_mfma_f32_16x16x32_bf16 v[148:151], v[16:19], v[132:135], v[148:151]\n"
"v_mfma_f32_16x16x32_bf16 v[180:183], a[84:87], v[88:91], v[180:183]\n"
"s_add_u32 m0, s46, 1024\n"
"s_xor_b32 s47, s44, 278528\n"
"buffer_load_dwordx4 v59, s[36:39], s47 offen  lds\n"
"v_mfma_f32_16x16x32_bf16 v[180:183], a[68:71], v[84:87], v[180:183]\n"
"buffer_store_dwordx4 v[204:207], v1, s[24:27], s62 offen offset:0 sc1\n"
"v_mfma_f32_16x16x32_bf16 v[184:187], a[72:75], v[84:87], v[184:187]\n"
"v_mul_f32 v196, v40, v172\n"
"v_mul_f32 v197, v42, v172\n"
"v_mfma_f32_16x16x32_bf16 v[184:187], a[88:91], v[88:91], v[184:187]\n"
"v_fmac_f32 v196, v41, v164\n"
"v_fmac_f32 v197, v43, v165\n"
"v_mfma_f32_16x16x32_bf16 v[188:191], a[92:95], v[88:91], v[188:191]\n"
"v_mul_f32 v198, v44, v172\n"
"v_mul_f32 v199, v46, v172\n"
"v_mfma_f32_16x16x32_bf16 v[188:191], a[76:79], v[84:87], v[188:191]\n"
"v_fmac_f32 v198, v45, v166\n"
"v_fmac_f32 v199, v47, v167\n"
"v_mfma_f32_16x16x32_bf16 v[192:195], v[16:19], v[84:87], v[192:195]\n"
"v_mul_f32 v200, v48, v172\n"
"v_mul_f32 v201, v50, v172\n"
"v_mfma_f32_16x16x32_bf16 v[192:195], v[16:19], v[88:91], v[192:195]\n"
"s_waitcnt lgkmcnt(3)\n"
"v_fmac_f32 v200, v49, v168\n"
"v_fmac_f32 v201, v51, v169\n"
"v_mfma_f32_16x16x32_bf16 v[192:195], v[16:19], v[60:63], v[192:195]\n"
"s_waitcnt lgkmcnt(2)\n"
"s_waitcnt vmcnt(15)\n"
"ds_read_b128 v[76:79], v6 offset:12288\n"
"ds_read_b128 v[80:83], v7 offset:12288\n"
"v_mfma_f32_16x16x32_bf16 v[192:195], v[16:19], v[64:67], v[192:195]\n"
"s_add_u32 s45, s45, 331776\n"
"buffer_store_dwordx4 v[136:139], v1, s[40:43], s45 offen sc1\n"
"v_mfma_f32_16x16x32_bf16 v[152:155], v[16:19], v[136:139], v[152:155]\n"
"v_mfma_f32_16x16x32_bf16 v[188:191], a[124:127], v[64:67], v[188:191]\n"
"s_add_u32 m0, s46, 8192\n"
"s_xor_b32 s47, s44, 393216\n"
"buffer_load_dwordx4 v58, s[36:39], s47 offen  lds\n"
"v_mfma_f32_16x16x32_bf16 v[188:191], a[108:111], v[60:63], v[188:191]\n"
"v_mul_f32 v202, v52, v172\n"
"v_mul_f32 v203, v54, v172\n"
"v_mfma_f32_16x16x32_bf16 v[184:187], a[104:107], v[60:63], v[184:187]\n"
"v_fmac_f32 v202, v53, v170\n"
"v_fmac_f32 v203, v55, v171\n"
"v_mfma_f32_16x16x32_bf16 v[184:187], a[120:123], v[64:67], v[184:187]\n"
"v_cvt_pk_bf16_f32 v208, v196, v197\n"
"v_cvt_pk_bf16_f32 v209, v198, v199\n"
"v_mfma_f32_16x16x32_bf16 v[180:183], a[116:119], v[64:67], v[180:183]\n"
"v_cvt_pk_bf16_f32 v210, v200, v201\n"
"v_cvt_pk_bf16_f32 v211, v202, v203\n"
"v_mfma_f32_16x16x32_bf16 v[180:183], a[100:103], v[60:63], v[180:183]\n"
"buffer_store_dwordx4 v[208:211], v1, s[24:27], s63 offen offset:0 sc1\n"
"v_mfma_f32_16x16x32_bf16 v[176:179], a[96:99], v[60:63], v[176:179]\n"
"ds_write_b32 v14, v172 offset:0\n"
"v_mfma_f32_16x16x32_bf16 v[176:179], a[112:115], v[64:67], v[176:179]\n"
"s_waitcnt lgkmcnt(4)\n"
"v_mfma_f32_16x16x32_bf16 v[176:179], a[128:131], v[68:71], v[176:179]\n"
"s_waitcnt lgkmcnt(3)\n"
"s_waitcnt vmcnt(15)\n"
"ds_read_b128 v[84:87], v6 offset:14336\n"
"ds_read_b128 v[88:91], v7 offset:14336\n"
"v_mfma_f32_16x16x32_bf16 v[176:179], a[144:147], v[72:75], v[176:179]\n"
"s_cmp_eq_u32 s85, 1\n"
"s_cbranch_scc0 36f\n"
"s_mul_i32 s33, s53, 18432\n"
"s_lshl_b32 s34, s52, 8\n"
"s_add_u32 s33, s33, s34\n"
"s_add_u32 s33, s33, 11206656\n"
"s_lshl_b64 exec, 0xffff, 16\n"
"v_mov_b32 v140, v144\n"
"s_lshl_b64 exec, 0xffff, 32\n"
"v_mov_b32 v140, v148\n"
"s_lshl_b64 exec, 0xffff, 48\n"
"v_mov_b32 v140, v152\n"
"s_mov_b64 exec, -1\n"
"buffer_store_dword v140, v20, s[24:27], s33 offen sc1\n"
"36:\n"
"v_mfma_f32_16x16x32_bf16 v[180:183], a[148:151], v[72:75], v[180:183]\n"
"s_add_u32 m0, s46, 9216\n"
"s_xor_b32 s47, s44, 409600\n"
"buffer_load_dwordx4 v59, s[36:39], s47 offen  lds\n"
"v_mfma_f32_16x16x32_bf16 v[180:183], a[132:135], v[68:71], v[180:183]\n"
"v_mfma_f32_16x16x32_bf16 v[184:187], a[136:139], v[68:71], v[184:187]\n"
"v_mfma_f32_16x16x32_bf16 v[184:187], a[152:155], v[72:75], v[184:187]\n"
"v_mfma_f32_16x16x32_bf16 v[188:191], a[156:159], v[72:75], v[188:191]\n"
"v_mfma_f32_16x16x32_bf16 v[188:191], a[140:143], v[68:71], v[188:191]\n"
"v_mfma_f32_16x16x32_bf16 v[192:195], v[16:19], v[68:71], v[192:195]\n"
"v_mfma_f32_16x16x32_bf16 v[192:195], v[16:19], v[72:75], v[192:195]\n"
"s_waitcnt lgkmcnt(4)\n"
"v_mfma_f32_16x16x32_bf16 v[192:195], v[16:19], v[76:79], v[192:195]\n"
"s_waitcnt lgkmcnt(3)\n"
"s_waitcnt vmcnt(13)\n"
"ds_read_b128 v[60:63], v6 offset:0\n"
"ds_read_b128 v[64:67], v7 offset:0\n"
"v_mfma_f32_16x16x32_bf16 v[192:195], v[16:19], v[80:83], v[192:195]\n"
"s_add_u32 m0, s46, 16384\n"
"s_xor_b32 s47, s44, 262400\n"
"buffer_load_dwordx4 v58, s[36:39], s47 offen  lds\n"
"v_mfma_f32_16x16x32_bf16 v[188:191], a[188:191], v[80:83], v[188:191]\n"
"s_add_u32 m0, s46, 17408\n"
"s_xor_b32 s47, s44, 278784\n"
"buffer_load_dwordx4 v59, s[36:39], s47 offen  lds\n"
"v_mfma_f32_16x16x32_bf16 v[188:191], a[172:175], v[76:79], v[188:191]\n"
"v_mfma_f32_16x16x32_bf16 v[184:187], a[168:171], v[76:79], v[184:187]\n"
"v_mfma_f32_16x16x32_bf16 v[184:187], a[184:187], v[80:83], v[184:187]\n"
"v_mfma_f32_16x16x32_bf16 v[180:183], a[180:183], v[80:83], v[180:183]\n"
"v_mfma_f32_16x16x32_bf16 v[180:183], a[164:167], v[76:79], v[180:183]\n"
"v_mfma_f32_16x16x32_bf16 v[176:179], a[160:163], v[76:79], v[176:179]\n"
"v_mfma_f32_16x16x32_bf16 v[176:179], a[176:179], v[80:83], v[176:179]\n"
"s_waitcnt lgkmcnt(3)\n"
"v_mfma_f32_16x16x32_bf16 v[176:179], a[192:195], v[84:87], v[176:179]\n"
"s_waitcnt lgkmcnt(2)\n"
"s_waitcnt vmcnt(12)\n"
"ds_read_b128 v[68:71], v6 offset:2048\n"
"ds_read_b128 v[72:75], v7 offset:2048\n"
"v_mfma_f32_16x16x32_bf16 v[176:179], a[208:211], v[88:91], v[176:179]\n"
"s_add_u32 m0, s46, 24576\n"
"s_xor_b32 s47, s44, 393472\n"
"buffer_load_dwordx4 v58, s[36:39], s47 offen  lds\n"
"v_mfma_f32_16x16x32_bf16 v[180:183], a[212:215], v[88:91], v[180:183]\n"
"s_add_u32 m0, s46, 25600\n"
"s_xor_b32 s47, s44, 409856\n"
"buffer_load_dwordx4 v59, s[36:39], s47 offen  lds\n"
"v_mfma_f32_16x16x32_bf16 v[180:183], a[196:199], v[84:87], v[180:183]\n"
".Lq1_%=:\n"
"s_cbranch_execz .Lqret%=\n"
"v_mfma_f32_16x16x32_bf16 v[184:187], a[200:203], v[84:87], v[184:187]\n"
"v_mfma_f32_16x16x32_bf16 v[184:187], a[216:219], v[88:91], v[184:187]\n"
"v_mfma_f32_16x16x32_bf16 v[188:191], a[220:223], v[88:91], v[188:191]\n"
"v_mfma_f32_16x16x32_bf16 v[188:191], a[204:207], v[84:87], v[188:191]\n"
"v_mfma_f32_16x16x32_bf16 v[192:195], v[16:19], v[84:87], v[192:195]\n"
"v_mfma_f32_16x16x32_bf16 v[192:195], v[16:19], v[88:91], v[192:195]\n"
"s_waitcnt lgkmcnt(3)\n"
"v_mfma_f32_16x16x32_bf16 v[192:195], v[16:19], v[60:63], v[192:195]\n"
"s_waitcnt lgkmcnt(2)\n"
"s_add_u32 m0, s46, 32768\n"
"s_xor_b32 s47, s44, 262656\n"
"buffer_load_dwordx4 v58, s[36:39], s47 offen  lds\n"
"v_mfma_f32_16x16x32_bf16 v[192:195], v[16:19], v[64:67], v[192:195]\n"
"s_add_u32 m0, s46, 33792\n"
"s_xor_b32 s47, s44, 279040\n"
"buffer_load_dwordx4 v59, s[36:39], s47 offen  lds\n"
"v_mfma_f32_16x16x32_bf16 v[188:191], a[252:255], v[64:67], v[188:191]\n"
"v_mfma_f32_16x16x32_bf16 v[188:191], a[236:239], v[60:63], v[188:191]\n"
"v_mfma_f32_16x16x32_bf16 v[184:187], a[232:235], v[60:63], v[184:187]\n"
"v_mfma_f32_16x16x32_bf16 v[184:187], a[248:251], v[64:67], v[184:187]\n"
"v_mfma_f32_16x16x32_bf16 v[180:183], a[244:247], v[64:67], v[180:183]\n"
"v_mfma_f32_16x16x32_bf16 v[180:183], a[228:231], v[60:63], v[180:183]\n"
"v_mfma_f32_16x16x32_bf16 v[176:179], a[224:227], v[60:63], v[176:179]\n"
"v_mfma_f32_16x16x32_bf16 v[176:179], a[240:243], v[64:67], v[176:179]\n"
"s_waitcnt lgkmcnt(1)\n"
"v_mfma_f32_16x16x32_bf16 v[176:179], v[224:227], v[68:71], v[176:179]\n"
"s_waitcnt lgkmcnt(0)\n"
"s_add_u32 m0, s46, 40960\n"
"s_xor_b32 s47, s44, 393728\n"
"buffer_load_dwordx4 v58, s[36:39], s47 offen  lds\n"
"v_mfma_f32_16x16x32_bf16 v[176:179], v[240:243], v[72:75], v[176:179]\n"
"s_add_u32 m0, s46, 41984\n"
"s_xor_b32 s47, s44, 410112\n"
"buffer_load_dwordx4 v59, s[36:39], s47 offen  lds\n"
"v_mfma_f32_16x16x32_bf16 v[180:183], v[244:247], v[72:75], v[180:183]\n"
"v_mfma_f32_16x16x32_bf16 v[180:183], v[228:231], v[68:71], v[180:183]\n"
"v_mfma_f32_16x16x32_bf16 v[184:187], v[232:235], v[68:71], v[184:187]\n"
"v_mfma_f32_16x16x32_bf16 v[184:187], v[248:251], v[72:75], v[184:187]\n"
"v_mfma_f32_16x16x32_bf16 v[188:191], v[252:255], v[72:75], v[188:191]\n"
"v_mfma_f32_16x16x32_bf16 v[188:191], v[236:239], v[68:71], v[188:191]\n"
"v_mfma_f32_16x16x32_bf16 v[192:195], v[16:19], v[68:71], v[192:195]\n"
"v_mfma_f32_16x16x32_bf16 v[192:195], v[16:19], v[72:75], v[192:195]\n"
"s_nop 7\n"
"v_mul_f32 v24, v24, v192\n"
"v_mul_f32 v26, v26, v192\n"
"v_fmac_f32 v24, v25, v176\n"
"v_fmac_f32 v26, v27, v177\n"
"v_mul_f32 v28, v28, v192\n"
"v_mul_f32 v30, v30, v192\n"
"v_fmac_f32 v28, v29, v178\n"
"v_fmac_f32 v30, v31, v179\n"
"v_mul_f32 v32, v32, v192\n"
"v_mul_f32 v34, v34, v192\n"
"v_fmac_f32 v32, v33, v180\n"
"v_fmac_f32 v34, v35, v181\n"
"v_mul_f32 v36, v36, v192\n"
"v_mul_f32 v38, v38, v192\n"
"v_fmac_f32 v36, v37, v182\n"
"v_fmac_f32 v38, v39, v183\n"
"v_cvt_pk_bf16_f32 v204, v24, v26\n"
"v_cvt_pk_bf16_f32 v205, v28, v30\n"
"v_cvt_pk_bf16_f32 v206, v32, v34\n"
"v_cvt_pk_bf16_f32 v207, v36, v38\n"
"buffer_store_dwordx4 v[204:207], v1, s[24:27], s62 offen offset:1024 sc1\n"
"v_mul_f32 v40, v40, v192\n"
"v_mul_f32 v42, v42, v192\n"
"v_fmac_f32 v40, v41, v184\n"
"v_fmac_f32 v42, v43, v185\n"
"v_mul_f32 v44, v44, v192\n"
"v_mul_f32 v46, v46, v192\n"
"v_fmac_f32 v44, v45, v186\n"
"v_fmac_f32 v46, v47, v187\n"
"v_mul_f32 v48, v48, v192\n"
"v_mul_f32 v50, v50, v192\n"
"v_fmac_f32 v48, v49, v188\n"
"v_fmac_f32 v50, v51, v189\n"
"v_mul_f32 v52, v52, v192\n"
"v_mul_f32 v54, v54, v192\n"
"v_fmac_f32 v52, v53, v190\n"
"v_fmac_f32 v54, v55, v191\n"
"v_cvt_pk_bf16_f32 v208, v40, v42\n"
"v_cvt_pk_bf16_f32 v209, v44, v46\n"
"v_cvt_pk_bf16_f32 v210, v48, v50\n"
"v_cvt_pk_bf16_f32 v211, v52, v54\n"
"buffer_store_dwordx4 v[208:211], v1, s[24:27], s63 offen offset:1024 sc1\n"
"ds_write_b32 v14, v192 offset:64\n"
"s_waitcnt lgkmcnt(0)\n"
"s_barrier\n"
"s_lshl_b32 s84, s77, 1\n"
"s_add_u32 s84, s84, s57\n"
"s_lshl_b32 s84, s84, 6\n"
"s_add_u32 s84, s84, s76\n"
"s_lshl_b32 s84, s84, 2\n"
"s_mov_b32 s75, 2097152\n"
"1:\n"
"s_lshl2_add_u32 s35, s56, s72\n"
"s_cmp_lg_u32 s30, 0\n"
"s_cbranch_scc0 37f\n"
"s_lshr_b32 s48, s35, 1\n"
"s_lshl_b32 s48, s48, 7\n"
"s_add_u32 s48, s48, 12288\n"
"v_and_b32 v2, 7, v0\n"
"v_lshlrev_b32 v2, 3, v2\n"
"buffer_load_dwordx2 v[16:17], v2, s[24:27], s48 offen sc1\n"
"37:\n"
"s_add_u32 m0, s46, 49152\n"
"s_xor_b32 s47, s44, 262912\n"
"buffer_load_dwordx4 v58, s[36:39], s47 offen  lds\n"
"s_add_u32 m0, s46, 50176\n"
"s_xor_b32 s47, s44, 279296\n"
"buffer_load_dwordx4 v59, s[36:39], s47 offen  lds\n"
"s_add_u32 m0, s46, 57344\n"
"s_xor_b32 s47, s44, 393984\n"
"buffer_load_dwordx4 v58, s[36:39], s47 offen  lds\n"
"s_add_u32 m0, s46, 58368\n"
"s_xor_b32 s47, s44, 410368\n"
"buffer_load_dwordx4 v59, s[36:39], s47 offen  lds\n"
"s_add_u32 m0, s46, 65536\n"
"s_xor_b32 s47, s44, 263168\n"
"buffer_load_dwordx4 v58, s[36:39], s47 offen  lds\n"
"s_add_u32 m0, s46, 66560\n"
"s_xor_b32 s47, s44, 279552\n"
"buffer_load_dwordx4 v59, s[36:39], s47 offen  lds\n"
"s_add_u32 m0, s46, 73728\n"
"s_xor_b32 s47, s44, 394240\n"
"buffer_load_dwordx4 v58, s[36:39], s47 offen  lds\n"
"s_add_u32 m0, s46, 74752\n"
"s_xor_b32 s47, s44, 410624\n"
"buffer_load_dwordx4 v59, s[36:39], s47 offen  lds\n"
"s_add_u32 m0, s46, 81920\n"
"s_xor_b32 s47, s44, 263424\n"
"buffer_load_dwordx4 v58, s[36:39], s47 offen  lds\n"
"s_add_u32 m0, s46, 82944\n"
"s_xor_b32 s47, s44, 279808\n"
"buffer_load_dwordx4 v59, s[36:39], s47 offen  lds\n"
"s_cmp_eq_u32 s30, 0\n"
"s_cselect_b64 s[40:41], s[6:7], s[24:25]\n"
"s_cselect_b32 s42, 2359296, s26\n"
"s_mov_b32 s43, 0x20000\n"
"v_and_b32 v12, 3, v0\n"
"v_lshrrev_b32 v13, 2, v0\n"
"v_mul_u32_u24 v3, 1152, v13\n"
"v_lshl_add_u32 v21, v12, 4, v3\n"
"v_lshl_add_u32 v3, v12, 3, v3\n"
"s_mul_i32 s33, s35, 36864\n"
"s_mul_i32 s34, s57, 288\n"
"s_add_u32 s33, s33, s34\n"
"s_mul_i32 s34, s30, 39632896\n"
"s_add_u32 s33, s33, s34\n"
"s_add_u32 s52, s33, 256\n"
"s_lshl_b32 s34, s28, 6\n"
"s_add_u32 s51, s33, s34\n"
"v_lshlrev_b32 v15, 11, v0\n"
"s_lshl_b32 s35, s30, 14\n"
"s_add_u32 s85, s35, 20480\n"
"s_add_u32 s33, s84, s85\n"
"s_waitcnt vmcnt(10)\n"
"s_barrier\n"
"v_mov_b32 v30, s96\n"
"v_mov_b32 v31, s97\n"
"s_cmp_eq_u32 s28, 0\n"
"s_cbranch_scc0 38f\n"
"s_mov_b64 exec, 0xff\n"
"buffer_store_dwordx2 v[30:31], v15, s[24:27], s33 offen sc1\n"
"s_mov_b64 exec, -1\n"
"38:\n"
"s_cmp_lg_u32 s30, 0\n"
"s_cbranch_scc0 39f\n"
"v_cmp_ne_u64 vcc, s[96:97], v[16:17]\n"
"s_nop 4\n"
"s_cbranch_vccz 40f\n"
"41:\n"
"buffer_load_dwordx2 v[16:17], v2, s[24:27], s48 offen sc1\n"
"s_waitcnt vmcnt(0)\n"
"v_cmp_ne_u64 vcc, s[96:97], v[16:17]\n"
"s_nop 4\n"
"s_cbranch_vccz 40f\n"
"s_sleep 1\n"
"s_sub_u32 s75, s75, 1\n"
"s_cmp_gt_i32 s75, 0\n"
"s_cbranch_scc1 41b\n"
"40:\n"
"39:\n"
"v_mov_b32 v112, 0\n"
"v_mov_b32 v113, 0\n"
"v_mov_b32 v114, 0\n"
"v_mov_b32 v115, 0\n"
"s_cmp_eq_u32 s28, 0\n"
"s_cbranch_scc0 42f\n"
"s_add_u32 s33, s52, 18432\n"
"buffer_load_dwordx2 v[112:113], v3, s[40:43], s52 offen sc1\n"
"buffer_load_dwordx2 v[114:115], v3, s[40:43], s33 offen sc1\n"
"42:\n"
"s_add_u32 s33, s51, 18432\n"
"buffer_load_dwordx4 v[104:107], v21, s[40:43], s51 offen sc1\n"
"buffer_load_dwordx4 v[108:111], v21, s[40:43], s33 offen sc1\n"
"s_add_u32 m0, s46, 90112\n"
"s_xor_b32 s47, s44, 394496\n"
"buffer_load_dwordx4 v58, s[36:39], s47 offen  lds\n"
"s_add_u32 m0, s46, 91136\n"
"s_xor_b32 s47, s44, 410880\n"
"buffer_load_dwordx4 v59, s[36:39], s47 offen  lds\n"
"s_add_u32 m0, s46, 98304\n"
"s_xor_b32 s47, s44, 263680\n"
"buffer_load_dwordx4 v58, s[36:39], s47 offen  lds\n"
"s_add_u32 m0, s46, 99328\n"
"s_xor_b32 s47, s44, 280064\n"
"buffer_load_dwordx4 v59, s[36:39], s47 offen  lds\n"
"s_add_u32 m0, s46, 106496\n"
"s_xor_b32 s47, s44, 394752\n"
"buffer_load_dwordx4 v58, s[36:39], s47 offen  lds\n"
"s_add_u32 m0, s46, 107520\n"
"s_xor_b32 s47, s44, 411136\n"
"buffer_load_dwordx4 v59, s[36:39], s47 offen  lds\n"
"s_add_u32 m0, s46, 114688\n"
"s_xor_b32 s47, s44, 263936\n"
"buffer_load_dwordx4 v58, s[36:39], s47 offen  lds\n"
"s_add_u32 m0, s46, 115712\n"
"s_xor_b32 s47, s44, 280320\n"
"buffer_load_dwordx4 v59, s[36:39], s47 offen  lds\n"
"s_add_u32 m0, s46, 122880\n"
"s_xor_b32 s47, s44, 395008\n"
"buffer_load_dwordx4 v58, s[36:39], s47 offen  lds\n"
"s_add_u32 m0, s46, 123904\n"
"s_xor_b32 s47, s44, 411392\n"
"buffer_load_dwordx4 v59, s[36:39], s47 offen  lds\n"
"v_lshrrev_b32 v12, 4, v0\n"
"v_and_b32 v13, 15, v0\n"
"v_xor_b32 v2, v12, v13\n"
"v_lshlrev_b32 v2, 4, v2\n"
"v_lshl_add_u32 v4, v13, 8, v2\n"
"s_lshl_b32 s45, s55, 2\n"
"s_add_u32 s45, s45, s57\n"
"s_lshl_b32 s45, s45, 2\n"
"s_add_u32 s45, s45, s28\n"
"s_mul_i32 s45, s45, 73728\n"
"s_mul_i32 s33, s30, 4718592\n"
"s_add_u32 s45, s45, s33\n"
"s_add_u32 s45, s45, 11321344\n"
"s_lshl_b32 s33, s28, 6\n"
"v_xor_b32 v4, s33, v4\n"
"v_xor_b32 v5, 64, v4\n"
"v_xor_b32 v6, 128, v4\n"
"v_xor_b32 v7, 192, v4\n"
"v_xor_b32 v8, 65536, v4\n"
"v_xor_b32 v9, 65600, v4\n"
"v_xor_b32 v10, 65664, v4\n"
"v_xor_b32 v11, 65728, v4\n"
"s_lshl_b32 s35, s56, 2\n"
"s_add_u32 s35, s35, s72\n"
"s_lshr_b32 s33, s35, 1\n"
"s_mul_i32 s49, s33, 73728\n"
"s_and_b32 s33, s35, 1\n"
"s_lshl_b32 s33, s33, 11\n"
"s_add_u32 s49, s49, s33\n"
"s_lshr_b32 s33, s33, 1\n"
"s_sub_u32 s50, s49, s33\n"
"s_add_u32 s33, s57, 32\n"
"s_lshl_b32 s33, s33, 11\n"
"s_add_u32 s50, s50, s33\n"
"s_lshl_b32 s33, s57, 14\n"
"s_add_u32 s49, s49, s33\n"
"s_lshl_b32 s33, s28, 12\n"
"s_add_u32 s49, s49, s33\n"
"s_mul_i32 s33, s30, 2359296\n"
"s_add_u32 s33, s33, 34914304\n"
"s_add_u32 s49, s49, s33\n"
"s_add_u32 s50, s50, s33\n"
"s_lshl_b32 s33, s30, 4\n"
"s_add_u32 s33, s33, s56\n"
"s_lshl_b32 s33, s33, 2\n"
"s_add_u32 s33, s33, s57\n"
"s_lshl_b32 s54, s33, 2\n"
"s_add_u32 s54, s54, s72\n"
"s_lshl_b32 s54, s54, 7\n"
"s_add_u32 s54, s54, 69632\n"
"s_mul_i32 s53, s33, 110592\n"
"s_add_u32 s53, s53, 20758528\n"
"s_lshl_b32 s33, s28, 12\n"
"s_add_u32 s33, s33, 131072\n"
"v_add_u32 v18, s33, v1\n"
"v_add_u32 v19, s46, v1\n"
"s_lshl_b32 s33, s58, 11\n"
"s_add_u32 s33, s33, 131072\n"
"v_add_u32 v20, s33, v1\n"
"v_mov_b32 v16, 0x7fffffff\n"
"v_mov_b32 v36, 0\n"
"v_mov_b32 v24, 0\n"
"v_mov_b32 v37, 0\n"
"v_mov_b32 v25, 0\n"
"v_mov_b32 v38, 0\n"
"v_mov_b32 v26, 0\n"
"v_mov_b32 v39, 0\n"
"v_mov_b32 v27, 0\n"
"v_and_b32 v32, 1, v0\n"
"v_lshlrev_b32 v32, 4, v32\n"
"v_mov_b32 v33, 0x3f80\n"
"v_lshlrev_b32 v32, v32, v33\n"
"s_mov_b32 exec_lo, 0x300003\n"
"s_mov_b32 exec_hi, 0x30000300\n"
"v_mov_b32 v36, v32\n"
"v_mov_b32 v26, v32\n"
"s_mov_b32 exec_lo, 0xc0000c\n"
"s_mov_b32 exec_hi, 0xc0000c00\n"
"v_mov_b32 v37, v32\n"
"v_mov_b32 v27, v32\n"
"s_mov_b64 exec, -1\n"
"v_mov_b32 v28, 0\n"
"v_mov_b32 v29, 0\n"
"s_lshl_b32 s62, s60, 2\n"
"s_add_u32 s62, s62, s28\n"
"s_mul_i32 s62, s62, 331776\n"
"s_add_u32 s62, s62, 589824\n"
"s_lshl_b32 s33, s28, 10\n"
"v_add_u32 v14, s33, v1\n"
"v_cmp_gt_u32 vcc, 2304, v14\n"
"s_cmp_eq_u32 s30, 0\n"
"s_cselect_b64 s[94:95], vcc, 0\n"
"s_mov_b64 vcc, s[94:95]\n"
"v_cndmask_b32 v13, v16, v14, vcc\n"
"v_add_u32 v14, 161536, v14\n"
"v_mov_b32 v88, 0\n"
"v_mov_b32 v89, 0\n"
"v_mov_b32 v90, 0\n"
"v_mov_b32 v91, 0\n"
"v_mov_b32 v92, 0\n"
"v_mov_b32 v93, 0\n"
"v_mov_b32 v94, 0\n"
"v_mov_b32 v95, 0\n"
"s_mov_b32 exec_lo, 0x3000003\n"
"s_mov_b32 exec_hi, 0\n"
"v_mov_b32 v88, v32\n"
"s_lshl_b64 exec, exec, 32\n"
"v_mov_b32 v92, v32\n"
"s_mov_b32 exec_lo, 0xc00000c\n"
"s_mov_b32 exec_hi, 0\n"
"v_mov_b32 v89, v32\n"
"s_lshl_b64 exec, exec, 32\n"
"v_mov_b32 v93, v32\n"
"s_mov_b32 exec_lo, 0x30000030\n"
"s_mov_b32 exec_hi, 0\n"
"v_mov_b32 v90, v32\n"
"s_lshl_b64 exec, exec, 32\n"
"v_mov_b32 v94, v32\n"
"s_mov_b32 exec_lo, 0xc00000c0\n"
"s_mov_b32 exec_hi, 0\n"
"v_mov_b32 v91, v32\n"
"s_lshl_b64 exec, exec, 32\n"
"v_mov_b32 v95, v32\n"
"s_mov_b64 exec, -1\n"
"v_and_b32 v32, 15, v0\n"
"v_lshlrev_b32 v32, 3, v32\n"
"v_lshlrev_b32 v15, 3, v0\n"
"s_lshr_b32 s33, s56, 1\n"
"s_lshl_b32 s33, s33, 11\n"
"s_add_u32 s85, s85, s33\n"
"s_and_b32 s33, s55, 2\n"
"s_lshl_b32 s33, s33, 1\n"
"s_add_u32 s33, s33, s57\n"
"s_lshl_b32 s33, s33, 1\n"
"s_and_b32 s34, s55, 1\n"
"s_add_u32 s33, s33, s34\n"
"s_lshl_b32 s33, s33, 7\n"
"s_add_u32 s33, s33, s85\n"
"s_lshl_b32 s34, s28, 9\n"
"s_add_u32 s85, s85, s34\n"
"s_cmp_eq_u32 s28, 0\n"
"s_cbranch_scc1 43f\n"
"s_cmp_lg_u32 s30, 0\n"
"s_cbranch_scc1 44f\n"
"s_mov_b64 exec, 0\n"
"s_cmp_eq_u32 s28, 1\n"
"s_cbranch_scc1 .Lp0_0_%=\n"
"s_cmp_eq_u32 s28, 2\n"
"s_cbranch_scc1 .Lp1_0_%=\n"
"s_branch .Lp2_0_%=\n"
".Lpret%=:\n"
"s_mov_b64 exec, -1\n"
"s_branch 44f\n"
"43:\n"
"45:\n"
"buffer_load_dwordx2 v[16:17], v32, s[24:27], s33 offen sc1\n"
"s_waitcnt vmcnt(0)\n"
"v_cmp_ne_u64 vcc, s[96:97], v[16:17]\n"
"s_nop 4\n"
"s_cbranch_vccz 44f\n"
"s_sleep 1\n"
"s_sub_u32 s75, s75, 1\n"
"s_cmp_gt_i32 s75, 0\n"
"s_cbranch_scc1 45b\n"
"44:\n"
"s_barrier\n"
"s_add_u32 s48, s45, 0\n"
"buffer_load_dwordx4 a[0:3], v1, s[24:27], s48 offen offset:0 sc1\n"
"buffer_load_dwordx4 a[4:7], v1, s[24:27], s48 offen offset:1024 sc1\n"
"buffer_load_dwordx4 a[8:11], v1, s[24:27], s48 offen offset:2048 sc1\n"
"buffer_load_dwordx4 a[12:15], v1, s[24:27], s48 offen offset:3072 sc1\n"
"s_add_u32 s48, s45, 4096\n"
"buffer_load_dwordx4 a[16:19], v1, s[24:27], s48 offen offset:0 sc1\n"
"buffer_load_dwordx4 a[20:23], v1, s[24:27], s48 offen offset:1024 sc1\n"
"buffer_load_dwordx4 a[24:27], v1, s[24:27], s48 offen offset:2048 sc1\n"
"buffer_load_dwordx4 a[28:31], v1, s[24:27], s48 offen offset:3072 sc1\n"
"s_add_u32 s48, s45, 8192\n"
"buffer_load_dwordx4 a[32:35], v1, s[24:27], s48 offen offset:0 sc1\n"
"buffer_load_dwordx4 a[36:39], v1, s[24:27], s48 offen offset:1024 sc1\n"
"buffer_load_dwordx4 a[40:43], v1, s[24:27], s48 offen offset:2048 sc1\n"
"buffer_load_dwordx4 a[44:47], v1, s[24:27], s48 offen offset:3072 sc1\n"
"s_add_u32 s48, s45, 12288\n"
"buffer_load_dwordx4 a[48:51], v1, s[24:27], s48 offen offset:0 sc1\n"
"buffer_load_dwordx4 a[52:55], v1, s[24:27], s48 offen offset:1024 sc1\n"
"v_and_b32 v21, 15, v0\n"
"v_lshrrev_b32 v22, 4, v0\n"
"v_lshl_add_u32 v21, v21, 2, v22\n"
"v_lshlrev_b32 v21, 2, v21\n"
"s_waitcnt vmcnt(25)\n"
"ds_bpermute_b32 v104, v21, v104\n"
"ds_bpermute_b32 v105, v21, v105\n"
"ds_bpermute_b32 v106, v21, v106\n"
"ds_bpermute_b32 v107, v21, v107\n"
"s_waitcnt vmcnt(24)\n"
"ds_bpermute_b32 v108, v21, v108\n"
"ds_bpermute_b32 v109, v21, v109\n"
"ds_bpermute_b32 v110, v21, v110\n"
"ds_bpermute_b32 v111, v21, v111\n"
"ds_bpermute_b32 v112, v21, v112\n"
"ds_bpermute_b32 v113, v21, v113\n"
"ds_bpermute_b32 v114, v21, v114\n"
"ds_bpermute_b32 v115, v21, v115\n"
"s_waitcnt lgkmcnt(8)\n"
"v_mfma_f32_16x16x32_bf16 v[32:35], v[88:91], v[104:107], 0\n"
"v_mfma_f32_16x16x32_bf16 v[36:39], v[92:95], v[104:107], 0\n"
"s_waitcnt lgkmcnt(0)\n"
"v_mfma_f32_16x16x32_bf16 v[48:51], v[26:29], v[112:115], 0\n"
"v_mfma_f32_16x16x32_bf16 v[40:43], v[88:91], v[108:111], 0\n"
"v_mfma_f32_16x16x32_bf16 v[44:47], v[92:95], v[108:111], 0\n"
"v_mfma_f32_16x16x32_bf16 v[52:55], v[24:27], v[112:115], 0\n"
"ds_read_b128 v[192:195], v4 offset:0\n"
"ds_read_b128 v[196:199], v4 offset:4096\n"
"ds_read_b128 v[200:203], v4 offset:8192\n"
"ds_read_b128 v[204:207], v4 offset:12288\n"
"s_waitcnt vmcnt(13) lgkmcnt(3)\n"
"v_mfma_f32_16x16x32_bf16 v[144:147], a[0:3], v[192:195], 0\n"
"s_waitcnt vmcnt(12)\n"
"ds_read_b128 v[208:211], v5 offset:0\n"
"v_mfma_f32_16x16x32_bf16 v[148:151], a[4:7], v[192:195], 0\n"
"s_waitcnt vmcnt(11)\n"
"ds_read_b128 v[212:215], v5 offset:4096\n"
"v_mfma_f32_16x16x32_bf16 v[176:179], a[8:11], v[192:195], 0\n"
"s_waitcnt lgkmcnt(4)\n"
"ds_read_b128 v[216:219], v5 offset:8192\n"
"v_mfma_f32_16x16x32_bf16 v[152:155], a[0:3], v[196:199], 0\n"
"ds_read_b128 v[220:223], v5 offset:12288\n"
"buffer_load_dwordx4 a[56:59], v1, s[24:27], s48 offen offset:2048 sc1\n"
"v_mfma_f32_16x16x32_bf16 v[156:159], a[4:7], v[196:199], 0\n"
"v_mfma_f32_16x16x32_bf16 v[180:183], a[8:11], v[196:199], 0\n"
"s_waitcnt lgkmcnt(5)\n"
"v_mfma_f32_16x16x32_bf16 v[160:163], a[0:3], v[200:203], 0\n"
"v_mfma_f32_16x16x32_bf16 v[164:167], a[4:7], v[200:203], 0\n"
"buffer_load_dwordx4 a[60:63], v1, s[24:27], s48 offen offset:3072 sc1\n"
"v_mfma_f32_16x16x32_bf16 v[184:187], a[8:11], v[200:203], 0\n"
"s_waitcnt lgkmcnt(4)\n"
"v_mfma_f32_16x16x32_bf16 v[168:171], a[0:3], v[204:207], 0\n"
"v_mfma_f32_16x16x32_bf16 v[172:175], a[4:7], v[204:207], 0\n"
"v_mfma_f32_16x16x32_bf16 v[188:191], a[8:11], v[204:207], 0\n"
"s_add_u32 s48, s45, 16384\n"
"buffer_load_dwordx4 a[64:67], v1, s[24:27], s48 offen offset:0 sc1\n"
"s_waitcnt vmcnt(13) lgkmcnt(3)\n"
"v_mfma_f32_16x16x32_bf16 v[144:147], a[12:15], v[208:211], v[144:147]\n"
"s_waitcnt vmcnt(12)\n"
"ds_read_b128 v[192:195], v6 offset:0\n"
"v_mfma_f32_16x16x32_bf16 v[148:151], a[16:19], v[208:211], v[148:151]\n"
"s_waitcnt lgkmcnt(3)\n"
"ds_read_b128 v[196:199], v6 offset:4096\n"
"v_mfma_f32_16x16x32_bf16 v[152:155], a[12:15], v[212:215], v[152:155]\n"
"ds_read_b128 v[200:203], v6 offset:8192\n"
"v_mfma_f32_16x16x32_bf16 v[156:159], a[16:19], v[212:215], v[156:159]\n"
"s_waitcnt lgkmcnt(4)\n"
"ds_read_b128 v[204:207], v6 offset:12288\n"
"buffer_load_dwordx4 a[68:71], v1, s[24:27], s48 offen offset:1024 sc1\n"
"v_mfma_f32_16x16x32_bf16 v[160:163], a[12:15], v[216:219], v[160:163]\n"
"v_mfma_f32_16x16x32_bf16 v[164:167], a[16:19], v[216:219], v[164:167]\n"
"s_waitcnt lgkmcnt(4)\n"
"v_mfma_f32_16x16x32_bf16 v[168:171], a[12:15], v[220:223], v[168:171]\n"
"buffer_load_dwordx4 a[72:75], v1, s[24:27], s48 offen offset:2048 sc1\n"
"v_mfma_f32_16x16x32_bf16 v[172:175], a[16:19], v[220:223], v[172:175]\n"
"buffer_load_dwordx2 v[16:17], v15, s[24:27], s85 offen sc1\n"
"s_waitcnt vmcnt(14) lgkmcnt(3)\n"
"v_mfma_f32_16x16x32_bf16 v[144:147], a[20:23], v[192:195], v[144:147]\n"
"s_waitcnt vmcnt(13)\n"
"ds_read_b128 v[208:211], v7 offset:0\n"
"v_mfma_f32_16x16x32_bf16 v[148:151], a[24:27], v[192:195], v[148:151]\n"
"s_waitcnt lgkmcnt(3)\n"
"ds_read_b128 v[212:215], v7 offset:4096\n"
"v_mfma_f32_16x16x32_bf16 v[152:155], a[20:23], v[196:199], v[152:155]\n"
"ds_read_b128 v[216:219], v7 offset:8192\n"
"buffer_load_dwordx4 a[76:79], v1, s[24:27], s48 offen offset:3072 sc1\n"
"v_mfma_f32_16x16x32_bf16 v[156:159], a[24:27], v[196:199], v[156:159]\n"
"s_waitcnt lgkmcnt(4)\n"
"ds_read_b128 v[220:223], v7 offset:12288\n"
"v_mfma_f32_16x16x32_bf16 v[160:163], a[20:23], v[200:203], v[160:163]\n"
"v_mfma_f32_16x16x32_bf16 v[164:167], a[24:27], v[200:203], v[164:167]\n"
"s_waitcnt lgkmcnt(4)\n"
"v_mfma_f32_16x16x32_bf16 v[168:171], a[20:23], v[204:207], v[168:171]\n"
"s_add_u32 s48, s45, 20480\n"
"buffer_load_dwordx4 a[80:83], v1, s[24:27], s48 offen offset:0 sc1\n"
"v_mfma_f32_16x16x32_bf16 v[172:175], a[24:27], v[204:207], v[172:175]\n"
"s_waitcnt vmcnt(14) lgkmcnt(3)\n"
"v_mfma_f32_16x16x32_bf16 v[144:147], a[28:31], v[208:211], v[144:147]\n"
"s_waitcnt vmcnt(13)\n"
"ds_read_b128 v[192:195], v4 offset:16384\n"
"v_mfma_f32_16x16x32_bf16 v[148:151], a[32:35], v[208:211], v[148:151]\n"
"s_waitcnt lgkmcnt(3)\n"
"ds_read_b128 v[196:199], v4 offset:20480\n"
"v_mfma_f32_16x16x32_bf16 v[152:155], a[28:31], v[212:215], v[152:155]\n"
"ds_read_b128 v[200:203], v4 offset:24576\n"
"buffer_load_dwordx4 a[84:87], v1, s[24:27], s48 offen offset:1024 sc1\n"
"v_mfma_f32_16x16x32_bf16 v[156:159], a[32:35], v[212:215], v[156:159]\n"
"s_waitcnt lgkmcnt(4)\n"
"ds_read_b128 v[204:207], v4 offset:28672\n"
"v_mfma_f32_16x16x32_bf16 v[160:163], a[28:31], v[216:219], v[160:163]\n"
"v_mfma_f32_16x16x32_bf16 v[164:167], a[32:35], v[216:219], v[164:167]\n"
"s_waitcnt lgkmcnt(4)\n"
"v_mfma_f32_16x16x32_bf16 v[168:171], a[28:31], v[220:223], v[168:171]\n"
"buffer_load_dwordx4 a[88:91], v1, s[24:27], s48 offen offset:2048 sc1\n"
"v_mfma_f32_16x16x32_bf16 v[172:175], a[32:35], v[220:223], v[172:175]\n"
"s_waitcnt vmcnt(14) lgkmcnt(3)\n"
"v_mfma_f32_16x16x32_bf16 v[144:147], a[36:39], v[192:195], v[144:147]\n"
"s_waitcnt vmcnt(13)\n"
"ds_read_b128 v[208:211], v5 offset:16384\n"
"v_mfma_f32_16x16x32_bf16 v[148:151], a[40:43], v[192:195], v[148:151]\n"
"s_waitcnt vmcnt(12)\n"
"ds_read_b128 v[212:215], v5 offset:20480\n"
"buffer_load_dwordx4 a[92:95], v1, s[24:27], s48 offen offset:3072 sc1\n"
"v_mfma_f32_16x16x32_bf16 v[176:179], a[44:47], v[192:195], v[176:179]\n"
"s_waitcnt lgkmcnt(4)\n"
"ds_read_b128 v[216:219], v5 offset:24576\n"
"v_mfma_f32_16x16x32_bf16 v[152:155], a[36:39], v[196:199], v[152:155]\n"
"ds_read_b128 v[220:223], v5 offset:28672\n"
"v_mfma_f32_16x16x32_bf16 v[156:159], a[40:43], v[196:199], v[156:159]\n"
"v_mfma_f32_16x16x32_bf16 v[180:183], a[44:47], v[196:199], v[180:183]\n"
"s_waitcnt lgkmcnt(5)\n"
"s_add_u32 s48, s45, 24576\n"
"buffer_load_dwordx4 a[96:99], v1, s[24:27], s48 offen offset:0 sc1\n"
"v_mfma_f32_16x16x32_bf16 v[160:163], a[36:39], v[200:203], v[160:163]\n"
"v_mfma_f32_16x16x32_bf16 v[164:167], a[40:43], v[200:203], v[164:167]\n"
"v_mfma_f32_16x16x32_bf16 v[184:187], a[44:47], v[200:203], v[184:187]\n"
"s_waitcnt lgkmcnt(4)\n"
"v_mfma_f32_16x16x32_bf16 v[168:171], a[36:39], v[204:207], v[168:171]\n"
"buffer_load_dwordx4 a[100:103], v1, s[24:27], s48 offen offset:1024 sc1\n"
"v_mfma_f32_16x16x32_bf16 v[172:175], a[40:43], v[204:207], v[172:175]\n"
"v_mfma_f32_16x16x32_bf16 v[188:191], a[44:47], v[204:207], v[188:191]\n"
"s_waitcnt vmcnt(14) lgkmcnt(3)\n"
"v_mfma_f32_16x16x32_bf16 v[144:147], a[48:51], v[208:211], v[144:147]\n"
"s_waitcnt vmcnt(13)\n"
"ds_read_b128 v[192:195], v6 offset:16384\n"
"v_mfma_f32_16x16x32_bf16 v[148:151], a[52:55], v[208:211], v[148:151]\n"
"s_waitcnt lgkmcnt(3)\n"
"ds_read_b128 v[196:199], v6 offset:20480\n"
"buffer_load_dwordx4 a[104:107], v1, s[24:27], s48 offen offset:2048 sc1\n"
"v_mfma_f32_16x16x32_bf16 v[152:155], a[48:51], v[212:215], v[152:155]\n"
"ds_read_b128 v[200:203], v6 offset:24576\n"
"v_mfma_f32_16x16x32_bf16 v[156:159], a[52:55], v[212:215], v[156:159]\n"
"s_waitcnt lgkmcnt(4)\n"
"ds_read_b128 v[204:207], v6 offset:28672\n"
"v_mfma_f32_16x16x32_bf16 v[160:163], a[48:51], v[216:219], v[160:163]\n"
"v_mfma_f32_16x16x32_bf16 v[164:167], a[52:55], v[216:219], v[164:167]\n"
"s_waitcnt lgkmcnt(4)\n"
"buffer_load_dwordx4 a[108:111], v1, s[24:27], s48 offen offset:3072 sc1\n"
"v_mfma_f32_16x16x32_bf16 v[168:171], a[48:51], v[220:223], v[168:171]\n"
"v_mfma_f32_16x16x32_bf16 v[172:175], a[52:55], v[220:223], v[172:175]\n"
"s_waitcnt vmcnt(14) lgkmcnt(3)\n"
"v_mfma_f32_16x16x32_bf16 v[144:147], a[56:59], v[192:195], v[144:147]\n"
"s_waitcnt vmcnt(13)\n"
"ds_read_b128 v[208:211], v7 offset:16384\n"
"s_add_u32 s48, s45, 28672\n"
"buffer_load_dwordx4 a[112:115], v1, s[24:27], s48 offen offset:0 sc1\n"
"v_mfma_f32_16x16x32_bf16 v[148:151], a[60:63], v[192:195], v[148:151]\n"
"s_waitcnt lgkmcnt(3)\n"
"ds_read_b128 v[212:215], v7 offset:20480\n"
"v_mfma_f32_16x16x32_bf16 v[152:155], a[56:59], v[196:199], v[152:155]\n"
"ds_read_b128 v[216:219], v7 offset:24576\n"
"v_mfma_f32_16x16x32_bf16 v[156:159], a[60:63], v[196:199], v[156:159]\n"
"s_waitcnt lgkmcnt(4)\n"
"ds_read_b128 v[220:223], v7 offset:28672\n"
"v_mfma_f32_16x16x32_bf16 v[160:163], a[56:59], v[200:203], v[160:163]\n"
"buffer_load_dwordx4 a[116:119], v1, s[24:27], s48 offen offset:1024 sc1\n"
"v_mfma_f32_16x16x32_bf16 v[164:167], a[60:63], v[200:203], v[164:167]\n"
"s_waitcnt lgkmcnt(4)\n"
"v_mfma_f32_16x16x32_bf16 v[168:171], a[56:59], v[204:207], v[168:171]\n"
"v_mfma_f32_16x16x32_bf16 v[172:175], a[60:63], v[204:207], v[172:175]\n"
"s_waitcnt vmcnt(11)\n"
"v_cmp_ne_u64 vcc, s[96:97], v[16:17]\n"
"s_nop 4\n"
"s_cbranch_vccz 46f\n"
"47:\n"
"buffer_load_dwordx2 v[16:17], v15, s[24:27], s85 offen sc1\n"
"s_waitcnt vmcnt(0)\n"
"v_cmp_ne_u64 vcc, s[96:97], v[16:17]\n"
"s_nop 4\n"
"s_cbranch_vccz 46f\n"
"s_sleep 1\n"
"s_sub_u32 s75, s75, 1\n"
"s_cmp_gt_i32 s75, 0\n"
"s_cbranch_scc1 47b\n"
"46:\n"
"s_waitcnt lgkmcnt(0)\n"
"s_barrier\n"
"v_mfma_f32_16x16x32_bf16 v[144:147], a[64:67], v[208:211], v[144:147]\n"
"ds_read_b128 v[192:195], v4 offset:32768\n"
"buffer_load_dwordx4 a[120:123], v1, s[24:27], s48 offen offset:2048 sc1\n"
"v_mfma_f32_16x16x32_bf16 v[148:151], a[68:71], v[208:211], v[148:151]\n"
"ds_read_b128 v[196:199], v4 offset:36864\n"
"v_mfma_f32_16x16x32_bf16 v[152:155], a[64:67], v[212:215], v[152:155]\n"
"ds_read_b128 v[200:203], v4 offset:40960\n"
"v_mfma_f32_16x16x32_bf16 v[156:159], a[68:71], v[212:215], v[156:159]\n"
"ds_read_b128 v[204:207], v4 offset:45056\n"
"v_mfma_f32_16x16x32_bf16 v[160:163], a[64:67], v[216:219], v[160:163]\n"
"buffer_load_dwordx4 a[124:127], v1, s[24:27], s48 offen offset:3072 sc1\n"
"v_mfma_f32_16x16x32_bf16 v[164:167], a[68:71], v[216:219], v[164:167]\n"
"v_mfma_f32_16x16x32_bf16 v[168:171], a[64:67], v[220:223], v[168:171]\n"
"v_mfma_f32_16x16x32_bf16 v[172:175], a[68:71], v[220:223], v[172:175]\n"
"s_or_b32 s33, s29, s28\n"
"s_cmp_eq_u32 s33, 0\n"
"s_cbranch_scc0 48f\n"
"s_mov_b64 exec, 1\n"
"buffer_store_dword v30, off, s[24:27], 0 offset:0 sc1\n"
"s_mov_b64 exec, -1\n"
"48:\n"
"s_waitcnt lgkmcnt(3)\n"
"v_mfma_f32_16x16x32_bf16 v[144:147], a[72:75], v[192:195], v[144:147]\n"
"s_waitcnt vmcnt(12)\n"
"ds_read_b128 v[208:211], v5 offset:32768\n"
"s_add_u32 s48, s45, 32768\n"
"buffer_load_dwordx4 a[128:131], v1, s[24:27], s48 offen offset:0 sc1\n"
"v_mfma_f32_16x16x32_bf16 v[148:151], a[76:79], v[192:195], v[148:151]\n"
"s_waitcnt vmcnt(12)\n"
"ds_read_b128 v[212:215], v5 offset:36864\n"
"v_mfma_f32_16x16x32_bf16 v[176:179], a[80:83], v[192:195], v[176:179]\n"
"s_waitcnt lgkmcnt(4)\n"
"ds_read_b128 v[216:219], v5 offset:40960\n"
"v_mfma_f32_16x16x32_bf16 v[152:155], a[72:75], v[196:199], v[152:155]\n"
"ds_read_b128 v[220:223], v5 offset:45056\n"
"buffer_load_dwordx4 a[132:135], v1, s[24:27], s48 offen offset:1024 sc1\n"
"v_mfma_f32_16x16x32_bf16 v[156:159], a[76:79], v[196:199], v[156:159]\n"
"v_mfma_f32_16x16x32_bf16 v[180:183], a[80:83], v[196:199], v[180:183]\n"
"s_waitcnt lgkmcnt(5)\n"
"v_mfma_f32_16x16x32_bf16 v[160:163], a[72:75], v[200:203], v[160:163]\n"
"v_mfma_f32_16x16x32_bf16 v[164:167], a[76:79], v[200:203], v[164:167]\n"
"buffer_load_dwordx4 a[136:139], v1, s[24:27], s48 offen offset:2048 sc1\n"
"v_mfma_f32_16x16x32_bf16 v[184:187], a[80:83], v[200:203], v[184:187]\n"
"s_waitcnt lgkmcnt(4)\n"
"v_mfma_f32_16x16x32_bf16 v[168:171], a[72:75], v[204:207], v[168:171]\n"
"v_mfma_f32_16x16x32_bf16 v[172:175], a[76:79], v[204:207], v[172:175]\n"
"v_mfma_f32_16x16x32_bf16 v[188:191], a[80:83], v[204:207], v[188:191]\n"
"buffer_load_dwordx4 a[140:143], v1, s[24:27], s48 offen offset:3072 sc1\n"
"s_waitcnt vmcnt(14) lgkmcnt(3)\n"
"v_mfma_f32_16x16x32_bf16 v[144:147], a[84:87], v[208:211], v[144:147]\n"
"s_waitcnt vmcnt(13)\n"
"ds_read_b128 v[192:195], v6 offset:32768\n"
"v_mfma_f32_16x16x32_bf16 v[148:151], a[88:91], v[208:211], v[148:151]\n"
"s_waitcnt lgkmcnt(3)\n"
"ds_read_b128 v[196:199], v6 offset:36864\n"
"v_mfma_f32_16x16x32_bf16 v[152:155], a[84:87], v[212:215], v[152:155]\n"
"ds_read_b128 v[200:203], v6 offset:40960\n"
"v_mfma_f32_16x16x32_bf16 v[156:159], a[88:91], v[212:215], v[156:159]\n"
"s_waitcnt lgkmcnt(4)\n"
"ds_read_b128 v[204:207], v6 offset:45056\n"
"s_add_u32 s48, s45, 36864\n"
"buffer_load_dwordx4 a[144:147], v1, s[24:27], s48 offen offset:0 sc1\n"
"v_mfma_f32_16x16x32_bf16 v[160:163], a[84:87], v[216:219], v[160:163]\n"
"v_mfma_f32_16x16x32_bf16 v[164:167], a[88:91], v[216:219], v[164:167]\n"
".Lp0_0_%=:\n"
"s_cbranch_execz .Lp0_1_%=\n"
"s_waitcnt lgkmcnt(4)\n"
"v_mfma_f32_16x16x32_bf16 v[168:171], a[84:87], v[220:223], v[168:171]\n"
"v_mfma_f32_16x16x32_bf16 v[172:175], a[88:91], v[220:223], v[172:175]\n"
"buffer_load_dwordx4 a[148:151], v1, s[24:27], s48 offen offset:1024 sc1\n"
"s_waitcnt vmcnt(14) lgkmcnt(3)\n"
"v_mfma_f32_16x16x32_bf16 v[144:147], a[92:95], v[192:195], v[144:147]\n"
"s_waitcnt vmcnt(13)\n"
"ds_read_b128 v[208:211], v7 offset:32768\n"
"v_mfma_f32_16x16x32_bf16 v[148:151], a[96:99], v[192:195], v[148:151]\n"
"s_waitcnt lgkmcnt(3)\n"
"ds_read_b128 v[212:215], v7 offset:36864\n"
"v_mfma_f32_16x16x32_bf16 v[152:155], a[92:95], v[196:199], v[152:155]\n"
"ds_read_b128 v[216:219], v7 offset:40960\n"
"buffer_load_dwordx4 a[152:155], v1, s[24:27], s48 offen offset:2048 sc1\n"
"v_mfma_f32_16x16x32_bf16 v[156:159], a[96:99], v[196:199], v[156:159]\n"
"s_waitcnt lgkmcnt(4)\n"
"ds_read_b128 v[220:223], v7 offset:45056\n"
"v_mfma_f32_16x16x32_bf16 v[160:163], a[92:95], v[200:203], v[160:163]\n"
"v_mfma_f32_16x16x32_bf16 v[164:167], a[96:99], v[200:203], v[164:167]\n"
"s_waitcnt lgkmcnt(4)\n"
"v_mfma_f32_16x16x32_bf16 v[168:171], a[92:95], v[204:207], v[168:171]\n"
"buffer_load_dwordx4 a[156:159], v1, s[24:27], s48 offen offset:3072 sc1\n"
"v_mfma_f32_16x16x32_bf16 v[172:175], a[96:99], v[204:207], v[172:175]\n"
"s_waitcnt vmcnt(14) lgkmcnt(3)\n"
"v_mfma_f32_16x16x32_bf16 v[144:147], a[100:103], v[208:211], v[144:147]\n"
"s_waitcnt vmcnt(13)\n"
"ds_read_b128 v[192:195], v4 offset:49152\n"
"v_mfma_f32_16x16x32_bf16 v[148:151], a[104:107], v[208:211], v[148:151]\n"
"s_waitcnt lgkmcnt(3)\n"
"ds_read_b128 v[196:199], v4 offset:53248\n"
"v_mfma_f32_16x16x32_bf16 v[152:155], a[100:103], v[212:215], v[152:155]\n"
"ds_read_b128 v[200:203], v4 offset:57344\n"
"s_add_u32 s48, s45, 40960\n"
"buffer_load_dwordx4 a[160:163], v1, s[24:27], s48 offen offset:0 sc1\n"
"v_mfma_f32_16x16x32_bf16 v[156:159], a[104:107], v[212:215], v[156:159]\n"
"s_waitcnt lgkmcnt(4)\n"
"ds_read_b128 v[204:207], v4 offset:61440\n"
"v_mfma_f32_16x16x32_bf16 v[160:163], a[100:103], v[216:219], v[160:163]\n"
"v_mfma_f32_16x16x32_bf16 v[164:167], a[104:107], v[216:219], v[164:167]\n"
"s_waitcnt lgkmcnt(4)\n"
"v_mfma_f32_16x16x32_bf16 v[168:171], a[100:103], v[220:223], v[168:171]\n"
"buffer_load_dwordx4 a[164:167], v1, s[24:27], s48 offen offset:1024 sc1\n"
"v_mfma_f32_16x16x32_bf16 v[172:175], a[104:107], v[220:223], v[172:175]\n"
"s_waitcnt vmcnt(14) lgkmcnt(3)\n"
"v_mfma_f32_16x16x32_bf16 v[144:147], a[108:111], v[192:195], v[144:147]\n"
"s_waitcnt vmcnt(13)\n"
"ds_read_b128 v[208:211], v5 offset:49152\n"
"v_mfma_f32_16x16x32_bf16 v[148:151], a[112:115], v[192:195], v[148:151]\n"
"s_waitcnt vmcnt(12)\n"
"ds_read_b128 v[212:215], v5 offset:53248\n"
"v_mfma_f32_16x16x32_bf16 v[176:179], a[116:119], v[192:195], v[176:179]\n"
"s_waitcnt lgkmcnt(4)\n"
"ds_read_b128 v[216:219], v5 offset:57344\n"
"buffer_load_dwordx4 a[168:171], v1, s[24:27], s48 offen offset:2048 sc1\n"
"v_mfma_f32_16x16x32_bf16 v[152:155], a[108:111], v[196:199], v[152:155]\n"
"ds_read_b128 v[220:223], v5 offset:61440\n"
"v_mfma_f32_16x16x32_bf16 v[156:159], a[112:115], v[196:199], v[156:159]\n"
"v_mfma_f32_16x16x32_bf16 v[180:183], a[116:119], v[196:199], v[180:183]\n"
"s_waitcnt lgkmcnt(5)\n"
"buffer_load_dwordx4 a[172:175], v1, s[24:27], s48 offen offset:3072 sc1\n"
"v_mfma_f32_16x16x32_bf16 v[160:163], a[108:111], v[200:203], v[160:163]\n"
"v_mfma_f32_16x16x32_bf16 v[164:167], a[112:115], v[200:203], v[164:167]\n"
"v_mfma_f32_16x16x32_bf16 v[184:187], a[116:119], v[200:203], v[184:187]\n"
"s_waitcnt lgkmcnt(4)\n"
"v_mfma_f32_16x16x32_bf16 v[168:171], a[108:111], v[204:207], v[168:171]\n"
"s_add_u32 s48, s45, 45056\n"
"buffer_load_dwordx4 a[176:179], v1, s[24:27], s48 offen offset:0 sc1\n"
"v_mfma_f32_16x16x32_bf16 v[172:175], a[112:115], v[204:207], v[172:175]\n"
"v_mfma_f32_16x16x32_bf16 v[188:191], a[116:119], v[204:207], v[188:191]\n"
"s_waitcnt vmcnt(14) lgkmcnt(3)\n"
"v_mfma_f32_16x16x32_bf16 v[144:147], a[120:123], v[208:211], v[144:147]\n"
"s_waitcnt vmcnt(13)\n"
"ds_read_b128 v[192:195], v6 offset:49152\n"
"v_mfma_f32_16x16x32_bf16 v[148:151], a[124:127], v[208:211], v[148:151]\n"
"s_waitcnt lgkmcnt(3)\n"
"ds_read_b128 v[196:199], v6 offset:53248\n"
"buffer_load_dwordx4 a[180:183], v1, s[24:27], s48 offen offset:1024 sc1\n"
"v_mfma_f32_16x16x32_bf16 v[152:155], a[120:123], v[212:215], v[152:155]\n"
"ds_read_b128 v[200:203], v6 offset:57344\n"
"v_mfma_f32_16x16x32_bf16 v[156:159], a[124:127], v[212:215], v[156:159]\n"
"s_waitcnt lgkmcnt(4)\n"
"ds_read_b128 v[204:207], v6 offset:61440\n"
"v_mfma_f32_16x16x32_bf16 v[160:163], a[120:123], v[216:219], v[160:163]\n"
"v_mfma_f32_16x16x32_bf16 v[164:167], a[124:127], v[216:219], v[164:167]\n"
"s_waitcnt lgkmcnt(4)\n"
"buffer_load_dwordx4 a[184:187], v1, s[24:27], s48 offen offset:2048 sc1\n"
"v_mfma_f32_16x16x32_bf16 v[168:171], a[120:123], v[220:223], v[168:171]\n"
"v_mfma_f32_16x16x32_bf16 v[172:175], a[124:127], v[220:223], v[172:175]\n"
"s_waitcnt vmcnt(14) lgkmcnt(3)\n"
"v_mfma_f32_16x16x32_bf16 v[144:147], a[128:131], v[192:195], v[144:147]\n"
"s_waitcnt vmcnt(13)\n"
"ds_read_b128 v[208:211], v7 offset:49152\n"
"v_mfma_f32_16x16x32_bf16 v[148:151], a[132:135], v[192:195], v[148:151]\n"
"s_waitcnt lgkmcnt(3)\n"
"ds_read_b128 v[212:215], v7 offset:53248\n"
"buffer_load_dwordx4 a[188:191], v1, s[24:27], s48 offen offset:3072 sc1\n"
"v_mfma_f32_16x16x32_bf16 v[152:155], a[128:131], v[196:199], v[152:155]\n"
"ds_read_b128 v[216:219], v7 offset:57344\n"
"v_mfma_f32_16x16x32_bf16 v[156:159], a[132:135], v[196:199], v[156:159]\n"
"s_waitcnt lgkmcnt(4)\n"
"ds_read_b128 v[220:223], v7 offset:61440\n"
"v_mfma_f32_16x16x32_bf16 v[160:163], a[128:131], v[200:203], v[160:163]\n"
"s_add_u32 s48, s45, 49152\n"
"buffer_load_dwordx4 a[192:195], v1, s[24:27], s48 offen offset:0 sc1\n"
"v_mfma_f32_16x16x32_bf16 v[164:167], a[132:135], v[200:203], v[164:167]\n"
"s_waitcnt lgkmcnt(4)\n"
"v_mfma_f32_16x16x32_bf16 v[168:171], a[128:131], v[204:207], v[168:171]\n"
"v_mfma_f32_16x16x32_bf16 v[172:175], a[132:135], v[204:207], v[172:175]\n"
"s_waitcnt lgkmcnt(0)\n"
"s_barrier\n"
"s_waitcnt vmcnt(14)\n"
"v_mfma_f32_16x16x32_bf16 v[144:147], a[136:139], v[208:211], v[144:147]\n"
"s_waitcnt vmcnt(13)\n"
"ds_read_b128 v[192:195], v8 offset:0\n"
"buffer_load_dwordx4 a[196:199], v1, s[24:27], s48 offen offset:1024 sc1\n"
"v_mfma_f32_16x16x32_bf16 v[148:151], a[140:143], v[208:211], v[148:151]\n"
"ds_read_b128 v[196:199], v8 offset:4096\n"
"v_mfma_f32_16x16x32_bf16 v[152:155], a[136:139], v[212:215], v[152:155]\n"
"ds_read_b128 v[200:203], v8 offset:8192\n"
"v_mfma_f32_16x16x32_bf16 v[156:159], a[140:143], v[212:215], v[156:159]\n"
"ds_read_b128 v[204:207], v8 offset:12288\n"
"v_mfma_f32_16x16x32_bf16 v[160:163], a[136:139], v[216:219], v[160:163]\n"
"buffer_load_dwordx4 a[200:203], v1, s[24:27], s48 offen offset:2048 sc1\n"
"v_mfma_f32_16x16x32_bf16 v[164:167], a[140:143], v[216:219], v[164:167]\n"
"v_mfma_f32_16x16x32_bf16 v[168:171], a[136:139], v[220:223], v[168:171]\n"
"v_mfma_f32_16x16x32_bf16 v[172:175], a[140:143], v[220:223], v[172:175]\n"
"s_waitcnt vmcnt(14) lgkmcnt(3)\n"
"v_mfma_f32_16x16x32_bf16 v[144:147], a[144:147], v[192:195], v[144:147]\n"
"s_waitcnt vmcnt(13)\n"
"ds_read_b128 v[208:211], v9 offset:0\n"
"buffer_load_dwordx4 a[204:207], v1, s[24:27], s48 offen offset:3072 sc1\n"
"v_mfma_f32_16x16x32_bf16 v[148:151], a[148:151], v[192:195], v[148:151]\n"
"s_waitcnt vmcnt(13)\n"
"ds_read_b128 v[212:215], v9 offset:4096\n"
"v_mfma_f32_16x16x32_bf16 v[176:179], a[152:155], v[192:195], v[176:179]\n"
"s_waitcnt lgkmcnt(4)\n"
"ds_read_b128 v[216:219], v9 offset:8192\n"
"v_mfma_f32_16x16x32_bf16 v[152:155], a[144:147], v[196:199], v[152:155]\n"
"ds_read_b128 v[220:223], v9 offset:12288\n"
"v_mfma_f32_16x16x32_bf16 v[156:159], a[148:151], v[196:199], v[156:159]\n"
"s_add_u32 s48, s45, 53248\n"
"buffer_load_dwordx4 a[208:211], v1, s[24:27], s48 offen offset:0 sc1\n"
"v_mfma_f32_16x16x32_bf16 v[180:183], a[152:155], v[196:199], v[180:183]\n"
"s_waitcnt lgkmcnt(5)\n"
"v_mfma_f32_16x16x32_bf16 v[160:163], a[144:147], v[200:203], v[160:163]\n"
"v_mfma_f32_16x16x32_bf16 v[164:167], a[148:151], v[200:203], v[164:167]\n"
"buffer_load_dwordx4 a[212:215], v1, s[24:27], s48 offen offset:1024 sc1\n"
"v_mfma_f32_16x16x32_bf16 v[184:187], a[152:155], v[200:203], v[184:187]\n"
"s_waitcnt lgkmcnt(4)\n"
"v_mfma_f32_16x16x32_bf16 v[168:171], a[144:147], v[204:207], v[168:171]\n"
"v_mfma_f32_16x16x32_bf16 v[172:175], a[148:151], v[204:207], v[172:175]\n"
"v_mfma_f32_16x16x32_bf16 v[188:191], a[152:155], v[204:207], v[188:191]\n"
"buffer_load_dwordx4 a[216:219], v1, s[24:27], s48 offen offset:2048 sc1\n"
"s_waitcnt vmcnt(15) lgkmcnt(3)\n"
"v_mfma_f32_16x16x32_bf16 v[144:147], a[156:159], v[208:211], v[144:147]\n"
"s_waitcnt vmcnt(14)\n"
"ds_read_b128 v[192:195], v10 offset:0\n"
"v_mfma_f32_16x16x32_bf16 v[148:151], a[160:163], v[208:211], v[148:151]\n"
"s_waitcnt lgkmcnt(3)\n"
"ds_read_b128 v[196:199], v10 offset:4096\n"
"v_mfma_f32_16x16x32_bf16 v[152:155], a[156:159], v[212:215], v[152:155]\n"
"ds_read_b128 v[200:203], v10 offset:8192\n"
"v_mfma_f32_16x16x32_bf16 v[156:159], a[160:163], v[212:215], v[156:159]\n"
"s_waitcnt lgkmcnt(4)\n"
"ds_read_b128 v[204:207], v10 offset:12288\n"
"buffer_load_dwordx4 a[220:223], v1, s[24:27], s48 offen offset:3072 sc1\n"
"v_mfma_f32_16x16x32_bf16 v[160:163], a[156:159], v[216:219], v[160:163]\n"
"v_mfma_f32_16x16x32_bf16 v[164:167], a[160:163], v[216:219], v[164:167]\n"
"s_waitcnt lgkmcnt(4)\n"
"v_mfma_f32_16x16x32_bf16 v[168:171], a[156:159], v[220:223], v[168:171]\n"
"v_mfma_f32_16x16x32_bf16 v[172:175], a[160:163], v[220:223], v[172:175]\n"
"s_add_u32 s48, s45, 57344\n"
"buffer_load_dwordx4 a[224:227], v1, s[24:27], s48 offen offset:0 sc1\n"
"s_waitcnt vmcnt(15) lgkmcnt(3)\n"
"v_mfma_f32_16x16x32_bf16 v[144:147], a[164:167], v[192:195], v[144:147]\n"
"s_waitcnt vmcnt(14)\n"
"ds_read_b128 v[208:211], v11 offset:0\n"
"v_mfma_f32_16x16x32_bf16 v[148:151], a[168:171], v[192:195], v[148:151]\n"
"s_waitcnt lgkmcnt(3)\n"
"ds_read_b128 v[212:215], v11 offset:4096\n"
"v_mfma_f32_16x16x32_bf16 v[152:155], a[164:167], v[196:199], v[152:155]\n"
"ds_read_b128 v[216:219], v11 offset:8192\n"
"v_mfma_f32_16x16x32_bf16 v[156:159], a[168:171], v[196:199], v[156:159]\n"
"s_waitcnt lgkmcnt(4)\n"
"ds_read_b128 v[220:223], v11 offset:12288\n"
"buffer_load_dwordx4 a[228:231], v1, s[24:27], s48 offen offset:1024 sc1\n"
"v_mfma_f32_16x16x32_bf16 v[160:163], a[164:167], v[200:203], v[160:163]\n"
"v_mfma_f32_16x16x32_bf16 v[164:167], a[168:171], v[200:203], v[164:167]\n"
"s_waitcnt lgkmcnt(4)\n"
"v_mfma_f32_16x16x32_bf16 v[168:171], a[164:167], v[204:207], v[168:171]\n"
"buffer_load_dwordx4 a[232:235], v1, s[24:27], s48 offen offset:2048 sc1\n"
"v_mfma_f32_16x16x32_bf16 v[172:175], a[168:171], v[204:207], v[172:175]\n"
"s_waitcnt vmcnt(15) lgkmcnt(3)\n"
"v_mfma_f32_16x16x32_bf16 v[144:147], a[172:175], v[208:211], v[144:147]\n"
"s_waitcnt vmcnt(14)\n"
"ds_read_b128 v[192:195], v8 offset:16384\n"
"v_mfma_f32_16x16x32_bf16 v[148:151], a[176:179], v[208:211], v[148:151]\n"
"s_waitcnt lgkmcnt(3)\n"
"ds_read_b128 v[196:199], v8 offset:20480\n"
"v_mfma_f32_16x16x32_bf16 v[152:155], a[172:175], v[212:215], v[152:155]\n"
"ds_read_b128 v[200:203], v8 offset:24576\n"
"buffer_load_dwordx4 a[236:239], v1, s[24:27], s48 offen offset:3072 sc1\n"
"v_mfma_f32_16x16x32_bf16 v[156:159], a[176:179], v[212:215], v[156:159]\n"
"s_waitcnt lgkmcnt(4)\n"
"ds_read_b128 v[204:207], v8 offset:28672\n"
"v_mfma_f32_16x16x32_bf16 v[160:163], a[172:175], v[216:219], v[160:163]\n"
"v_mfma_f32_16x16x32_bf16 v[164:167], a[176:179], v[216:219], v[164:167]\n"
"s_waitcnt lgkmcnt(4)\n"
"v_mfma_f32_16x16x32_bf16 v[168:171], a[172:175], v[220:223], v[168:171]\n"
"s_add_u32 s48, s45, 61440\n"
"buffer_load_dwordx4 a[240:243], v1, s[24:27], s48 offen offset:0 sc1\n"
"v_mfma_f32_16x16x32_bf16 v[172:175], a[176:179], v[220:223], v[172:175]\n"
"s_waitcnt vmcnt(15) lgkmcnt(3)\n"
"v_mfma_f32_16x16x32_bf16 v[144:147], a[180:183], v[192:195], v[144:147]\n"
"s_waitcnt vmcnt(14)\n"
"ds_read_b128 v[208:211], v9 offset:16384\n"
"v_mfma_f32_16x16x32_bf16 v[148:151], a[184:187], v[192:195], v[148:151]\n"
"s_waitcnt vmcnt(13)\n"
"ds_read_b128 v[212:215], v9 offset:20480\n"
"v_mfma_f32_16x16x32_bf16 v[176:179], a[188:191], v[192:195], v[176:179]\n"
"s_waitcnt lgkmcnt(4)\n"
"ds_read_b128 v[216:219], v9 offset:24576\n"
"buffer_load_dwordx4 a[244:247], v1, s[24:27], s48 offen offset:1024 sc1\n"
"v_mfma_f32_16x16x32_bf16 v[152:155], a[180:183], v[196:199], v[152:155]\n"
"ds_read_b128 v[220:223], v9 offset:28672\n"
"v_mfma_f32_16x16x32_bf16 v[156:159], a[184:187], v[196:199], v[156:159]\n"
"v_mfma_f32_16x16x32_bf16 v[180:183], a[188:191], v[196:199], v[180:183]\n"
"s_waitcnt lgkmcnt(5)\n"
"v_mfma_f32_16x16x32_bf16 v[160:163], a[180:183], v[200:203], v[160:163]\n"
"s_add_u32 m0, s46, 0\n"
"s_xor_b32 s47, s44, 131072\n"
"buffer_load_dwordx4 v58, s[36:39], s47 offen  lds\n"
"v_mfma_f32_16x16x32_bf16 v[164:167], a[184:187], v[200:203], v[164:167]\n"
"v_mfma_f32_16x16x32_bf16 v[184:187], a[188:191], v[200:203], v[184:187]\n"
"s_waitcnt lgkmcnt(4)\n"
"v_mfma_f32_16x16x32_bf16 v[168:171], a[180:183], v[204:207], v[168:171]\n"
"buffer_load_dwordx4 a[248:251], v1, s[24:27], s48 offen offset:2048 sc1\n"
"v_mfma_f32_16x16x32_bf16 v[172:175], a[184:187], v[204:207], v[172:175]\n"
"v_mfma_f32_16x16x32_bf16 v[188:191], a[188:191], v[204:207], v[188:191]\n"
"s_waitcnt vmcnt(15) lgkmcnt(3)\n"
"v_mfma_f32_16x16x32_bf16 v[144:147], a[192:195], v[208:211], v[144:147]\n"
"s_waitcnt vmcnt(14)\n"
"ds_read_b128 v[192:195], v10 offset:16384\n"
"v_mfma_f32_16x16x32_bf16 v[148:151], a[196:199], v[208:211], v[148:151]\n"
"s_waitcnt lgkmcnt(3)\n"
"ds_read_b128 v[196:199], v10 offset:20480\n"
"buffer_load_dwordx4 a[252:255], v1, s[24:27], s48 offen offset:3072 sc1\n"
"v_mfma_f32_16x16x32_bf16 v[152:155], a[192:195], v[212:215], v[152:155]\n"
"ds_read_b128 v[200:203], v10 offset:24576\n"
"v_mfma_f32_16x16x32_bf16 v[156:159], a[196:199], v[212:215], v[156:159]\n"
"s_waitcnt lgkmcnt(4)\n"
"ds_read_b128 v[204:207], v10 offset:28672\n"
"v_mfma_f32_16x16x32_bf16 v[160:163], a[192:195], v[216:219], v[160:163]\n"
"v_mfma_f32_16x16x32_bf16 v[164:167], a[196:199], v[216:219], v[164:167]\n"
"s_waitcnt lgkmcnt(4)\n"
"s_add_u32 m0, s46, 1024\n"
"s_xor_b32 s47, s44, 147456\n"
"buffer_load_dwordx4 v59, s[36:39], s47 offen  lds\n"
"v_mfma_f32_16x16x32_bf16 v[168:171], a[192:195], v[220:223], v[168:171]\n"
"v_mfma_f32_16x16x32_bf16 v[172:175], a[196:199], v[220:223], v[172:175]\n"
"s_waitcnt vmcnt(15) lgkmcnt(3)\n"
"v_mfma_f32_16x16x32_bf16 v[144:147], a[200:203], v[192:195], v[144:147]\n"
"s_waitcnt vmcnt(14)\n"
"ds_read_b128 v[208:211], v11 offset:16384\n"
"v_mfma_f32_16x16x32_bf16 v[148:151], a[204:207], v[192:195], v[148:151]\n"
"s_waitcnt lgkmcnt(3)\n"
"ds_read_b128 v[212:215], v11 offset:20480\n"
"s_add_u32 s48, s45, 65536\n"
"buffer_load_dwordx4 v[224:227], v1, s[24:27], s48 offen offset:0 sc1\n"
"v_mfma_f32_16x16x32_bf16 v[152:155], a[200:203], v[196:199], v[152:155]\n"
"ds_read_b128 v[216:219], v11 offset:24576\n"
"v_mfma_f32_16x16x32_bf16 v[156:159], a[204:207], v[196:199], v[156:159]\n"
"s_waitcnt lgkmcnt(4)\n"
"ds_read_b128 v[220:223], v11 offset:28672\n"
"v_mfma_f32_16x16x32_bf16 v[160:163], a[200:203], v[200:203], v[160:163]\n"
"v_mfma_f32_16x16x32_bf16 v[164:167], a[204:207], v[200:203], v[164:167]\n"
"s_waitcnt lgkmcnt(4)\n"
"buffer_load_dwordx4 v[228:231], v1, s[24:27], s48 offen offset:1024 sc1\n"
"v_mfma_f32_16x16x32_bf16 v[168:171], a[200:203], v[204:207], v[168:171]\n"
"v_mfma_f32_16x16x32_bf16 v[172:175], a[204:207], v[204:207], v[172:175]\n"
"s_waitcnt lgkmcnt(0)\n"
"s_barrier\n"
"s_waitcnt vmcnt(15)\n"
"v_mfma_f32_16x16x32_bf16 v[144:147], a[208:211], v[208:211], v[144:147]\n"
"s_waitcnt vmcnt(14)\n"
"ds_read_b128 v[192:195], v8 offset:32768\n"
"s_add_u32 m0, s46, 8192\n"
"s_xor_b32 s47, s44, 131328\n"
"buffer_load_dwordx4 v58, s[36:39], s47 offen  lds\n"
"v_mfma_f32_16x16x32_bf16 v[148:151], a[212:215], v[208:211], v[148:151]\n"
"ds_read_b128 v[196:199], v8 offset:36864\n"
"v_mfma_f32_16x16x32_bf16 v[152:155], a[208:211], v[212:215], v[152:155]\n"
"ds_read_b128 v[200:203], v8 offset:40960\n"
"v_mfma_f32_16x16x32_bf16 v[156:159], a[212:215], v[212:215], v[156:159]\n"
"ds_read_b128 v[204:207], v8 offset:45056\n"
"v_mfma_f32_16x16x32_bf16 v[160:163], a[208:211], v[216:219], v[160:163]\n"
"buffer_load_dwordx4 v[232:235], v1, s[24:27], s48 offen offset:2048 sc1\n"
"v_mfma_f32_16x16x32_bf16 v[164:167], a[212:215], v[216:219], v[164:167]\n"
"v_mfma_f32_16x16x32_bf16 v[168:171], a[208:211], v[220:223], v[168:171]\n"
"v_mfma_f32_16x16x32_bf16 v[172:175], a[212:215], v[220:223], v[172:175]\n"
"s_waitcnt vmcnt(15) lgkmcnt(3)\n"
"v_mfma_f32_16x16x32_bf16 v[144:147], a[216:219], v[192:195], v[144:147]\n"
"s_waitcnt vmcnt(14)\n"
"ds_read_b128 v[208:211], v9 offset:32768\n"
"buffer_load_dwordx4 v[236:239], v1, s[24:27], s48 offen offset:3072 sc1\n"
"v_mfma_f32_16x16x32_bf16 v[148:151], a[220:223], v[192:195], v[148:151]\n"
"s_waitcnt vmcnt(14)\n"
"ds_read_b128 v[212:215], v9 offset:36864\n"
"v_mfma_f32_16x16x32_bf16 v[176:179], a[224:227], v[192:195], v[176:179]\n"
"s_waitcnt lgkmcnt(4)\n"
"ds_read_b128 v[216:219], v9 offset:40960\n"
"v_mfma_f32_16x16x32_bf16 v[152:155], a[216:219], v[196:199], v[152:155]\n"
"ds_read_b128 v[220:223], v9 offset:45056\n"
"v_mfma_f32_16x16x32_bf16 v[156:159], a[220:223], v[196:199], v[156:159]\n"
"s_add_u32 m0, s46, 9216\n"
"s_xor_b32 s47, s44, 147712\n"
"buffer_load_dwordx4 v59, s[36:39], s47 offen  lds\n"
"v_mfma_f32_16x16x32_bf16 v[180:183], a[224:227], v[196:199], v[180:183]\n"
"s_waitcnt lgkmcnt(5)\n"
"v_mfma_f32_16x16x32_bf16 v[160:163], a[216:219], v[200:203], v[160:163]\n"
"v_mfma_f32_16x16x32_bf16 v[164:167], a[220:223], v[200:203], v[164:167]\n"
"v_mfma_f32_16x16x32_bf16 v[184:187], a[224:227], v[200:203], v[184:187]\n"
"s_waitcnt lgkmcnt(4)\n"
"s_add_u32 s48, s45, 69632\n"
"buffer_load_dwordx4 v[240:243], v1, s[24:27], s48 offen offset:0 sc1\n"
"v_mfma_f32_16x16x32_bf16 v[168:171], a[216:219], v[204:207], v[168:171]\n"
"v_mfma_f32_16x16x32_bf16 v[172:175], a[220:223], v[204:207], v[172:175]\n"
"v_mfma_f32_16x16x32_bf16 v[188:191], a[224:227], v[204:207], v[188:191]\n"
"buffer_load_dwordx4 v[244:247], v1, s[24:27], s48 offen offset:1024 sc1\n"
"s_waitcnt vmcnt(16) lgkmcnt(3)\n"
"v_mfma_f32_16x16x32_bf16 v[144:147], a[228:231], v[208:211], v[144:147]\n"
"s_waitcnt vmcnt(15)\n"
"ds_read_b128 v[192:195], v10 offset:32768\n"
"v_mfma_f32_16x16x32_bf16 v[148:151], a[232:235], v[208:211], v[148:151]\n"
"s_waitcnt lgkmcnt(3)\n"
"ds_read_b128 v[196:199], v10 offset:36864\n"
"v_mfma_f32_16x16x32_bf16 v[152:155], a[228:231], v[212:215], v[152:155]\n"
"ds_read_b128 v[200:203], v10 offset:40960\n"
"v_mfma_f32_16x16x32_bf16 v[156:159], a[232:235], v[212:215], v[156:159]\n"
"s_waitcnt lgkmcnt(4)\n"
"ds_read_b128 v[204:207], v10 offset:45056\n"
"s_add_u32 m0, s46, 16384\n"
"s_xor_b32 s47, s44, 131584\n"
"buffer_load_dwordx4 v58, s[36:39], s47 offen  lds\n"
"v_mfma_f32_16x16x32_bf16 v[160:163], a[228:231], v[216:219], v[160:163]\n"
"v_mfma_f32_16x16x32_bf16 v[164:167], a[232:235], v[216:219], v[164:167]\n"
"s_waitcnt lgkmcnt(4)\n"
"v_mfma_f32_16x16x32_bf16 v[168:171], a[228:231], v[220:223], v[168:171]\n"
"v_mfma_f32_16x16x32_bf16 v[172:175], a[232:235], v[220:223], v[172:175]\n"
"buffer_load_dwordx4 v[248:251], v1, s[24:27], s48 offen offset:2048 sc1\n"
"s_waitcnt vmcnt(16) lgkmcnt(3)\n"
"v_mfma_f32_16x16x32_bf16 v[144:147], a[236:239], v[192:195], v[144:147]\n"
"s_waitcnt vmcnt(15)\n"
"ds_read_b128 v[208:211], v11 offset:32768\n"
"v_mfma_f32_16x16x32_bf16 v[148:151], a[240:243], v[192:195], v[148:151]\n"
"s_waitcnt lgkmcnt(3)\n"
"ds_read_b128 v[212:215], v11 offset:36864\n"
"v_mfma_f32_16x16x32_bf16 v[152:155], a[236:239], v[196:199], v[152:155]\n"
"ds_read_b128 v[216:219], v11 offset:40960\n"
"v_mfma_f32_16x16x32_bf16 v[156:159], a[240:243], v[196:199], v[156:159]\n"
"s_waitcnt lgkmcnt(4)\n"
"ds_read_b128 v[220:223], v11 offset:45056\n"
"buffer_load_dwordx4 v[252:255], v1, s[24:27], s48 offen offset:3072 sc1\n"
"v_mfma_f32_16x16x32_bf16 v[160:163], a[236:239], v[200:203], v[160:163]\n"
"v_mfma_f32_16x16x32_bf16 v[164:167], a[240:243], v[200:203], v[164:167]\n"
"s_waitcnt lgkmcnt(4)\n"
"v_mfma_f32_16x16x32_bf16 v[168:171], a[236:239], v[204:207], v[168:171]\n"
"v_mfma_f32_16x16x32_bf16 v[172:175], a[240:243], v[204:207], v[172:175]\n"
"s_waitcnt vmcnt(15) lgkmcnt(3)\n"
"v_mfma_f32_16x16x32_bf16 v[144:147], a[244:247], v[208:211], v[144:147]\n"
"s_waitcnt vmcnt(13)\n"
"ds_read_b128 v[192:195], v8 offset:49152\n"
"s_add_u32 m0, s46, 17408\n"
"s_xor_b32 s47, s44, 147968\n"
"buffer_load_dwordx4 v59, s[36:39], s47 offen  lds\n"
"v_mfma_f32_16x16x32_bf16 v[148:151], a[248:251], v[208:211], v[148:151]\n"
"s_waitcnt lgkmcnt(3)\n"
"ds_read_b128 v[196:199], v8 offset:53248\n"
"v_mfma_f32_16x16x32_bf16 v[152:155], a[244:247], v[212:215], v[152:155]\n"
"ds_read_b128 v[200:203], v8 offset:57344\n"
"v_mfma_f32_16x16x32_bf16 v[156:159], a[248:251], v[212:215], v[156:159]\n"
"s_waitcnt lgkmcnt(4)\n"
"ds_read_b128 v[204:207], v8 offset:61440\n"
"v_mfma_f32_16x16x32_bf16 v[160:163], a[244:247], v[216:219], v[160:163]\n"
"v_mfma_f32_16x16x32_bf16 v[164:167], a[248:251], v[216:219], v[164:167]\n"
"s_waitcnt lgkmcnt(4)\n"
"s_add_u32 m0, s46, 24576\n"
"s_xor_b32 s47, s44, 131840\n"
"buffer_load_dwordx4 v58, s[36:39], s47 offen  lds\n"
"v_mfma_f32_16x16x32_bf16 v[168:171], a[244:247], v[220:223], v[168:171]\n"
"v_mfma_f32_16x16x32_bf16 v[172:175], a[248:251], v[220:223], v[172:175]\n"
"s_waitcnt vmcnt(14) lgkmcnt(3)\n"
"v_mfma_f32_16x16x32_bf16 v[144:147], a[252:255], v[192:195], v[144:147]\n"
"s_waitcnt vmcnt(12)\n"
"ds_read_b128 v[208:211], v9 offset:49152\n"
"v_mfma_f32_16x16x32_bf16 v[148:151], v[224:227], v[192:195], v[148:151]\n"
"s_waitcnt vmcnt(11)\n"
"ds_read_b128 v[212:215], v9 offset:53248\n"
"v_mfma_f32_16x16x32_bf16 v[176:179], v[228:231], v[192:195], v[176:179]\n"
"s_waitcnt lgkmcnt(4)\n"
"ds_read_b128 v[216:219], v9 offset:57344\n"
"s_add_u32 m0, s46, 25600\n"
"s_xor_b32 s47, s44, 148224\n"
"buffer_load_dwordx4 v59, s[36:39], s47 offen  lds\n"
"v_mfma_f32_16x16x32_bf16 v[152:155], a[252:255], v[196:199], v[152:155]\n"
"ds_read_b128 v[220:223], v9 offset:61440\n"
"v_mfma_f32_16x16x32_bf16 v[156:159], v[224:227], v[196:199], v[156:159]\n"
"v_mfma_f32_16x16x32_bf16 v[180:183], v[228:231], v[196:199], v[180:183]\n"
"s_waitcnt lgkmcnt(5)\n"
"v_mfma_f32_16x16x32_bf16 v[160:163], a[252:255], v[200:203], v[160:163]\n"
"v_mfma_f32_16x16x32_bf16 v[164:167], v[224:227], v[200:203], v[164:167]\n"
"s_add_u32 m0, s46, 32768\n"
"s_xor_b32 s47, s44, 132096\n"
"buffer_load_dwordx4 v58, s[36:39], s47 offen  lds\n"
"v_mfma_f32_16x16x32_bf16 v[184:187], v[228:231], v[200:203], v[184:187]\n"
"s_waitcnt lgkmcnt(4)\n"
"v_mfma_f32_16x16x32_bf16 v[168:171], a[252:255], v[204:207], v[168:171]\n"
"v_mfma_f32_16x16x32_bf16 v[172:175], v[224:227], v[204:207], v[172:175]\n"
"v_mfma_f32_16x16x32_bf16 v[188:191], v[228:231], v[204:207], v[188:191]\n"
"s_waitcnt vmcnt(11) lgkmcnt(3)\n"
"v_mfma_f32_16x16x32_bf16 v[144:147], v[232:235], v[208:211], v[144:147]\n"
"s_waitcnt vmcnt(10)\n"
"ds_read_b128 v[192:195], v10 offset:49152\n"
"s_add_u32 m0, s46, 33792\n"
"s_xor_b32 s47, s44, 148480\n"
"buffer_load_dwordx4 v59, s[36:39], s47 offen  lds\n"
"v_mfma_f32_16x16x32_bf16 v[148:151], v[236:239], v[208:211], v[148:151]\n"
"s_waitcnt lgkmcnt(3)\n"
"ds_read_b128 v[196:199], v10 offset:53248\n"
"v_mfma_f32_16x16x32_bf16 v[152:155], v[232:235], v[212:215], v[152:155]\n"
"ds_read_b128 v[200:203], v10 offset:57344\n"
"v_mfma_f32_16x16x32_bf16 v[156:159], v[236:239], v[212:215], v[156:159]\n"
"s_waitcnt lgkmcnt(4)\n"
"ds_read_b128 v[204:207], v10 offset:61440\n"
"ds_write_b128 v18, v[176:179] offset:0\n"
"v_mfma_f32_16x16x32_bf16 v[160:163], v[232:235], v[216:219], v[160:163]\n"
"ds_write_b128 v18, v[180:183] offset:1024\n"
"ds_write_b128 v18, v[184:187] offset:2048\n"
"v_mfma_f32_16x16x32_bf16 v[164:167], v[236:239], v[216:219], v[164:167]\n"
"s_waitcnt lgkmcnt(7)\n"
"s_add_u32 m0, s46, 40960\n"
"s_xor_b32 s47, s44, 132352\n"
"buffer_load_dwordx4 v58, s[36:39], s47 offen  lds\n"
"ds_write_b128 v18, v[188:191] offset:3072\n"
"v_mfma_f32_16x16x32_bf16 v[168:171], v[232:235], v[220:223], v[168:171]\n"
"v_mfma_f32_16x16x32_bf16 v[172:175], v[236:239], v[220:223], v[172:175]\n"
"s_waitcnt vmcnt(10) lgkmcnt(7)\n"
"v_mfma_f32_16x16x32_bf16 v[144:147], v[240:243], v[192:195], v[144:147]\n"
"s_waitcnt vmcnt(9)\n"
"ds_read_b128 v[208:211], v11 offset:49152\n"
"v_mfma_f32_16x16x32_bf16 v[148:151], v[244:247], v[192:195], v[148:151]\n"
"s_waitcnt lgkmcnt(7)\n"
"ds_read_b128 v[212:215], v11 offset:53248\n"
"v_mfma_f32_16x16x32_bf16 v[152:155], v[240:243], v[196:199], v[152:155]\n"
"ds_read_b128 v[216:219], v11 offset:57344\n"
"s_add_u32 m0, s46, 41984\n"
"s_xor_b32 s47, s44, 148736\n"
"buffer_load_dwordx4 v59, s[36:39], s47 offen  lds\n"
"v_mfma_f32_16x16x32_bf16 v[156:159], v[244:247], v[196:199], v[156:159]\n"
"s_waitcnt lgkmcnt(8)\n"
"ds_read_b128 v[220:223], v11 offset:61440\n"
"v_mfma_f32_16x16x32_bf16 v[160:163], v[240:243], v[200:203], v[160:163]\n"
"v_mfma_f32_16x16x32_bf16 v[164:167], v[244:247], v[200:203], v[164:167]\n"
"s_waitcnt lgkmcnt(8)\n"
"v_mfma_f32_16x16x32_bf16 v[168:171], v[240:243], v[204:207], v[168:171]\n"
"v_mfma_f32_16x16x32_bf16 v[172:175], v[244:247], v[204:207], v[172:175]\n"
"s_add_u32 m0, s46, 49152\n"
"s_xor_b32 s47, s44, 132608\n"
"buffer_load_dwordx4 v58, s[36:39], s47 offen  lds\n"
"s_waitcnt lgkmcnt(0)\n"
"s_barrier\n"
"s_waitcnt vmcnt(9)\n"
"v_mfma_f32_16x16x32_bf16 v[144:147], v[248:251], v[208:211], v[144:147]\n"
"s_waitcnt vmcnt(8)\n"
"ds_read_b128 v[192:195], v4 offset:0\n"
"v_mfma_f32_16x16x32_bf16 v[148:151], v[252:255], v[208:211], v[148:151]\n"
"ds_read_b128 v[196:199], v4 offset:4096\n"
"v_mfma_f32_16x16x32_bf16 v[152:155], v[248:251], v[212:215], v[152:155]\n"
"ds_read_b128 v[200:203], v5 offset:0\n"
"v_mfma_f32_16x16x32_bf16 v[156:159], v[252:255], v[212:215], v[156:159]\n"
"ds_read_b128 v[204:207], v5 offset:4096\n"
"v_mfma_f32_16x16x32_bf16 v[160:163], v[248:251], v[216:219], v[160:163]\n"
"s_add_u32 m0, s46, 50176\n"
"s_xor_b32 s47, s44, 148992\n"
"buffer_load_dwordx4 v59, s[36:39], s47 offen  lds\n"
"v_mfma_f32_16x16x32_bf16 v[164:167], v[252:255], v[216:219], v[164:167]\n"
"v_mfma_f32_16x16x32_bf16 v[168:171], v[248:251], v[220:223], v[168:171]\n"
"v_mfma_f32_16x16x32_bf16 v[172:175], v[252:255], v[220:223], v[172:175]\n"
"s_waitcnt lgkmcnt(0)\n"
"s_barrier\n"
"v_mfma_f32_16x16x32_bf16 v[120:123], a[0:3], v[192:195], 0\n"
"ds_read_b128 v[208:211], v6 offset:0\n"
"v_mfma_f32_16x16x32_bf16 v[124:127], a[4:7], v[192:195], 0\n"
"ds_read_b128 v[212:215], v6 offset:4096\n"
"s_add_u32 m0, s46, 57344\n"
"s_xor_b32 s47, s44, 132864\n"
"buffer_load_dwordx4 v58, s[36:39], s47 offen  lds\n"
"v_mfma_f32_16x16x32_bf16 v[136:139], a[8:11], v[192:195], 0\n"
"v_mfma_f32_16x16x32_bf16 v[128:131], a[0:3], v[196:199], 0\n"
"v_cvt_pk_bf16_f32 v144, v144, v145\n"
"v_mfma_f32_16x16x32_bf16 v[132:135], a[4:7], v[196:199], 0\n"
"v_cvt_pk_bf16_f32 v145, v146, v147\n"
"v_cvt_pk_bf16_f32 v146, v148, v149\n"
"v_mfma_f32_16x16x32_bf16 v[140:143], a[8:11], v[196:199], 0\n"
"v_cvt_pk_bf16_f32 v147, v150, v151\n"
"s_xor_b32 s33, s72, 2\n"
"s_mul_i32 s33, s33, 3\n"
"s_add_u32 s33, s33, 2\n"
"s_mul_i32 s33, s33, 9216\n"
"s_add_u32 s33, s33, s53\n"
"s_sub_u32 s67, s33, 9216\n"
"buffer_store_dwordx4 v[144:147], v19, s[24:27], s67 offen offset:0 sc1\n"
"v_mfma_f32_16x16x32_bf16 v[120:123], a[12:15], v[200:203], v[120:123]\n"
"ds_read_b128 v[216:219], v7 offset:0\n"
"s_add_u32 m0, s46, 58368\n"
"s_xor_b32 s47, s44, 149248\n"
"buffer_load_dwordx4 v59, s[36:39], s47 offen  lds\n"
"v_mfma_f32_16x16x32_bf16 v[124:127], a[16:19], v[200:203], v[124:127]\n"
"ds_read_b128 v[220:223], v7 offset:4096\n"
"v_cvt_pk_bf16_f32 v152, v152, v153\n"
"v_mfma_f32_16x16x32_bf16 v[128:131], a[12:15], v[204:207], v[128:131]\n"
"v_cvt_pk_bf16_f32 v153, v154, v155\n"
"v_cvt_pk_bf16_f32 v154, v156, v157\n"
"v_mfma_f32_16x16x32_bf16 v[132:135], a[16:19], v[204:207], v[132:135]\n"
"v_cvt_pk_bf16_f32 v155, v158, v159\n"
"buffer_store_dwordx4 v[152:155], v19, s[24:27], s67 offen offset:1024 sc1\n"
"s_waitcnt lgkmcnt(3)\n"
"v_mfma_f32_16x16x32_bf16 v[120:123], a[20:23], v[208:211], v[120:123]\n"
"ds_read_b128 v[192:195], v4 offset:8192\n"
"v_cvt_pk_bf16_f32 v160, v160, v161\n"
"v_mfma_f32_16x16x32_bf16 v[124:127], a[24:27], v[208:211], v[124:127]\n"
"s_waitcnt lgkmcnt(3)\n"
"ds_read_b128 v[196:199], v4 offset:12288\n"
"s_add_u32 m0, s46, 65536\n"
"s_xor_b32 s47, s44, 0\n"
"buffer_load_dwordx4 v58, s[36:39], s47 offen  lds\n"
"v_mfma_f32_16x16x32_bf16 v[128:131], a[20:23], v[212:215], v[128:131]\n"
"v_cvt_pk_bf16_f32 v161, v162, v163\n"
"v_cvt_pk_bf16_f32 v162, v164, v165\n"
"v_mfma_f32_16x16x32_bf16 v[132:135], a[24:27], v[212:215], v[132:135]\n"
"v_cvt_pk_bf16_f32 v163, v166, v167\n"
"s_xor_b32 s33, s72, 3\n"
"s_mul_i32 s33, s33, 3\n"
"s_add_u32 s33, s33, 3\n"
"s_mul_i32 s33, s33, 9216\n"
"s_add_u32 s33, s33, s53\n"
"s_sub_u32 s67, s33, 9216\n"
"buffer_store_dwordx4 v[160:163], v19, s[24:27], s67 offen offset:0 sc1\n"
"s_waitcnt lgkmcnt(3)\n"
"v_mfma_f32_16x16x32_bf16 v[120:123], a[28:31], v[216:219], v[120:123]\n"
"ds_read_b128 v[200:203], v5 offset:8192\n"
"v_cvt_pk_bf16_f32 v168, v168, v169\n"
"v_mfma_f32_16x16x32_bf16 v[124:127], a[32:35], v[216:219], v[124:127]\n"
"s_waitcnt lgkmcnt(3)\n"
"ds_read_b128 v[204:207], v5 offset:12288\n"
"v_cvt_pk_bf16_f32 v169, v170, v171\n"
"v_mfma_f32_16x16x32_bf16 v[128:131], a[28:31], v[220:223], v[128:131]\n"
"s_add_u32 m0, s46, 66560\n"
"s_xor_b32 s47, s44, 16384\n"
"buffer_load_dwordx4 v59, s[36:39], s47 offen  lds\n"
"v_cvt_pk_bf16_f32 v170, v172, v173\n"
"v_mfma_f32_16x16x32_bf16 v[132:135], a[32:35], v[220:223], v[132:135]\n"
"v_cvt_pk_bf16_f32 v171, v174, v175\n"
"buffer_store_dwordx4 v[168:171], v19, s[24:27], s67 offen offset:1024 sc1\n"
"s_waitcnt lgkmcnt(3)\n"
"v_mfma_f32_16x16x32_bf16 v[120:123], a[36:39], v[192:195], v[120:123]\n"
"ds_read_b128 v[208:211], v6 offset:8192\n"
"v_mfma_f32_16x16x32_bf16 v[124:127], a[40:43], v[192:195], v[124:127]\n"
"ds_read_b128 v[212:215], v6 offset:12288\n"
"ds_read_b128 v[88:91], v20 offset:0\n"
"v_mfma_f32_16x16x32_bf16 v[136:139], a[44:47], v[192:195], v[136:139]\n"
"s_waitcnt lgkmcnt(5)\n"
"ds_read_b128 v[92:95], v20 offset:1024\n"
"ds_read_b128 v[96:99], v20 offset:4096\n"
"v_mfma_f32_16x16x32_bf16 v[128:131], a[36:39], v[196:199], v[128:131]\n"
"s_add_u32 m0, s46, 73728\n"
"s_xor_b32 s47, s44, 256\n"
"buffer_load_dwordx4 v58, s[36:39], s47 offen  lds\n"
"ds_read_b128 v[100:103], v20 offset:5120\n"
"v_mfma_f32_16x16x32_bf16 v[132:135], a[40:43], v[196:199], v[132:135]\n"
".Lp1_0_%=:\n"
"s_cbranch_execz .Lp1_1_%=\n"
"ds_read_b128 v[104:107], v20 offset:8192\n"
"ds_read_b128 v[108:111], v20 offset:9216\n"
"v_mfma_f32_16x16x32_bf16 v[140:143], a[44:47], v[196:199], v[140:143]\n"
"ds_read_b128 v[112:115], v20 offset:12288\n"
"ds_read_b128 v[116:119], v20 offset:13312\n"
"s_waitcnt lgkmcnt(11)\n"
"v_mfma_f32_16x16x32_bf16 v[120:123], a[48:51], v[200:203], v[120:123]\n"
"ds_read_b128 v[216:219], v7 offset:8192\n"
"v_add_u32 v20, 131072, v1\n"
"v_mfma_f32_16x16x32_bf16 v[124:127], a[52:55], v[200:203], v[124:127]\n"
"s_waitcnt lgkmcnt(11)\n"
"ds_read_b128 v[220:223], v7 offset:12288\n"
"v_mfma_f32_16x16x32_bf16 v[128:131], a[48:51], v[204:207], v[128:131]\n"
"s_add_u32 m0, s46, 74752\n"
"s_xor_b32 s47, s44, 16640\n"
"buffer_load_dwordx4 v59, s[36:39], s47 offen  lds\n"
"v_mfma_f32_16x16x32_bf16 v[132:135], a[52:55], v[204:207], v[132:135]\n"
"s_waitcnt vmcnt(16)\n"
"s_barrier\n"
"s_waitcnt lgkmcnt(11)\n"
"v_mfma_f32_16x16x32_bf16 v[120:123], a[56:59], v[208:211], v[120:123]\n"
"ds_read_b128 v[192:195], v4 offset:16384\n"
"s_waitcnt lgkmcnt(8)\n"
"v_add_f32 v88, v88, v96\n"
"v_mfma_f32_16x16x32_bf16 v[124:127], a[60:63], v[208:211], v[124:127]\n"
"ds_read_b128 v[196:199], v4 offset:20480\n"
"v_add_f32 v89, v89, v97\n"
"v_mfma_f32_16x16x32_bf16 v[128:131], a[56:59], v[212:215], v[128:131]\n"
"v_add_f32 v90, v90, v98\n"
"v_add_f32 v91, v91, v99\n"
"v_mfma_f32_16x16x32_bf16 v[132:135], a[60:63], v[212:215], v[132:135]\n"
"s_add_u32 m0, s46, 81920\n"
"s_xor_b32 s47, s44, 512\n"
"buffer_load_dwordx4 v58, s[36:39], s47 offen  lds\n"
"s_waitcnt lgkmcnt(8)\n"
"v_add_f32 v92, v92, v100\n"
"s_waitcnt lgkmcnt(3)\n"
"v_mfma_f32_16x16x32_bf16 v[120:123], a[64:67], v[216:219], v[120:123]\n"
"ds_read_b128 v[200:203], v5 offset:16384\n"
"v_add_f32 v93, v93, v101\n"
"v_mfma_f32_16x16x32_bf16 v[124:127], a[68:71], v[216:219], v[124:127]\n"
"s_waitcnt lgkmcnt(3)\n"
"ds_read_b128 v[204:207], v5 offset:20480\n"
"v_add_f32 v94, v94, v102\n"
"v_mfma_f32_16x16x32_bf16 v[128:131], a[64:67], v[220:223], v[128:131]\n"
"v_add_f32 v95, v95, v103\n"
"v_add_f32 v104, v104, v112\n"
"v_mfma_f32_16x16x32_bf16 v[132:135], a[68:71], v[220:223], v[132:135]\n"
"v_add_f32 v105, v105, v113\n"
"v_add_f32 v106, v106, v114\n"
"s_waitcnt lgkmcnt(3)\n"
"v_mfma_f32_16x16x32_bf16 v[120:123], a[72:75], v[192:195], v[120:123]\n"
"ds_read_b128 v[208:211], v6 offset:16384\n"
"s_add_u32 m0, s46, 82944\n"
"s_xor_b32 s47, s44, 16896\n"
"buffer_load_dwordx4 v59, s[36:39], s47 offen  lds\n"
"v_mfma_f32_16x16x32_bf16 v[124:127], a[76:79], v[192:195], v[124:127]\n"
"ds_read_b128 v[212:215], v6 offset:20480\n"
"v_add_f32 v107, v107, v115\n"
"v_mfma_f32_16x16x32_bf16 v[136:139], a[80:83], v[192:195], v[136:139]\n"
"s_waitcnt lgkmcnt(4)\n"
"v_add_f32 v108, v108, v116\n"
"v_add_f32 v109, v109, v117\n"
"v_mfma_f32_16x16x32_bf16 v[128:131], a[72:75], v[196:199], v[128:131]\n"
"v_add_f32 v110, v110, v118\n"
"v_add_f32 v111, v111, v119\n"
"v_mfma_f32_16x16x32_bf16 v[132:135], a[76:79], v[196:199], v[132:135]\n"
"v_add_f32 v88, v88, v104\n"
"v_add_f32 v89, v89, v105\n"
"v_mfma_f32_16x16x32_bf16 v[140:143], a[80:83], v[196:199], v[140:143]\n"
"s_add_u32 m0, s46, 90112\n"
"s_xor_b32 s47, s44, 768\n"
"buffer_load_dwordx4 v58, s[36:39], s47 offen  lds\n"
"v_add_f32 v90, v90, v106\n"
"s_waitcnt lgkmcnt(3)\n"
"v_mfma_f32_16x16x32_bf16 v[120:123], a[84:87], v[200:203], v[120:123]\n"
"ds_read_b128 v[216:219], v7 offset:16384\n"
"v_add_f32 v91, v91, v107\n"
"v_mfma_f32_16x16x32_bf16 v[124:127], a[88:91], v[200:203], v[124:127]\n"
"s_waitcnt lgkmcnt(3)\n"
"ds_read_b128 v[220:223], v7 offset:20480\n"
"v_add_f32 v92, v92, v108\n"
"v_mfma_f32_16x16x32_bf16 v[128:131], a[84:87], v[204:207], v[128:131]\n"
"v_add_f32 v93, v93, v109\n"
"v_add_f32 v94, v94, v110\n"
"v_mfma_f32_16x16x32_bf16 v[132:135], a[88:91], v[204:207], v[132:135]\n"
"v_add_f32 v95, v95, v111\n"
"v_cvt_pk_bf16_f32 v96, v88, v89\n"
"s_waitcnt lgkmcnt(3)\n"
"v_mfma_f32_16x16x32_bf16 v[120:123], a[92:95], v[208:211], v[120:123]\n"
"ds_read_b128 v[192:195], v4 offset:24576\n"
"s_add_u32 m0, s46, 91136\n"
"s_xor_b32 s47, s44, 17152\n"
"buffer_load_dwordx4 v59, s[36:39], s47 offen  lds\n"
"v_mfma_f32_16x16x32_bf16 v[124:127], a[96:99], v[208:211], v[124:127]\n"
"s_waitcnt lgkmcnt(3)\n"
"ds_read_b128 v[196:199], v4 offset:28672\n"
"v_cvt_pk_bf16_f32 v97, v90, v91\n"
"v_mfma_f32_16x16x32_bf16 v[128:131], a[92:95], v[212:215], v[128:131]\n"
"v_cvt_pk_bf16_f32 v98, v92, v93\n"
"v_cvt_pk_bf16_f32 v99, v94, v95\n"
"v_mfma_f32_16x16x32_bf16 v[132:135], a[96:99], v[212:215], v[132:135]\n"
"s_cmp_lt_u32 s28, 2\n"
"s_cbranch_scc0 49f\n"
"s_add_u32 s34, s58, 2\n"
"s_xor_b32 s33, s72, s34\n"
"s_mul_i32 s33, s33, 3\n"
"s_add_u32 s33, s33, s34\n"
"s_mul_i32 s33, s33, 9216\n"
"s_add_u32 s33, s33, s53\n"
"s_sub_u32 s33, s33, 1024\n"
"buffer_store_dwordx4 v[96:99], v1, s[24:27], s33 offen sc1\n"
"49:\n"
"s_waitcnt lgkmcnt(3)\n"
"v_mfma_f32_16x16x32_bf16 v[120:123], a[100:103], v[216:219], v[120:123]\n"
"ds_read_b128 v[200:203], v5 offset:24576\n"
"v_mfma_f32_16x16x32_bf16 v[124:127], a[104:107], v[216:219], v[124:127]\n"
"s_waitcnt lgkmcnt(3)\n"
"ds_read_b128 v[204:207], v5 offset:28672\n"
"s_add_u32 m0, s46, 98304\n"
"s_xor_b32 s47, s44, 1024\n"
"buffer_load_dwordx4 v58, s[36:39], s47 offen  lds\n"
"v_mfma_f32_16x16x32_bf16 v[128:131], a[100:103], v[220:223], v[128:131]\n"
"v_mfma_f32_16x16x32_bf16 v[132:135], a[104:107], v[220:223], v[132:135]\n"
"s_waitcnt lgkmcnt(3)\n"
"v_mfma_f32_16x16x32_bf16 v[120:123], a[108:111], v[192:195], v[120:123]\n"
"ds_read_b128 v[208:211], v6 offset:24576\n"
"v_mfma_f32_16x16x32_bf16 v[124:127], a[112:115], v[192:195], v[124:127]\n"
"ds_read_b128 v[212:215], v6 offset:28672\n"
"v_mfma_f32_16x16x32_bf16 v[136:139], a[116:119], v[192:195], v[136:139]\n"
"s_waitcnt lgkmcnt(4)\n"
"s_add_u32 m0, s46, 99328\n"
"s_xor_b32 s47, s44, 17408\n"
"buffer_load_dwordx4 v59, s[36:39], s47 offen  lds\n"
"v_mfma_f32_16x16x32_bf16 v[128:131], a[108:111], v[196:199], v[128:131]\n"
"v_mfma_f32_16x16x32_bf16 v[132:135], a[112:115], v[196:199], v[132:135]\n"
"v_mfma_f32_16x16x32_bf16 v[140:143], a[116:119], v[196:199], v[140:143]\n"
"s_waitcnt lgkmcnt(3)\n"
"v_mfma_f32_16x16x32_bf16 v[120:123], a[120:123], v[200:203], v[120:123]\n"
"ds_read_b128 v[216:219], v7 offset:24576\n"
"v_mfma_f32_16x16x32_bf16 v[124:127], a[124:127], v[200:203], v[124:127]\n"
"s_waitcnt lgkmcnt(3)\n"
"ds_read_b128 v[220:223], v7 offset:28672\n"
"s_add_u32 m0, s46, 106496\n"
"s_xor_b32 s47, s44, 1280\n"
"buffer_load_dwordx4 v58, s[36:39], s47 offen  lds\n"
"v_mfma_f32_16x16x32_bf16 v[128:131], a[120:123], v[204:207], v[128:131]\n"
"v_mfma_f32_16x16x32_bf16 v[132:135], a[124:127], v[204:207], v[132:135]\n"
"s_waitcnt vmcnt(19)\n"
"s_barrier\n"
"s_waitcnt lgkmcnt(3)\n"
"v_mfma_f32_16x16x32_bf16 v[120:123], a[128:131], v[208:211], v[120:123]\n"
"ds_read_b128 v[192:195], v4 offset:32768\n"
"v_mfma_f32_16x16x32_bf16 v[124:127], a[132:135], v[208:211], v[124:127]\n"
"s_waitcnt lgkmcnt(3)\n"
"ds_read_b128 v[196:199], v4 offset:36864\n"
"v_mfma_f32_16x16x32_bf16 v[128:131], a[128:131], v[212:215], v[128:131]\n"
"s_add_u32 m0, s46, 107520\n"
"s_xor_b32 s47, s44, 17664\n"
"buffer_load_dwordx4 v59, s[36:39], s47 offen  lds\n"
"v_mfma_f32_16x16x32_bf16 v[132:135], a[132:135], v[212:215], v[132:135]\n"
"s_waitcnt lgkmcnt(3)\n"
"v_mfma_f32_16x16x32_bf16 v[120:123], a[136:139], v[216:219], v[120:123]\n"
"ds_read_b128 v[200:203], v5 offset:32768\n"
"v_mfma_f32_16x16x32_bf16 v[124:127], a[140:143], v[216:219], v[124:127]\n"
"s_waitcnt lgkmcnt(3)\n"
"ds_read_b128 v[204:207], v5 offset:36864\n"
"v_mfma_f32_16x16x32_bf16 v[128:131], a[136:139], v[220:223], v[128:131]\n"
"v_mfma_f32_16x16x32_bf16 v[132:135], a[140:143], v[220:223], v[132:135]\n"
"s_add_u32 m0, s46, 114688\n"
"s_xor_b32 s47, s44, 1536\n"
"buffer_load_dwordx4 v58, s[36:39], s47 offen  lds\n"
"s_waitcnt lgkmcnt(3)\n"
"v_mfma_f32_16x16x32_bf16 v[120:123], a[144:147], v[192:195], v[120:123]\n"
"ds_read_b128 v[208:211], v6 offset:32768\n"
"v_mfma_f32_16x16x32_bf16 v[124:127], a[148:151], v[192:195], v[124:127]\n"
"ds_read_b128 v[212:215], v6 offset:36864\n"
"v_mfma_f32_16x16x32_bf16 v[136:139], a[152:155], v[192:195], v[136:139]\n"
"s_waitcnt lgkmcnt(4)\n"
"v_mfma_f32_16x16x32_bf16 v[128:131], a[144:147], v[196:199], v[128:131]\n"
"v_mfma_f32_16x16x32_bf16 v[132:135], a[148:151], v[196:199], v[132:135]\n"
"s_add_u32 m0, s46, 115712\n"
"s_xor_b32 s47, s44, 17920\n"
"buffer_load_dwordx4 v59, s[36:39], s47 offen  lds\n"
"v_mfma_f32_16x16x32_bf16 v[140:143], a[152:155], v[196:199], v[140:143]\n"
"s_waitcnt lgkmcnt(3)\n"
"v_mfma_f32_16x16x32_bf16 v[120:123], a[156:159], v[200:203], v[120:123]\n"
"ds_read_b128 v[216:219], v7 offset:32768\n"
"v_mfma_f32_16x16x32_bf16 v[124:127], a[160:163], v[200:203], v[124:127]\n"
"s_waitcnt lgkmcnt(3)\n"
"ds_read_b128 v[220:223], v7 offset:36864\n"
"v_mfma_f32_16x16x32_bf16 v[128:131], a[156:159], v[204:207], v[128:131]\n"
"v_mfma_f32_16x16x32_bf16 v[132:135], a[160:163], v[204:207], v[132:135]\n"
"s_add_u32 m0, s46, 122880\n"
"s_xor_b32 s47, s44, 1792\n"
"buffer_load_dwordx4 v58, s[36:39], s47 offen  lds\n"
"s_waitcnt lgkmcnt(3)\n"
"v_mfma_f32_16x16x32_bf16 v[120:123], a[164:167], v[208:211], v[120:123]\n"
"ds_read_b128 v[192:195], v4 offset:40960\n"
"v_mfma_f32_16x16x32_bf16 v[124:127], a[168:171], v[208:211], v[124:127]\n"
"s_waitcnt lgkmcnt(3)\n"
"ds_read_b128 v[196:199], v4 offset:45056\n"
"v_mfma_f32_16x16x32_bf16 v[128:131], a[164:167], v[212:215], v[128:131]\n"
"v_mfma_f32_16x16x32_bf16 v[132:135], a[168:171], v[212:215], v[132:135]\n"
"s_waitcnt lgkmcnt(3)\n"
"v_mfma_f32_16x16x32_bf16 v[120:123], a[172:175], v[216:219], v[120:123]\n"
"ds_read_b128 v[200:203], v5 offset:40960\n"
"s_add_u32 m0, s46, 123904\n"
"s_xor_b32 s47, s44, 18176\n"
"buffer_load_dwordx4 v59, s[36:39], s47 offen  lds\n"
"v_mfma_f32_16x16x32_bf16 v[124:127], a[176:179], v[216:219], v[124:127]\n"
"s_waitcnt lgkmcnt(3)\n"
"ds_read_b128 v[204:207], v5 offset:45056\n"
"v_mfma_f32_16x16x32_bf16 v[128:131], a[172:175], v[220:223], v[128:131]\n"
"v_mfma_f32_16x16x32_bf16 v[132:135], a[176:179], v[220:223], v[132:135]\n"
"s_waitcnt lgkmcnt(3)\n"
"v_mfma_f32_16x16x32_bf16 v[120:123], a[180:183], v[192:195], v[120:123]\n"
"ds_read_b128 v[208:211], v6 offset:40960\n"
"v_mfma_f32_16x16x32_bf16 v[124:127], a[184:187], v[192:195], v[124:127]\n"
"ds_read_b128 v[212:215], v6 offset:45056\n"
"v_mfma_f32_16x16x32_bf16 v[136:139], a[188:191], v[192:195], v[136:139]\n"
"s_waitcnt lgkmcnt(4)\n"
"v_mfma_f32_16x16x32_bf16 v[128:131], a[180:183], v[196:199], v[128:131]\n"
"v_mfma_f32_16x16x32_bf16 v[132:135], a[184:187], v[196:199], v[132:135]\n"
"v_mfma_f32_16x16x32_bf16 v[140:143], a[188:191], v[196:199], v[140:143]\n"
"s_waitcnt lgkmcnt(3)\n"
"v_mfma_f32_16x16x32_bf16 v[120:123], a[192:195], v[200:203], v[120:123]\n"
"ds_read_b128 v[216:219], v7 offset:40960\n"
"v_mfma_f32_16x16x32_bf16 v[124:127], a[196:199], v[200:203], v[124:127]\n"
"s_waitcnt lgkmcnt(3)\n"
"ds_read_b128 v[220:223], v7 offset:45056\n"
"v_mfma_f32_16x16x32_bf16 v[128:131], a[192:195], v[204:207], v[128:131]\n"
"v_mfma_f32_16x16x32_bf16 v[132:135], a[196:199], v[204:207], v[132:135]\n"
"s_waitcnt vmcnt(19)\n"
"s_barrier\n"
"s_waitcnt lgkmcnt(3)\n"
"v_mfma_f32_16x16x32_bf16 v[120:123], a[200:203], v[208:211], v[120:123]\n"
"ds_read_b128 v[192:195], v4 offset:49152\n"
"v_mfma_f32_16x16x32_bf16 v[124:127], a[204:207], v[208:211], v[124:127]\n"
"s_waitcnt lgkmcnt(3)\n"
"ds_read_b128 v[196:199], v4 offset:53248\n"
"v_mfma_f32_16x16x32_bf16 v[128:131], a[200:203], v[212:215], v[128:131]\n"
"v_mfma_f32_16x16x32_bf16 v[132:135], a[204:207], v[212:215], v[132:135]\n"
"s_waitcnt lgkmcnt(3)\n"
"v_mfma_f32_16x16x32_bf16 v[120:123], a[208:211], v[216:219], v[120:123]\n"
"ds_read_b128 v[200:203], v5 offset:49152\n"
"ds_read_b128 v[112:115], v4 offset:57344\n"
"v_mfma_f32_16x16x32_bf16 v[124:127], a[212:215], v[216:219], v[124:127]\n"
"s_waitcnt lgkmcnt(4)\n"
"ds_read_b128 v[204:207], v5 offset:53248\n"
"ds_read_b128 v[116:119], v4 offset:61440\n"
"v_mfma_f32_16x16x32_bf16 v[128:131], a[208:211], v[220:223], v[128:131]\n"
"v_mfma_f32_16x16x32_bf16 v[132:135], a[212:215], v[220:223], v[132:135]\n"
"s_waitcnt lgkmcnt(5)\n"
"v_mfma_f32_16x16x32_bf16 v[120:123], a[216:219], v[192:195], v[120:123]\n"
"ds_read_b128 v[208:211], v6 offset:49152\n"
"v_mfma_f32_16x16x32_bf16 v[124:127], a[220:223], v[192:195], v[124:127]\n"
"ds_read_b128 v[212:215], v6 offset:53248\n"
"v_mfma_f32_16x16x32_bf16 v[136:139], a[224:227], v[192:195], v[136:139]\n"
"s_waitcnt lgkmcnt(6)\n"
"v_mfma_f32_16x16x32_bf16 v[128:131], a[216:219], v[196:199], v[128:131]\n"
"v_mfma_f32_16x16x32_bf16 v[132:135], a[220:223], v[196:199], v[132:135]\n"
"v_mfma_f32_16x16x32_bf16 v[140:143], a[224:227], v[196:199], v[140:143]\n"
"s_waitcnt lgkmcnt(5)\n"
"v_mfma_f32_16x16x32_bf16 v[120:123], a[228:231], v[200:203], v[120:123]\n"
"ds_read_b128 v[216:219], v7 offset:49152\n"
"s_waitcnt lgkmcnt(5)\n"
"v_mfma_f32_16x16x32_bf16 v[136:139], v[228:231], v[112:115], v[136:139]\n"
"v_mfma_f32_16x16x32_bf16 v[124:127], a[232:235], v[200:203], v[124:127]\n"
"s_waitcnt lgkmcnt(4)\n"
"ds_read_b128 v[220:223], v7 offset:53248\n"
"s_waitcnt lgkmcnt(4)\n"
"v_mfma_f32_16x16x32_bf16 v[140:143], v[228:231], v[116:119], v[140:143]\n"
"v_mfma_f32_16x16x32_bf16 v[128:131], a[228:231], v[204:207], v[128:131]\n"
"v_mfma_f32_16x16x32_bf16 v[132:135], a[232:235], v[204:207], v[132:135]\n"
"s_lshl_b32 s34, s28, 3\n"
"s_add_u32 s34, s34, s54\n"
"s_waitcnt vmcnt(8)\n"
"s_mov_b64 exec, 1\n"
"s_xor_b32 s33, s34, 256\n"
"buffer_store_dwordx2 v[30:31], off, s[24:27], s33 offset:32 sc1\n"
"s_xor_b32 s33, s34, 384\n"
"buffer_store_dwordx2 v[30:31], off, s[24:27], s33 offset:64 sc1\n"
"s_mov_b64 exec, -1\n"
"s_waitcnt lgkmcnt(3)\n"
"v_mfma_f32_16x16x32_bf16 v[120:123], a[236:239], v[208:211], v[120:123]\n"
"ds_read_b128 v[192:195], v4 offset:57344\n"
"v_mfma_f32_16x16x32_bf16 v[124:127], a[240:243], v[208:211], v[124:127]\n"
"s_waitcnt lgkmcnt(3)\n"
"ds_read_b128 v[196:199], v4 offset:61440\n"
"v_mfma_f32_16x16x32_bf16 v[128:131], a[236:239], v[212:215], v[128:131]\n"
"ds_write_b128 v18, v[136:139] offset:0\n"
"ds_write_b128 v18, v[140:143] offset:1024\n"
"v_mfma_f32_16x16x32_bf16 v[132:135], a[240:243], v[212:215], v[132:135]\n"
"s_waitcnt lgkmcnt(0)\n"
"s_barrier\n"
"v_mfma_f32_16x16x32_bf16 v[120:123], a[244:247], v[216:219], v[120:123]\n"
"ds_read_b128 v[200:203], v5 offset:57344\n"
"ds_read_b128 v[88:91], v20 offset:0\n"
"v_mfma_f32_16x16x32_bf16 v[124:127], a[248:251], v[216:219], v[124:127]\n"
"ds_read_b128 v[204:207], v5 offset:61440\n"
"ds_read_b128 v[92:95], v20 offset:1024\n"
"v_mfma_f32_16x16x32_bf16 v[128:131], a[244:247], v[220:223], v[128:131]\n"
"ds_read_b128 v[96:99], v20 offset:4096\n"
"ds_read_b128 v[100:103], v20 offset:5120\n"
"v_mfma_f32_16x16x32_bf16 v[132:135], a[248:251], v[220:223], v[132:135]\n"
"ds_read_b128 v[104:107], v20 offset:8192\n"
"ds_read_b128 v[108:111], v20 offset:9216\n"
"v_mfma_f32_16x16x32_bf16 v[120:123], a[252:255], v[192:195], v[120:123]\n"
"ds_read_b128 v[208:211], v6 offset:57344\n"
"ds_read_b128 v[112:115], v20 offset:12288\n"
"v_mfma_f32_16x16x32_bf16 v[124:127], v[224:227], v[192:195], v[124:127]\n"
"ds_read_b128 v[212:215], v6 offset:61440\n"
"ds_read_b128 v[116:119], v20 offset:13312\n"
"v_mfma_f32_16x16x32_bf16 v[128:131], a[252:255], v[196:199], v[128:131]\n"
"v_mfma_f32_16x16x32_bf16 v[132:135], v[224:227], v[196:199], v[132:135]\n"
"s_waitcnt lgkmcnt(11)\n"
"v_mfma_f32_16x16x32_bf16 v[120:123], v[232:235], v[200:203], v[120:123]\n"
"ds_read_b128 v[216:219], v7 offset:57344\n"
"s_waitcnt lgkmcnt(8)\n"
"v_add_f32 v88, v88, v96\n"
"v_mfma_f32_16x16x32_bf16 v[124:127], v[236:239], v[200:203], v[124:127]\n"
"ds_read_b128 v[220:223], v7 offset:61440\n"
"v_add_f32 v89, v89, v97\n"
"v_mfma_f32_16x16x32_bf16 v[128:131], v[232:235], v[204:207], v[128:131]\n"
"v_add_f32 v90, v90, v98\n"
"v_add_f32 v91, v91, v99\n"
"v_mfma_f32_16x16x32_bf16 v[132:135], v[236:239], v[204:207], v[132:135]\n"
"s_waitcnt lgkmcnt(8)\n"
"v_add_f32 v92, v92, v100\n"
"v_add_f32 v93, v93, v101\n"
"s_barrier\n"
"s_waitcnt lgkmcnt(5)\n"
"v_mfma_f32_16x16x32_bf16 v[120:123], v[240:243], v[208:211], v[120:123]\n"
"ds_read_b128 v[192:195], v8 offset:0\n"
"v_add_f32 v94, v94, v102\n"
"v_mfma_f32_16x16x32_bf16 v[124:127], v[244:247], v[208:211], v[124:127]\n"
"s_waitcnt lgkmcnt(4)\n"
"ds_read_b128 v[196:199], v8 offset:4096\n"
"v_add_f32 v95, v95, v103\n"
"v_mfma_f32_16x16x32_bf16 v[128:131], v[240:243], v[212:215], v[128:131]\n"
"v_add_f32 v104, v104, v112\n"
"v_add_f32 v105, v105, v113\n"
"v_mfma_f32_16x16x32_bf16 v[132:135], v[244:247], v[212:215], v[132:135]\n"
"v_add_f32 v106, v106, v114\n"
"v_add_f32 v107, v107, v115\n"
"s_waitcnt lgkmcnt(3)\n"
"v_mfma_f32_16x16x32_bf16 v[120:123], v[248:251], v[216:219], v[120:123]\n"
"ds_read_b128 v[200:203], v9 offset:0\n"
"v_add_f32 v108, v108, v116\n"
"v_mfma_f32_16x16x32_bf16 v[124:127], v[252:255], v[216:219], v[124:127]\n"
"s_waitcnt lgkmcnt(3)\n"
"ds_read_b128 v[204:207], v9 offset:4096\n"
"v_add_f32 v109, v109, v117\n"
"v_mfma_f32_16x16x32_bf16 v[128:131], v[248:251], v[220:223], v[128:131]\n"
"v_add_f32 v110, v110, v118\n"
"v_add_f32 v111, v111, v119\n"
"v_mfma_f32_16x16x32_bf16 v[132:135], v[252:255], v[220:223], v[132:135]\n"
"v_add_f32 v88, v88, v104\n"
"v_add_f32 v89, v89, v105\n"
"s_waitcnt lgkmcnt(3)\n"
"v_mfma_f32_16x16x32_bf16 v[32:35], a[0:3], v[192:195], v[32:35]\n"
"ds_read_b128 v[208:211], v10 offset:0\n"
"v_mfma_f32_16x16x32_bf16 v[36:39], a[4:7], v[192:195], v[36:39]\n"
"ds_read_b128 v[212:215], v10 offset:4096\n"
"v_mfma_f32_16x16x32_bf16 v[48:51], a[8:11], v[192:195], v[48:51]\n"
"s_waitcnt lgkmcnt(4)\n"
"v_mfma_f32_16x16x32_bf16 v[40:43], a[0:3], v[196:199], v[40:43]\n"
"v_cvt_pk_bf16_f32 v120, v120, v121\n"
"v_cvt_pk_bf16_f32 v121, v122, v123\n"
"v_mfma_f32_16x16x32_bf16 v[44:47], a[4:7], v[196:199], v[44:47]\n"
"s_add_u32 s93, s62, 0\n"
"buffer_load_dwordx4 v[60:63], v1, s[24:27], s93 offen offset:0 sc1\n"
"v_cvt_pk_bf16_f32 v122, v124, v125\n"
"v_mfma_f32_16x16x32_bf16 v[52:55], a[8:11], v[196:199], v[52:55]\n"
"v_cvt_pk_bf16_f32 v123, v126, v127\n"
"s_xor_b32 s33, s72, 1\n"
"s_mul_i32 s33, s33, 3\n"
"s_add_u32 s33, s33, 1\n"
"s_mul_i32 s33, s33, 9216\n"
"s_add_u32 s33, s33, s53\n"
"s_sub_u32 s67, s33, 9216\n"
"buffer_store_dwordx4 v[120:123], v19, s[24:27], s67 offen offset:0 sc1\n"
"s_waitcnt lgkmcnt(3)\n"
"v_mfma_f32_16x16x32_bf16 v[32:35], a[12:15], v[200:203], v[32:35]\n"
"ds_read_b128 v[216:219], v11 offset:0\n"
"v_cvt_pk_bf16_f32 v128, v128, v129\n"
"v_mfma_f32_16x16x32_bf16 v[36:39], a[16:19], v[200:203], v[36:39]\n"
"s_waitcnt lgkmcnt(3)\n"
"ds_read_b128 v[220:223], v11 offset:4096\n"
"v_cvt_pk_bf16_f32 v129, v130, v131\n"
"v_mfma_f32_16x16x32_bf16 v[40:43], a[12:15], v[204:207], v[40:43]\n"
"v_cvt_pk_bf16_f32 v130, v132, v133\n"
"v_cvt_pk_bf16_f32 v131, v134, v135\n"
"v_mfma_f32_16x16x32_bf16 v[44:47], a[16:19], v[204:207], v[44:47]\n"
"buffer_load_dwordx4 v[64:67], v1, s[24:27], s93 offen offset:1024 sc1\n"
"buffer_store_dwordx4 v[128:131], v19, s[24:27], s67 offen offset:1024 sc1\n"
"s_waitcnt lgkmcnt(3)\n"
"v_mfma_f32_16x16x32_bf16 v[32:35], a[20:23], v[208:211], v[32:35]\n"
"ds_read_b128 v[192:195], v8 offset:8192\n"
"v_mfma_f32_16x16x32_bf16 v[36:39], a[24:27], v[208:211], v[36:39]\n"
"s_waitcnt lgkmcnt(3)\n"
"ds_read_b128 v[196:199], v8 offset:12288\n"
"v_add_f32 v90, v90, v106\n"
"v_mfma_f32_16x16x32_bf16 v[40:43], a[20:23], v[212:215], v[40:43]\n"
"v_add_f32 v91, v91, v107\n"
"v_add_f32 v92, v92, v108\n"
"v_mfma_f32_16x16x32_bf16 v[44:47], a[24:27], v[212:215], v[44:47]\n"
"v_add_f32 v93, v93, v109\n"
"v_add_f32 v94, v94, v110\n"
"v_and_b32 v15, 7, v0\n"
"v_lshlrev_b32 v15, 3, v15\n"
"s_add_u32 s85, s54, 32\n"
"buffer_load_dwordx2 v[16:17], v15, s[24:27], s85 offen sc1\n"
"s_waitcnt lgkmcnt(3)\n"
"v_mfma_f32_16x16x32_bf16 v[32:35], a[28:31], v[216:219], v[32:35]\n"
"ds_read_b128 v[200:203], v9 offset:8192\n"
"buffer_load_dwordx4 v[68:71], v1, s[24:27], s93 offen offset:2048 sc1\n"
"v_mfma_f32_16x16x32_bf16 v[36:39], a[32:35], v[216:219], v[36:39]\n"
"s_waitcnt lgkmcnt(3)\n"
"ds_read_b128 v[204:207], v9 offset:12288\n"
"v_add_f32 v95, v95, v111\n"
"v_mfma_f32_16x16x32_bf16 v[40:43], a[28:31], v[220:223], v[40:43]\n"
"v_cvt_pk_bf16_f32 v96, v88, v89\n"
"v_cvt_pk_bf16_f32 v97, v90, v91\n"
"v_mfma_f32_16x16x32_bf16 v[44:47], a[32:35], v[220:223], v[44:47]\n"
"v_cvt_pk_bf16_f32 v98, v92, v93\n"
"v_cvt_pk_bf16_f32 v99, v94, v95\n"
"s_waitcnt lgkmcnt(3)\n"
"v_mfma_f32_16x16x32_bf16 v[32:35], a[36:39], v[192:195], v[32:35]\n"
"ds_read_b128 v[208:211], v10 offset:8192\n"
"s_cmp_lt_u32 s28, 1\n"
"s_cbranch_scc0 50f\n"
"s_xor_b32 s33, s72, 1\n"
"s_mul_i32 s33, s33, 3\n"
"s_add_u32 s33, s33, 1\n"
"s_mul_i32 s33, s33, 9216\n"
"s_add_u32 s33, s33, s53\n"
"s_sub_u32 s33, s33, 1024\n"
"buffer_store_dwordx4 v[96:99], v1, s[24:27], s33 offen sc1\n"
"50:\n"
"v_mfma_f32_16x16x32_bf16 v[36:39], a[40:43], v[192:195], v[36:39]\n"
"ds_read_b128 v[212:215], v10 offset:12288\n"
"buffer_load_dwordx4 v[72:75], v1, s[24:27], s93 offen offset:3072 sc1\n"
"v_mfma_f32_16x16x32_bf16 v[48:51], a[44:47], v[192:195], v[48:51]\n"
"s_waitcnt lgkmcnt(4)\n"
"v_mfma_f32_16x16x32_bf16 v[40:43], a[36:39], v[196:199], v[40:43]\n"
"v_mfma_f32_16x16x32_bf16 v[44:47], a[40:43], v[196:199], v[44:47]\n"
"v_mfma_f32_16x16x32_bf16 v[52:55], a[44:47], v[196:199], v[52:55]\n"
"s_waitcnt lgkmcnt(3)\n"
"v_mfma_f32_16x16x32_bf16 v[32:35], a[48:51], v[200:203], v[32:35]\n"
"ds_read_b128 v[216:219], v11 offset:8192\n"
"s_add_u32 s93, s62, 4096\n"
"buffer_load_dwordx4 v[76:79], v1, s[24:27], s93 offen offset:0 sc1\n"
"v_mfma_f32_16x16x32_bf16 v[36:39], a[52:55], v[200:203], v[36:39]\n"
"s_waitcnt lgkmcnt(3)\n"
"ds_read_b128 v[220:223], v11 offset:12288\n"
"v_mfma_f32_16x16x32_bf16 v[40:43], a[48:51], v[204:207], v[40:43]\n"
"v_mfma_f32_16x16x32_bf16 v[44:47], a[52:55], v[204:207], v[44:47]\n"
"s_barrier\n"
"s_waitcnt lgkmcnt(3)\n"
"v_mfma_f32_16x16x32_bf16 v[32:35], a[56:59], v[208:211], v[32:35]\n"
"ds_read_b128 v[192:195], v8 offset:16384\n"
"v_mfma_f32_16x16x32_bf16 v[36:39], a[60:63], v[208:211], v[36:39]\n"
"s_waitcnt lgkmcnt(3)\n"
"ds_read_b128 v[196:199], v8 offset:20480\n"
"buffer_load_dwordx4 v[80:83], v1, s[24:27], s93 offen offset:1024 sc1\n"
"v_mfma_f32_16x16x32_bf16 v[40:43], a[56:59], v[212:215], v[40:43]\n"
"v_mfma_f32_16x16x32_bf16 v[44:47], a[60:63], v[212:215], v[44:47]\n"
"s_waitcnt lgkmcnt(3)\n"
"v_mfma_f32_16x16x32_bf16 v[32:35], a[64:67], v[216:219], v[32:35]\n"
"ds_read_b128 v[200:203], v9 offset:16384\n"
"v_mfma_f32_16x16x32_bf16 v[36:39], a[68:71], v[216:219], v[36:39]\n"
"s_waitcnt lgkmcnt(3)\n"
"ds_read_b128 v[204:207], v9 offset:20480\n"
"v_mfma_f32_16x16x32_bf16 v[40:43], a[64:67], v[220:223], v[40:43]\n"
"buffer_load_dwordx4 v[84:87], v1, s[24:27], s93 offen offset:2048 sc1\n"
"v_mfma_f32_16x16x32_bf16 v[44:47], a[68:71], v[220:223], v[44:47]\n"
"s_waitcnt lgkmcnt(3)\n"
"v_mfma_f32_16x16x32_bf16 v[32:35], a[72:75], v[192:195], v[32:35]\n"
"ds_read_b128 v[208:211], v10 offset:16384\n"
"v_mfma_f32_16x16x32_bf16 v[36:39], a[76:79], v[192:195], v[36:39]\n"
"ds_read_b128 v[212:215], v10 offset:20480\n"
"v_mfma_f32_16x16x32_bf16 v[48:51], a[80:83], v[192:195], v[48:51]\n"
"s_waitcnt lgkmcnt(4)\n"
"v_mfma_f32_16x16x32_bf16 v[40:43], a[72:75], v[196:199], v[40:43]\n"
"s_add_u32 s93, s62, 20480\n"
"buffer_load_dwordx4 v[144:147], v1, s[24:27], s93 offen offset:1024 sc1\n"
"v_mfma_f32_16x16x32_bf16 v[44:47], a[76:79], v[196:199], v[44:47]\n"
"v_mfma_f32_16x16x32_bf16 v[52:55], a[80:83], v[196:199], v[52:55]\n"
"s_waitcnt lgkmcnt(3)\n"
"v_mfma_f32_16x16x32_bf16 v[32:35], a[84:87], v[200:203], v[32:35]\n"
"ds_read_b128 v[216:219], v11 offset:16384\n"
"v_mfma_f32_16x16x32_bf16 v[36:39], a[88:91], v[200:203], v[36:39]\n"
"s_waitcnt lgkmcnt(3)\n"
"ds_read_b128 v[220:223], v11 offset:20480\n"
"v_mfma_f32_16x16x32_bf16 v[40:43], a[84:87], v[204:207], v[40:43]\n"
"buffer_load_dwordx4 v[148:151], v1, s[24:27], s93 offen offset:2048 sc1\n"
"v_mfma_f32_16x16x32_bf16 v[44:47], a[88:91], v[204:207], v[44:47]\n"
"s_waitcnt lgkmcnt(3)\n"
"v_mfma_f32_16x16x32_bf16 v[32:35], a[92:95], v[208:211], v[32:35]\n"
"ds_read_b128 v[192:195], v8 offset:24576\n"
"v_mfma_f32_16x16x32_bf16 v[36:39], a[96:99], v[208:211], v[36:39]\n"
"s_waitcnt lgkmcnt(3)\n"
"ds_read_b128 v[196:199], v8 offset:28672\n"
"v_mfma_f32_16x16x32_bf16 v[40:43], a[92:95], v[212:215], v[40:43]\n"
"v_mfma_f32_16x16x32_bf16 v[44:47], a[96:99], v[212:215], v[44:47]\n"
"buffer_load_dwordx4 v[152:155], v1, s[24:27], s93 offen offset:3072 sc1\n"
"s_waitcnt vmcnt(8)\n"
"v_cmp_ne_u64 vcc, s[96:97], v[16:17]\n"
"s_nop 4\n"
"s_cbranch_vccz 51f\n"
"52:\n"
"buffer_load_dwordx2 v[16:17], v15, s[24:27], s85 offen sc1\n"
"s_waitcnt vmcnt(0)\n"
"v_cmp_ne_u64 vcc, s[96:97], v[16:17]\n"
"s_nop 4\n"
"s_cbranch_vccz 51f\n"
"s_sleep 1\n"
"s_sub_u32 s75, s75, 1\n"
"s_cmp_gt_i32 s75, 0\n"
"s_cbranch_scc1 52b\n"
"51:\n"
"s_mul_i32 s33, s72, 27648\n"
"s_add_u32 s33, s33, s53\n"
"s_cmp_eq_u32 s28, 0\n"
"s_cbranch_scc0 53f\n"
"s_add_u32 s34, s33, 17408\n"
"buffer_load_dwordx4 a[20:23], v1, s[24:27], s34 offen sc1\n"
"s_add_u32 s34, s33, 26624\n"
"buffer_load_dwordx4 a[32:35], v1, s[24:27], s34 offen sc1\n"
"53:\n"
"s_add_u32 s34, s33, 9216\n"
"buffer_load_dwordx4 a[12:15], v19, s[24:27], s34 offen offset:0 sc1\n"
"buffer_load_dwordx4 a[16:19], v19, s[24:27], s34 offen offset:1024 sc1\n"
"s_add_u32 s34, s33, 18432\n"
"buffer_load_dwordx4 a[24:27], v19, s[24:27], s34 offen offset:0 sc1\n"
"buffer_load_dwordx4 a[28:31], v19, s[24:27], s34 offen offset:1024 sc1\n"
"s_waitcnt lgkmcnt(3)\n"
"v_mfma_f32_16x16x32_bf16 v[32:35], a[100:103], v[216:219], v[32:35]\n"
"ds_read_b128 v[200:203], v9 offset:24576\n"
"v_mfma_f32_16x16x32_bf16 v[36:39], a[104:107], v[216:219], v[36:39]\n"
"s_waitcnt lgkmcnt(3)\n"
"ds_read_b128 v[204:207], v9 offset:28672\n"
"v_mfma_f32_16x16x32_bf16 v[40:43], a[100:103], v[220:223], v[40:43]\n"
"v_mfma_f32_16x16x32_bf16 v[44:47], a[104:107], v[220:223], v[44:47]\n"
"s_waitcnt lgkmcnt(3)\n"
"v_mfma_f32_16x16x32_bf16 v[32:35], a[108:111], v[192:195], v[32:35]\n"
"ds_read_b128 v[208:211], v10 offset:24576\n"
"s_add_u32 s93, s62, 24576\n"
"buffer_load_dwordx4 v[156:159], v1, s[24:27], s93 offen offset:0 sc1\n"
"v_mfma_f32_16x16x32_bf16 v[36:39], a[112:115], v[192:195], v[36:39]\n"
"ds_read_b128 v[212:215], v10 offset:28672\n"
"v_mfma_f32_16x16x32_bf16 v[48:51], a[116:119], v[192:195], v[48:51]\n"
"s_waitcnt lgkmcnt(4)\n"
"v_mfma_f32_16x16x32_bf16 v[40:43], a[108:111], v[196:199], v[40:43]\n"
"v_mfma_f32_16x16x32_bf16 v[44:47], a[112:115], v[196:199], v[44:47]\n"
"v_mfma_f32_16x16x32_bf16 v[52:55], a[116:119], v[196:199], v[52:55]\n"
"buffer_load_dwordx4 v[160:163], v1, s[24:27], s93 offen offset:1024 sc1\n"
"s_lshl_b32 s34, s28, 3\n"
"s_add_u32 s34, s34, s54\n"
"s_waitcnt vmcnt(13)\n"
"s_mov_b64 exec, 1\n"
"s_xor_b32 s33, s34, 128\n"
"buffer_store_dwordx2 v[30:31], off, s[24:27], s33 offset:0 sc1\n"
"s_mov_b64 exec, -1\n"
"s_waitcnt lgkmcnt(3)\n"
"v_mfma_f32_16x16x32_bf16 v[32:35], a[120:123], v[200:203], v[32:35]\n"
"ds_read_b128 v[216:219], v11 offset:24576\n"
"v_mfma_f32_16x16x32_bf16 v[36:39], a[124:127], v[200:203], v[36:39]\n"
"s_waitcnt lgkmcnt(3)\n"
"ds_read_b128 v[220:223], v11 offset:28672\n"
"v_mfma_f32_16x16x32_bf16 v[40:43], a[120:123], v[204:207], v[40:43]\n"
"v_mfma_f32_16x16x32_bf16 v[44:47], a[124:127], v[204:207], v[44:47]\n"
"s_barrier\n"
"s_waitcnt lgkmcnt(3)\n"
"v_mfma_f32_16x16x32_bf16 v[32:35], a[128:131], v[208:211], v[32:35]\n"
"ds_read_b128 v[192:195], v8 offset:32768\n"
"buffer_load_dwordx4 v[164:167], v1, s[24:27], s93 offen offset:2048 sc1\n"
"v_mfma_f32_16x16x32_bf16 v[36:39], a[132:135], v[208:211], v[36:39]\n"
"s_waitcnt lgkmcnt(3)\n"
"ds_read_b128 v[196:199], v8 offset:36864\n"
"v_mfma_f32_16x16x32_bf16 v[40:43], a[128:131], v[212:215], v[40:43]\n"
"v_mfma_f32_16x16x32_bf16 v[44:47], a[132:135], v[212:215], v[44:47]\n"
"s_waitcnt lgkmcnt(3)\n"
"v_mfma_f32_16x16x32_bf16 v[32:35], a[136:139], v[216:219], v[32:35]\n"
"ds_read_b128 v[200:203], v9 offset:32768\n"
"v_mfma_f32_16x16x32_bf16 v[36:39], a[140:143], v[216:219], v[36:39]\n"
"s_waitcnt lgkmcnt(3)\n"
"ds_read_b128 v[204:207], v9 offset:36864\n"
"buffer_load_dwordx4 v[168:171], v1, s[24:27], s93 offen offset:3072 sc1\n"
"v_mfma_f32_16x16x32_bf16 v[40:43], a[136:139], v[220:223], v[40:43]\n"
"v_mfma_f32_16x16x32_bf16 v[44:47], a[140:143], v[220:223], v[44:47]\n"
"s_waitcnt lgkmcnt(3)\n"
"v_mfma_f32_16x16x32_bf16 v[32:35], a[144:147], v[192:195], v[32:35]\n"
"ds_read_b128 v[208:211], v10 offset:32768\n"
"v_mfma_f32_16x16x32_bf16 v[36:39], a[148:151], v[192:195], v[36:39]\n"
"ds_read_b128 v[212:215], v10 offset:36864\n"
"v_mfma_f32_16x16x32_bf16 v[48:51], a[152:155], v[192:195], v[48:51]\n"
"s_waitcnt lgkmcnt(4)\n"
"s_add_u32 s93, s62, 28672\n"
"buffer_load_dwordx4 v[172:175], v1, s[24:27], s93 offen offset:0 sc1\n"
"v_mfma_f32_16x16x32_bf16 v[40:43], a[144:147], v[196:199], v[40:43]\n"
"v_mfma_f32_16x16x32_bf16 v[44:47], a[148:151], v[196:199], v[44:47]\n"
"v_mfma_f32_16x16x32_bf16 v[52:55], a[152:155], v[196:199], v[52:55]\n"
"s_waitcnt lgkmcnt(3)\n"
"v_mfma_f32_16x16x32_bf16 v[32:35], a[156:159], v[200:203], v[32:35]\n"
"ds_read_b128 v[216:219], v11 offset:32768\n"
"v_mfma_f32_16x16x32_bf16 v[36:39], a[160:163], v[200:203], v[36:39]\n"
"s_waitcnt lgkmcnt(3)\n"
"ds_read_b128 v[220:223], v11 offset:36864\n"
"buffer_load_dwordx4 v[176:179], v1, s[24:27], s93 offen offset:1024 sc1\n"
"v_mfma_f32_16x16x32_bf16 v[40:43], a[156:159], v[204:207], v[40:43]\n"
".Lp2_0_%=:\n"
"s_cbranch_execz .Lp2_1_%=\n"
"v_mfma_f32_16x16x32_bf16 v[44:47], a[160:163], v[204:207], v[44:47]\n"
"s_waitcnt lgkmcnt(3)\n"
"v_mfma_f32_16x16x32_bf16 v[32:35], a[164:167], v[208:211], v[32:35]\n"
"ds_read_b128 v[192:195], v8 offset:40960\n"
"v_mfma_f32_16x16x32_bf16 v[36:39], a[168:171], v[208:211], v[36:39]\n"
"s_waitcnt lgkmcnt(3)\n"
"ds_read_b128 v[196:199], v8 offset:45056\n"
"v_mfma_f32_16x16x32_bf16 v[40:43], a[164:167], v[212:215], v[40:43]\n"
"buffer_load_dwordx4 v[180:183], v1, s[24:27], s93 offen offset:2048 sc1\n"
"v_mfma_f32_16x16x32_bf16 v[44:47], a[168:171], v[212:215], v[44:47]\n"
"s_waitcnt lgkmcnt(3)\n"
"v_mfma_f32_16x16x32_bf16 v[32:35], a[172:175], v[216:219], v[32:35]\n"
"ds_read_b128 v[200:203], v9 offset:40960\n"
"v_mfma_f32_16x16x32_bf16 v[36:39], a[176:179], v[216:219], v[36:39]\n"
"s_waitcnt lgkmcnt(3)\n"
"ds_read_b128 v[204:207], v9 offset:45056\n"
"v_mfma_f32_16x16x32_bf16 v[40:43], a[172:175], v[220:223], v[40:43]\n"
"v_mfma_f32_16x16x32_bf16 v[44:47], a[176:179], v[220:223], v[44:47]\n"
"buffer_load_dwordx4 v[184:187], v1, s[24:27], s93 offen offset:3072 sc1\n"
"v_and_b32 v15, 3, v0\n"
"v_lshlrev_b32 v15, 3, v15\n"
"s_add_u32 s85, s54, 0\n"
"buffer_load_dwordx2 v[16:17], v15, s[24:27], s85 offen sc1\n"
"s_waitcnt lgkmcnt(3)\n"
"v_mfma_f32_16x16x32_bf16 v[32:35], a[180:183], v[192:195], v[32:35]\n"
"ds_read_b128 v[208:211], v10 offset:40960\n"
"s_waitcnt vmcnt(13)\n"
"v_mfma_f32_16x16x32_bf16 v[32:35], v[26:29], a[12:15], v[32:35]\n"
"v_mfma_f32_16x16x32_bf16 v[36:39], a[184:187], v[192:195], v[36:39]\n"
"ds_read_b128 v[212:215], v10 offset:45056\n"
"v_mfma_f32_16x16x32_bf16 v[36:39], v[24:27], a[12:15], v[36:39]\n"
"v_mfma_f32_16x16x32_bf16 v[48:51], a[188:191], v[192:195], v[48:51]\n"
"s_waitcnt lgkmcnt(4)\n"
"s_waitcnt vmcnt(12)\n"
"v_mfma_f32_16x16x32_bf16 v[40:43], v[26:29], a[16:19], v[40:43]\n"
"v_mfma_f32_16x16x32_bf16 v[44:47], v[24:27], a[16:19], v[44:47]\n"
"v_mfma_f32_16x16x32_bf16 v[40:43], a[180:183], v[196:199], v[40:43]\n"
"s_waitcnt vmcnt(11)\n"
"v_mfma_f32_16x16x32_bf16 v[32:35], v[26:29], a[24:27], v[32:35]\n"
"v_mfma_f32_16x16x32_bf16 v[36:39], v[24:27], a[24:27], v[36:39]\n"
"v_mfma_f32_16x16x32_bf16 v[44:47], a[184:187], v[196:199], v[44:47]\n"
"s_add_u32 s93, s62, 32768\n"
"buffer_load_dwordx4 v[188:191], v1, s[24:27], s93 offen offset:0 sc1\n"
"s_waitcnt vmcnt(11)\n"
"v_mfma_f32_16x16x32_bf16 v[40:43], v[26:29], a[28:31], v[40:43]\n"
"v_mfma_f32_16x16x32_bf16 v[52:55], a[188:191], v[196:199], v[52:55]\n"
"v_mfma_f32_16x16x32_bf16 v[44:47], v[24:27], a[28:31], v[44:47]\n"
"s_waitcnt lgkmcnt(3)\n"
"v_mfma_f32_16x16x32_bf16 v[32:35], a[192:195], v[200:203], v[32:35]\n"
"ds_read_b128 v[216:219], v11 offset:40960\n"
"v_mfma_f32_16x16x32_bf16 v[36:39], a[196:199], v[200:203], v[36:39]\n"
"s_waitcnt lgkmcnt(3)\n"
"ds_read_b128 v[220:223], v11 offset:45056\n"
"v_mfma_f32_16x16x32_bf16 v[40:43], a[192:195], v[204:207], v[40:43]\n"
"v_mfma_f32_16x16x32_bf16 v[44:47], a[196:199], v[204:207], v[44:47]\n"
"s_add_u32 s93, s62, 12288\n"
"buffer_load_dwordx4 v[120:123], v1, s[24:27], s93 offen offset:3072 sc1\n"
"s_barrier\n"
"s_waitcnt lgkmcnt(3)\n"
"v_mfma_f32_16x16x32_bf16 v[32:35], a[200:203], v[208:211], v[32:35]\n"
"ds_read_b128 v[192:195], v8 offset:49152\n"
"v_mfma_f32_16x16x32_bf16 v[36:39], a[204:207], v[208:211], v[36:39]\n"
"s_waitcnt lgkmcnt(3)\n"
"ds_read_b128 v[196:199], v8 offset:53248\n"
"v_mfma_f32_16x16x32_bf16 v[40:43], a[200:203], v[212:215], v[40:43]\n"
"v_mfma_f32_16x16x32_bf16 v[44:47], a[204:207], v[212:215], v[44:47]\n"
"s_waitcnt lgkmcnt(3)\n"
"v_mfma_f32_16x16x32_bf16 v[32:35], a[208:211], v[216:219], v[32:35]\n"
"ds_read_b128 v[200:203], v9 offset:49152\n"
"s_add_u32 s93, s62, 16384\n"
"buffer_load_dwordx4 v[124:127], v1, s[24:27], s93 offen offset:0 sc1\n"
"v_mfma_f32_16x16x32_bf16 v[36:39], a[212:215], v[216:219], v[36:39]\n"
"s_waitcnt lgkmcnt(3)\n"
"ds_read_b128 v[204:207], v9 offset:53248\n"
"ds_read_b128 v[112:115], v8 offset:57344\n"
"v_mfma_f32_16x16x32_bf16 v[40:43], a[208:211], v[220:223], v[40:43]\n"
"ds_read_b128 v[116:119], v8 offset:61440\n"
"v_mfma_f32_16x16x32_bf16 v[44:47], a[212:215], v[220:223], v[44:47]\n"
"s_waitcnt lgkmcnt(5)\n"
"v_mfma_f32_16x16x32_bf16 v[32:35], a[216:219], v[192:195], v[32:35]\n"
"ds_read_b128 v[208:211], v10 offset:49152\n"
"v_mfma_f32_16x16x32_bf16 v[36:39], a[220:223], v[192:195], v[36:39]\n"
"ds_read_b128 v[212:215], v10 offset:53248\n"
"buffer_load_dwordx4 v[128:131], v1, s[24:27], s93 offen offset:1024 sc1\n"
"v_mfma_f32_16x16x32_bf16 v[48:51], a[224:227], v[192:195], v[48:51]\n"
"s_waitcnt lgkmcnt(6)\n"
"v_mfma_f32_16x16x32_bf16 v[40:43], a[216:219], v[196:199], v[40:43]\n"
"v_mfma_f32_16x16x32_bf16 v[44:47], a[220:223], v[196:199], v[44:47]\n"
"v_mfma_f32_16x16x32_bf16 v[52:55], a[224:227], v[196:199], v[52:55]\n"
"s_waitcnt lgkmcnt(5)\n"
"v_mfma_f32_16x16x32_bf16 v[32:35], a[228:231], v[200:203], v[32:35]\n"
"ds_read_b128 v[216:219], v11 offset:49152\n"
"buffer_load_dwordx4 v[132:135], v1, s[24:27], s93 offen offset:2048 sc1\n"
"v_mfma_f32_16x16x32_bf16 v[36:39], a[232:235], v[200:203], v[36:39]\n"
"s_waitcnt lgkmcnt(5)\n"
"ds_read_b128 v[220:223], v11 offset:53248\n"
"s_waitcnt lgkmcnt(5)\n"
"v_mfma_f32_16x16x32_bf16 v[48:51], v[228:231], v[112:115], v[48:51]\n"
"v_mfma_f32_16x16x32_bf16 v[40:43], a[228:231], v[204:207], v[40:43]\n"
"s_waitcnt lgkmcnt(4)\n"
"v_mfma_f32_16x16x32_bf16 v[52:55], v[228:231], v[116:119], v[52:55]\n"
"s_cmp_eq_u32 s28, 0\n"
"s_cbranch_scc0 54f\n"
"v_mfma_f32_16x16x32_bf16 v[48:51], v[26:29], a[20:23], v[48:51]\n"
"v_mfma_f32_16x16x32_bf16 v[52:55], v[24:27], a[20:23], v[52:55]\n"
"v_mfma_f32_16x16x32_bf16 v[48:51], v[26:29], a[32:35], v[48:51]\n"
"v_mfma_f32_16x16x32_bf16 v[52:55], v[24:27], a[32:35], v[52:55]\n"
"54:\n"
"v_mfma_f32_16x16x32_bf16 v[44:47], a[232:235], v[204:207], v[44:47]\n"
"s_waitcnt vmcnt(5)\n"
"v_cmp_ne_u64 vcc, s[96:97], v[16:17]\n"
"s_nop 4\n"
"s_cbranch_vccz 55f\n"
"56:\n"
"buffer_load_dwordx2 v[16:17], v15, s[24:27], s85 offen sc1\n"
"s_waitcnt vmcnt(0)\n"
"v_cmp_ne_u64 vcc, s[96:97], v[16:17]\n"
"s_nop 4\n"
"s_cbranch_vccz 55f\n"
"s_sleep 1\n"
"s_sub_u32 s75, s75, 1\n"
"s_cmp_gt_i32 s75, 0\n"
"s_cbranch_scc1 56b\n"
"55:\n"
"s_mul_i32 s33, s72, 27648\n"
"s_add_u32 s33, s33, s53\n"
"s_cmp_eq_u32 s28, 0\n"
"s_cbranch_scc0 57f\n"
"s_add_u32 s34, s33, 8192\n"
"buffer_load_dwordx4 a[44:47], v1, s[24:27], s34 offen sc1\n"
"57:\n"
"s_add_u32 s34, s33, 0\n"
"buffer_load_dwordx4 a[36:39], v19, s[24:27], s34 offen offset:0 sc1\n"
"buffer_load_dwordx4 a[40:43], v19, s[24:27], s34 offen offset:1024 sc1\n"
"s_waitcnt lgkmcnt(3)\n"
"v_mfma_f32_16x16x32_bf16 v[32:35], a[236:239], v[208:211], v[32:35]\n"
"ds_read_b128 v[192:195], v8 offset:57344\n"
"v_mfma_f32_16x16x32_bf16 v[36:39], a[240:243], v[208:211], v[36:39]\n"
"s_waitcnt lgkmcnt(3)\n"
"ds_read_b128 v[196:199], v8 offset:61440\n"
"buffer_load_dwordx4 v[136:139], v1, s[24:27], s93 offen offset:3072 sc1\n"
"v_mfma_f32_16x16x32_bf16 v[40:43], a[236:239], v[212:215], v[40:43]\n"
"ds_write_b128 v18, v[48:51] offset:0\n"
"v_mfma_f32_16x16x32_bf16 v[44:47], a[240:243], v[212:215], v[44:47]\n"
"ds_write_b128 v18, v[52:55] offset:1024\n"
"s_waitcnt lgkmcnt(0)\n"
"s_barrier\n"
"v_mfma_f32_16x16x32_bf16 v[32:35], a[244:247], v[216:219], v[32:35]\n"
"ds_read_b128 v[200:203], v9 offset:57344\n"
"ds_read_b128 v[88:91], v20 offset:0\n"
"v_mfma_f32_16x16x32_bf16 v[36:39], a[248:251], v[216:219], v[36:39]\n"
"ds_read_b128 v[204:207], v9 offset:61440\n"
"ds_read_b128 v[92:95], v20 offset:1024\n"
"v_mfma_f32_16x16x32_bf16 v[40:43], a[244:247], v[220:223], v[40:43]\n"
"s_add_u32 s93, s62, 20480\n"
"buffer_load_dwordx4 v[140:143], v1, s[24:27], s93 offen offset:0 sc1\n"
"ds_read_b128 v[96:99], v20 offset:4096\n"
"v_mfma_f32_16x16x32_bf16 v[44:47], a[248:251], v[220:223], v[44:47]\n"
"ds_read_b128 v[100:103], v20 offset:5120\n"
"ds_read_b128 v[104:107], v20 offset:8192\n"
"v_mfma_f32_16x16x32_bf16 v[32:35], a[252:255], v[192:195], v[32:35]\n"
"ds_read_b128 v[208:211], v10 offset:57344\n"
"ds_read_b128 v[108:111], v20 offset:9216\n"
"v_mfma_f32_16x16x32_bf16 v[36:39], v[224:227], v[192:195], v[36:39]\n"
"ds_read_b128 v[212:215], v10 offset:61440\n"
"ds_read_b128 v[112:115], v20 offset:12288\n"
"v_mfma_f32_16x16x32_bf16 v[40:43], a[252:255], v[196:199], v[40:43]\n"
"ds_read_b128 v[116:119], v20 offset:13312\n"
"v_mfma_f32_16x16x32_bf16 v[44:47], v[224:227], v[196:199], v[44:47]\n"
"s_waitcnt lgkmcnt(11)\n"
"v_mfma_f32_16x16x32_bf16 v[32:35], v[232:235], v[200:203], v[32:35]\n"
"ds_read_b128 v[216:219], v11 offset:57344\n"
"v_mfma_f32_16x16x32_bf16 v[36:39], v[236:239], v[200:203], v[36:39]\n"
"s_waitcnt lgkmcnt(10)\n"
"ds_read_b128 v[220:223], v11 offset:61440\n"
"s_waitcnt lgkmcnt(9)\n"
"v_add_f32 v88, v88, v96\n"
"v_mfma_f32_16x16x32_bf16 v[40:43], v[232:235], v[204:207], v[40:43]\n"
"v_add_f32 v89, v89, v97\n"
"v_add_f32 v90, v90, v98\n"
"v_mfma_f32_16x16x32_bf16 v[44:47], v[236:239], v[204:207], v[44:47]\n"
"v_add_f32 v91, v91, v99\n"
"s_waitcnt lgkmcnt(8)\n"
"v_add_f32 v92, v92, v100\n"
"s_waitcnt lgkmcnt(6)\n"
"v_mfma_f32_16x16x32_bf16 v[32:35], v[240:243], v[208:211], v[32:35]\n"
"v_add_f32 v93, v93, v101\n"
"v_add_f32 v94, v94, v102\n"
"v_mfma_f32_16x16x32_bf16 v[36:39], v[244:247], v[208:211], v[36:39]\n"
"s_waitcnt lgkmcnt(4)\n"
"v_add_f32 v95, v95, v103\n"
"s_waitcnt lgkmcnt(3)\n"
"v_add_f32 v104, v104, v112\n"
"v_mfma_f32_16x16x32_bf16 v[40:43], v[240:243], v[212:215], v[40:43]\n"
"v_add_f32 v105, v105, v113\n"
"v_add_f32 v106, v106, v114\n"
"v_mfma_f32_16x16x32_bf16 v[44:47], v[244:247], v[212:215], v[44:47]\n"
"v_add_f32 v107, v107, v115\n"
"s_waitcnt lgkmcnt(2)\n"
"v_add_f32 v108, v108, v116\n"
"s_waitcnt lgkmcnt(1)\n"
"v_mfma_f32_16x16x32_bf16 v[32:35], v[248:251], v[216:219], v[32:35]\n"
"v_add_f32 v109, v109, v117\n"
"v_add_f32 v110, v110, v118\n"
"v_mfma_f32_16x16x32_bf16 v[36:39], v[252:255], v[216:219], v[36:39]\n"
"s_waitcnt lgkmcnt(0)\n"
"v_add_f32 v111, v111, v119\n"
"v_add_f32 v48, v88, v104\n"
"v_mfma_f32_16x16x32_bf16 v[40:43], v[248:251], v[220:223], v[40:43]\n"
"v_add_f32 v49, v89, v105\n"
"v_add_f32 v50, v90, v106\n"
"v_mfma_f32_16x16x32_bf16 v[44:47], v[252:255], v[220:223], v[44:47]\n"
"v_add_f32 v51, v91, v107\n"
"v_add_f32 v52, v92, v108\n"
"v_add_f32 v53, v93, v109\n"
"v_add_f32 v54, v94, v110\n"
"v_add_f32 v55, v95, v111\n"
"s_waitcnt vmcnt(4)\n"
"v_mfma_f32_16x16x32_bf16 v[48:51], v[26:29], a[44:47], v[48:51]\n"
"v_mfma_f32_16x16x32_bf16 v[52:55], v[24:27], a[44:47], v[52:55]\n"
"s_waitcnt vmcnt(3)\n"
"v_mfma_f32_16x16x32_bf16 v[32:35], v[26:29], a[36:39], v[32:35]\n"
"v_mfma_f32_16x16x32_bf16 v[36:39], v[24:27], a[36:39], v[36:39]\n"
"s_waitcnt vmcnt(2)\n"
"v_mfma_f32_16x16x32_bf16 v[40:43], v[26:29], a[40:43], v[40:43]\n"
"v_mfma_f32_16x16x32_bf16 v[44:47], v[24:27], a[40:43], v[44:47]\n"
"s_nop 0\n"
"v_cvt_pk_bf16_f32 v48, v48, v49\n"
"v_cvt_pk_bf16_f32 v49, v50, v51\n"
"v_cvt_pk_bf16_f32 v50, v52, v53\n"
"v_cvt_pk_bf16_f32 v51, v54, v55\n"
"s_cmp_eq_u32 s28, 0\n"
"s_cbranch_scc0 58f\n"
"buffer_store_dwordx4 v[48:51], v1, s[24:27], s50 offen sc1\n"
"58:\n"
"v_cvt_pk_bf16_f32 v32, v32, v33\n"
"v_cvt_pk_bf16_f32 v33, v34, v35\n"
"v_cvt_pk_bf16_f32 v34, v36, v37\n"
"v_cvt_pk_bf16_f32 v35, v38, v39\n"
"buffer_store_dwordx4 v[32:35], v1, s[24:27], s49 offen offset:0 sc1\n"
"v_cvt_pk_bf16_f32 v40, v40, v41\n"
"v_cvt_pk_bf16_f32 v41, v42, v43\n"
"v_cvt_pk_bf16_f32 v42, v44, v45\n"
"v_cvt_pk_bf16_f32 v43, v46, v47\n"
"buffer_store_dwordx4 v[40:43], v1, s[24:27], s49 offen offset:1024 sc1\n"
"s_add_u32 s93, s62, 4096\n"
"buffer_load_dwordx4 v[88:91], v1, s[24:27], s93 offen offset:3072 sc1\n"
"s_add_u32 s93, s62, 8192\n"
"buffer_load_dwordx4 v[92:95], v1, s[24:27], s93 offen offset:0 sc1\n"
"buffer_load_dwordx4 v[96:99], v1, s[24:27], s93 offen offset:1024 sc1\n"
"buffer_load_dwordx4 v[100:103], v1, s[24:27], s93 offen offset:2048 sc1\n"
"buffer_load_dwordx4 v[104:107], v1, s[24:27], s93 offen offset:3072 sc1\n"
"s_add_u32 s93, s62, 12288\n"
"buffer_load_dwordx4 v[108:111], v1, s[24:27], s93 offen offset:0 sc1\n"
"s_lshl_b32 s35, s30, 12\n"
"s_add_u32 s35, s35, 4096\n"
"s_lshl_b32 s33, s56, 1\n"
"s_lshr_b32 s34, s72, 1\n"
"s_add_u32 s33, s33, s34\n"
"s_lshl_b32 s33, s33, 7\n"
"s_add_u32 s33, s33, s35\n"
"s_and_b32 s34, s72, 1\n"
"s_lshl_b32 s34, s34, 2\n"
"s_add_u32 s34, s34, s57\n"
"s_lshl_b32 s34, s34, 3\n"
"s_add_u32 s85, s33, s34\n"
"v_and_b32 v19, 7, v0\n"
"v_lshlrev_b32 v19, 3, v19\n"
"v_add_u32 v2, 0x10000, v1\n"
"v_add_u32 v3, 0x20000, v1\n"
"s_lshl_b32 s33, s59, 12\n"
"s_lshl_b32 s34, s58, 3\n"
"s_add_u32 s33, s33, s34\n"
"s_add_u32 s33, s33, 73728\n"
"v_add_u32 v4, s33, v1\n"
"v_lshrrev_b32 v5, 4, v0\n"
"v_lshlrev_b32 v5, 4, v5\n"
"s_lshl_b32 s33, s28, 6\n"
"s_add_u32 s33, s33, 161536\n"
"v_add_u32 v5, s33, v5\n"
"s_lshl_b32 s33, s28, 10\n"
"v_add_u32 v6, s33, v1\n"
"v_add_u32 v7, 0x10000, v6\n"
"s_lshl_b32 s63, s30, 5\n"
"s_add_u32 s63, s63, s61\n"
"s_lshl_b32 s63, s63, 3\n"
"s_add_u32 s63, s63, s60\n"
"s_mul_i32 s63, s63, 73728\n"
"s_add_u32 s63, s63, s33\n"
"s_add_u32 s63, s63, 41992192\n"
"s_mul_i32 s34, s60, 73728\n"
"s_sub_u32 s53, s63, s34\n"
"s_lshl_b32 s54, s30, 5\n"
"s_add_u32 s54, s54, s61\n"
"s_mul_i32 s54, s54, 4096\n"
"s_lshl_b32 s34, s28, 7\n"
"s_add_u32 s54, s54, s34\n"
"s_add_u32 s54, s54, 200704\n"
"s_mul_i32 s34, s60, 512\n"
"s_add_u32 s51, s54, s34\n"
"s_lshl_b32 s49, s30, 5\n"
"s_add_u32 s49, s49, s61\n"
"s_lshl2_add_u32 s49, s49, s28\n"
"s_lshl_b32 s49, s49, 6\n"
"s_add_u32 s49, s49, 53248\n"
"s_add_u32 s67, s60, 1\n"
"s_and_b32 s67, s67, 7\n"
"s_lshl_b32 s67, s67, 12\n"
"s_mul_i32 s65, s61, 73728\n"
"s_add_u32 s65, s65, s33\n"
"s_mul_i32 s34, s30, 2359296\n"
"s_add_u32 s65, s65, s34\n"
"s_add_u32 s65, s65, 34914304\n"
"s_lshl_b32 s33, s61, 7\n"
"s_add_u32 s33, s33, s35\n"
"s_waitcnt vmcnt(6)\n"
"s_barrier\n"
"v_mov_b32 v20, s96\n"
"v_mov_b32 v21, s97\n"
"s_cmp_eq_u32 s28, 0\n"
"s_cbranch_scc0 59f\n"
"s_mov_b64 exec, 1\n"
"buffer_store_dwordx2 v[20:21], v19, s[24:27], s85 offen sc1\n"
"s_mov_b64 exec, -1\n"
"59:\n"
"s_cmp_lg_u64 s[94:95], 0\n"
"s_cbranch_scc0 60f\n"
"s_mul_i32 s48, s60, 2304\n"
"s_add_u32 s48, s48, 11206656\n"
"buffer_load_dwordx4 v[224:227], v13, s[24:27], s48 offen sc1\n"
"s_add_u32 s48, s48, 18432\n"
"buffer_load_dwordx4 v[228:231], v13, s[24:27], s48 offen sc1\n"
"s_add_u32 s48, s48, 18432\n"
"buffer_load_dwordx4 v[232:235], v13, s[24:27], s48 offen sc1\n"
"s_add_u32 s48, s48, 18432\n"
"buffer_load_dwordx4 v[236:239], v13, s[24:27], s48 offen sc1\n"
"s_add_u32 s48, s48, 18432\n"
"buffer_load_dwordx4 v[240:243], v13, s[24:27], s48 offen sc1\n"
"s_add_u32 s48, s48, 18432\n"
"buffer_load_dwordx4 v[244:247], v13, s[24:27], s48 offen sc1\n"
"60:\n"
"buffer_load_dwordx4 v[112:115], v1, s[24:27], s93 offen offset:1024 sc1\n"
"buffer_load_dwordx4 v[116:119], v1, s[24:27], s93 offen offset:2048 sc1\n"
"s_add_u32 s93, s62, 32768\n"
"buffer_load_dwordx4 v[192:195], v1, s[24:27], s93 offen offset:1024 sc1\n"
"buffer_load_dwordx4 v[196:199], v1, s[24:27], s93 offen offset:2048 sc1\n"
"buffer_load_dwordx4 v[200:203], v1, s[24:27], s93 offen offset:3072 sc1\n"
"s_add_u32 s93, s62, 36864\n"
"buffer_load_dwordx4 v[204:207], v1, s[24:27], s93 offen offset:0 sc1\n"
"buffer_load_dwordx4 v[208:211], v1, s[24:27], s93 offen offset:1024 sc1\n"
"buffer_load_dwordx4 v[212:215], v1, s[24:27], s93 offen offset:2048 sc1\n"
"buffer_load_dwordx4 v[216:219], v1, s[24:27], s93 offen offset:3072 sc1\n"
"s_add_u32 s93, s62, 40960\n"
"buffer_load_dwordx4 v[220:223], v1, s[24:27], s93 offen offset:0 sc1\n"
"v_mov_b32 v10, 0x3f803f80\n"
"v_mov_b32 v11, 0x3f803f80\n"
"v_mov_b32 v12, 0x3f803f80\n"
"v_mov_b32 v13, 0x3f803f80\n"
"s_cmp_lg_u32 s28, 0\n"
"s_cbranch_scc1 61f\n"
"62:\n"
"buffer_load_dwordx2 v[20:21], v19, s[24:27], s33 offen sc1\n"
"s_waitcnt vmcnt(0)\n"
"v_cmp_ne_u64 vcc, s[96:97], v[20:21]\n"
"s_nop 4\n"
"s_cbranch_vccz 61f\n"
"s_sleep 1\n"
"s_sub_u32 s75, s75, 1\n"
"s_cmp_gt_i32 s75, 0\n"
"s_cbranch_scc1 62b\n"
"61:\n"
"s_barrier\n"
"buffer_load_dwordx4 a[184:187], v1, s[24:27], s65 offen sc1\n"
"s_add_u32 s65, s65, 4096\n"
"buffer_load_dwordx4 a[188:191], v1, s[24:27], s65 offen sc1\n"
"s_add_u32 s65, s65, 4096\n"
"buffer_load_dwordx4 a[192:195], v1, s[24:27], s65 offen sc1\n"
"s_add_u32 s65, s65, 4096\n"
"buffer_load_dwordx4 a[196:199], v1, s[24:27], s65 offen sc1\n"
"s_add_u32 s65, s65, 4096\n"
"buffer_load_dwordx4 a[200:203], v1, s[24:27], s65 offen sc1\n"
"s_add_u32 s65, s65, 4096\n"
"buffer_load_dwordx4 a[204:207], v1, s[24:27], s65 offen sc1\n"
"s_add_u32 s65, s65, 4096\n"
"buffer_load_dwordx4 a[208:211], v1, s[24:27], s65 offen sc1\n"
"s_add_u32 s65, s65, 4096\n"
"buffer_load_dwordx4 a[212:215], v1, s[24:27], s65 offen sc1\n"
"s_cmp_lg_u64 s[94:95], 0\n"
"s_cbranch_scc0 63f\n"
"s_waitcnt vmcnt(18)\n"
"v_add_f32 v224, v224, v228\n"
"v_add_f32 v225, v225, v229\n"
"v_add_f32 v226, v226, v230\n"
"v_add_f32 v227, v227, v231\n"
"v_add_f32 v224, v224, v232\n"
"v_add_f32 v225, v225, v233\n"
"v_add_f32 v226, v226, v234\n"
"v_add_f32 v227, v227, v235\n"
"v_add_f32 v224, v224, v236\n"
"v_add_f32 v225, v225, v237\n"
"v_add_f32 v226, v226, v238\n"
"v_add_f32 v227, v227, v239\n"
"v_add_f32 v224, v224, v240\n"
"v_add_f32 v225, v225, v241\n"
"v_add_f32 v226, v226, v242\n"
"v_add_f32 v227, v227, v243\n"
"v_add_f32 v224, v224, v244\n"
"v_add_f32 v225, v225, v245\n"
"v_add_f32 v226, v226, v246\n"
"v_add_f32 v227, v227, v247\n"
"s_mov_b64 exec, s[94:95]\n"
"ds_write_b128 v14, v[224:227]\n"
"s_mov_b64 exec, -1\n"
"63:\n"
"s_waitcnt vmcnt(7)\n"
"ds_write_b128 v6, a[184:187] offset:0\n"
"v_mfma_f32_16x16x32_bf16 v[44:47], v[10:13], a[184:187], 0\n"
"v_mfma_f32_16x16x32_bf16 v[48:51], a[184:187], a[184:187], 0\n"
"s_add_u32 s65, s65, 4096\n"
"buffer_load_dwordx4 a[216:219], v1, s[24:27], s65 offen sc1\n"
"s_waitcnt vmcnt(7)\n"
"ds_write_b128 v6, a[188:191] offset:4096\n"
"v_mfma_f32_16x16x32_bf16 v[44:47], v[10:13], a[188:191], v[44:47]\n"
"v_mfma_f32_16x16x32_bf16 v[48:51], a[188:191], a[188:191], v[48:51]\n"
"s_add_u32 s65, s65, 4096\n"
"buffer_load_dwordx4 a[220:223], v1, s[24:27], s65 offen sc1\n"
"s_waitcnt vmcnt(7)\n"
"ds_write_b128 v6, a[192:195] offset:8192\n"
"v_mfma_f32_16x16x32_bf16 v[44:47], v[10:13], a[192:195], v[44:47]\n"
"v_mfma_f32_16x16x32_bf16 v[48:51], a[192:195], a[192:195], v[48:51]\n"
"s_add_u32 s65, s65, 4096\n"
"buffer_load_dwordx4 a[224:227], v1, s[24:27], s65 offen sc1\n"
"s_waitcnt vmcnt(7)\n"
"ds_write_b128 v6, a[196:199] offset:12288\n"
"v_mfma_f32_16x16x32_bf16 v[44:47], v[10:13], a[196:199], v[44:47]\n"
"v_mfma_f32_16x16x32_bf16 v[48:51], a[196:199], a[196:199], v[48:51]\n"
"s_add_u32 s65, s65, 4096\n"
"buffer_load_dwordx4 a[228:231], v1, s[24:27], s65 offen sc1\n"
"s_waitcnt vmcnt(7)\n"
"ds_write_b128 v6, a[200:203] offset:16384\n"
"v_mfma_f32_16x16x32_bf16 v[44:47], v[10:13], a[200:203], v[44:47]\n"
"v_mfma_f32_16x16x32_bf16 v[48:51], a[200:203], a[200:203], v[48:51]\n"
"s_add_u32 s65, s65, 4096\n"
"buffer_load_dwordx4 a[232:235], v1, s[24:27], s65 offen sc1\n"
"s_waitcnt vmcnt(7)\n"
"ds_write_b128 v6, a[204:207] offset:20480\n"
"v_mfma_f32_16x16x32_bf16 v[44:47], v[10:13], a[204:207], v[44:47]\n"
"v_mfma_f32_16x16x32_bf16 v[48:51], a[204:207], a[204:207], v[48:51]\n"
"s_add_u32 s65, s65, 4096\n"
"buffer_load_dwordx4 a[236:239], v1, s[24:27], s65 offen sc1\n"
"s_waitcnt lgkmcnt(0)\n"
"s_barrier\n"
"ds_read_b128 a[0:3], v1 offset:0\n"
"ds_read_b128 a[16:19], v1 offset:4096\n"
"ds_read_b128 a[20:23], v1 offset:5120\n"
"ds_read_b128 a[4:7], v1 offset:1024\n"
"ds_read_b128 a[8:11], v1 offset:2048\n"
"ds_read_b128 a[24:27], v1 offset:6144\n"
"ds_read_b128 a[28:31], v1 offset:7168\n"
"ds_read_b128 a[12:15], v1 offset:3072\n"
"ds_read_b128 a[44:47], v1 offset:11264\n"
"ds_read_b128 a[60:63], v1 offset:15360\n"
"ds_read_b128 a[56:59], v1 offset:14336\n"
"ds_read_b128 a[40:43], v1 offset:10240\n"
"s_add_u32 s65, s65, 4096\n"
"buffer_load_dwordx4 a[240:243], v1, s[24:27], s65 offen sc1\n"
"s_add_u32 s65, s65, 4096\n"
"buffer_load_dwordx4 a[244:247], v1, s[24:27], s65 offen sc1\n"
"s_add_u32 s65, s65, 4096\n"
"s_sub_u32 s33, s28, s59\n"
"s_lshl_b32 s33, s33, 10\n"
"s_sub_u32 s65, s65, s33\n"
"s_lshl3_add_u32 s65, s58, s65\n"
"buffer_load_dwordx2 a[248:249], v1, s[24:27], s65 offen offset:0 sc1\n"
"buffer_load_dwordx2 a[250:251], v1, s[24:27], s65 offen offset:2048 sc1\n"
"s_add_u32 s65, s65, 4096\n"
"buffer_load_dwordx2 a[252:253], v1, s[24:27], s65 offen offset:0 sc1\n"
"buffer_load_dwordx2 a[254:255], v1, s[24:27], s65 offen offset:2048 sc1\n"
"s_waitcnt lgkmcnt(11)\n"
"v_mfma_f32_16x16x32_bf16 v[28:31], v[60:63], a[0:3], 0\n"
"s_waitcnt lgkmcnt(10)\n"
"ds_read_b128 a[36:39], v1 offset:9216\n"
"v_mfma_f32_16x16x32_bf16 v[28:31], v[64:67], a[16:19], v[28:31]\n"
"s_waitcnt lgkmcnt(10)\n"
"ds_read_b128 a[52:55], v1 offset:13312\n"
"v_mfma_f32_16x16x32_bf16 v[32:35], v[64:67], a[20:23], 0\n"
"s_waitcnt lgkmcnt(10)\n"
"ds_read_b128 a[48:51], v1 offset:12288\n"
"v_mfma_f32_16x16x32_bf16 v[32:35], v[60:63], a[4:7], v[32:35]\n"
"s_waitcnt lgkmcnt(10)\n"
"ds_read_b128 a[32:35], v1 offset:8192\n"
"v_mfma_f32_16x16x32_bf16 v[36:39], v[60:63], a[8:11], 0\n"
"s_waitcnt lgkmcnt(10)\n"
"ds_read_b128 a[64:67], v1 offset:16384\n"
"v_mfma_f32_16x16x32_bf16 v[36:39], v[64:67], a[24:27], v[36:39]\n"
"s_waitcnt lgkmcnt(10)\n"
"ds_read_b128 a[80:83], v1 offset:20480\n"
"v_mfma_f32_16x16x32_bf16 v[40:43], v[64:67], a[28:31], 0\n"
"s_waitcnt lgkmcnt(10)\n"
"ds_read_b128 a[84:87], v1 offset:21504\n"
"v_mfma_f32_16x16x32_bf16 v[40:43], v[60:63], a[12:15], v[40:43]\n"
"s_waitcnt lgkmcnt(10)\n"
"buffer_load_dwordx4 v[60:63], v1, s[24:27], s93 offen offset:1024 sc1\n"
"ds_read_b128 a[68:71], v1 offset:17408\n"
"v_mfma_f32_16x16x32_bf16 v[40:43], v[68:71], a[44:47], v[40:43]\n"
"s_waitcnt lgkmcnt(10)\n"
"ds_read_b128 a[72:75], v1 offset:18432\n"
"v_mfma_f32_16x16x32_bf16 v[40:43], v[72:75], a[60:63], v[40:43]\n"
"s_waitcnt lgkmcnt(10)\n"
"ds_read_b128 a[88:91], v1 offset:22528\n"
"v_mfma_f32_16x16x32_bf16 v[36:39], v[72:75], a[56:59], v[36:39]\n"
"s_waitcnt lgkmcnt(10)\n"
"ds_read_b128 a[92:95], v1 offset:23552\n"
"v_mfma_f32_16x16x32_bf16 v[36:39], v[68:71], a[40:43], v[36:39]\n"
"s_waitcnt lgkmcnt(10)\n"
"buffer_load_dwordx4 v[64:67], v1, s[24:27], s93 offen offset:2048 sc1\n"
"ds_read_b128 a[76:79], v1 offset:19456\n"
"s_waitcnt vmcnt(15)\n"
"ds_write_b128 v6, a[208:211] offset:24576\n"
"v_mfma_f32_16x16x32_bf16 v[44:47], v[10:13], a[208:211], v[44:47]\n"
"v_mfma_f32_16x16x32_bf16 v[48:51], a[208:211], a[208:211], v[48:51]\n"
"s_waitcnt vmcnt(14)\n"
"ds_write_b128 v6, a[212:215] offset:28672\n"
"v_mfma_f32_16x16x32_bf16 v[44:47], v[10:13], a[212:215], v[44:47]\n"
"v_mfma_f32_16x16x32_bf16 v[48:51], a[212:215], a[212:215], v[48:51]\n"
"s_waitcnt vmcnt(13)\n"
"ds_write_b128 v6, a[216:219] offset:32768\n"
"v_mfma_f32_16x16x32_bf16 v[44:47], v[10:13], a[216:219], v[44:47]\n"
"v_mfma_f32_16x16x32_bf16 v[48:51], a[216:219], a[216:219], v[48:51]\n"
"s_waitcnt vmcnt(12)\n"
"ds_write_b128 v6, a[220:223] offset:36864\n"
"v_mfma_f32_16x16x32_bf16 v[44:47], v[10:13], a[220:223], v[44:47]\n"
"v_mfma_f32_16x16x32_bf16 v[48:51], a[220:223], a[220:223], v[48:51]\n"
"s_waitcnt vmcnt(11)\n"
"s_waitcnt lgkmcnt(14)\n"
"ds_write_b128 v6, a[224:227] offset:40960\n"
"v_mfma_f32_16x16x32_bf16 v[44:47], v[10:13], a[224:227], v[44:47]\n"
"v_mfma_f32_16x16x32_bf16 v[48:51], a[224:227], a[224:227], v[48:51]\n"
"s_waitcnt vmcnt(10)\n"
"s_waitcnt lgkmcnt(14)\n"
"ds_write_b128 v6, a[228:231] offset:45056\n"
"v_mfma_f32_16x16x32_bf16 v[44:47], v[10:13], a[228:231], v[44:47]\n"
"v_mfma_f32_16x16x32_bf16 v[48:51], a[228:231], a[228:231], v[48:51]\n"
"s_waitcnt lgkmcnt(0)\n"
"s_barrier\n"
"v_mfma_f32_16x16x32_bf16 v[32:35], v[68:71], a[36:39], v[32:35]\n"
"ds_read_b128 a[108:111], v1 offset:27648\n"
"v_mfma_f32_16x16x32_bf16 v[32:35], v[72:75], a[52:55], v[32:35]\n"
"ds_read_b128 a[124:127], v1 offset:31744\n"
"v_mfma_f32_16x16x32_bf16 v[28:31], v[72:75], a[48:51], v[28:31]\n"
"ds_read_b128 a[120:123], v1 offset:30720\n"
"v_mfma_f32_16x16x32_bf16 v[28:31], v[68:71], a[32:35], v[28:31]\n"
"buffer_load_dwordx4 v[68:71], v1, s[24:27], s93 offen offset:3072 sc1\n"
"ds_read_b128 a[104:107], v1 offset:26624\n"
"v_mfma_f32_16x16x32_bf16 v[28:31], v[76:79], a[64:67], v[28:31]\n"
"ds_read_b128 a[100:103], v1 offset:25600\n"
"v_mfma_f32_16x16x32_bf16 v[28:31], v[80:83], a[80:83], v[28:31]\n"
"ds_read_b128 a[116:119], v1 offset:29696\n"
"v_mfma_f32_16x16x32_bf16 v[32:35], v[80:83], a[84:87], v[32:35]\n"
"ds_read_b128 a[112:115], v1 offset:28672\n"
"v_mfma_f32_16x16x32_bf16 v[32:35], v[76:79], a[68:71], v[32:35]\n"
"s_add_u32 s93, s62, 45056\n"
"buffer_load_dwordx4 v[72:75], v1, s[24:27], s93 offen offset:0 sc1\n"
"ds_read_b128 a[96:99], v1 offset:24576\n"
"v_mfma_f32_16x16x32_bf16 v[36:39], v[76:79], a[72:75], v[36:39]\n"
"ds_read_b128 a[128:131], v1 offset:32768\n"
"v_mfma_f32_16x16x32_bf16 v[36:39], v[80:83], a[88:91], v[36:39]\n"
"ds_read_b128 a[144:147], v1 offset:36864\n"
"v_mfma_f32_16x16x32_bf16 v[40:43], v[80:83], a[92:95], v[40:43]\n"
"ds_read_b128 a[148:151], v1 offset:37888\n"
"v_mfma_f32_16x16x32_bf16 v[40:43], v[76:79], a[76:79], v[40:43]\n"
"s_waitcnt lgkmcnt(10)\n"
"buffer_load_dwordx4 v[76:79], v1, s[24:27], s93 offen offset:1024 sc1\n"
"ds_read_b128 a[132:135], v1 offset:33792\n"
"v_mfma_f32_16x16x32_bf16 v[40:43], v[84:87], a[108:111], v[40:43]\n"
"s_waitcnt lgkmcnt(10)\n"
"ds_read_b128 a[136:139], v1 offset:34816\n"
"v_mfma_f32_16x16x32_bf16 v[40:43], v[88:91], a[124:127], v[40:43]\n"
"s_waitcnt lgkmcnt(10)\n"
"ds_read_b128 a[152:155], v1 offset:38912\n"
"v_mfma_f32_16x16x32_bf16 v[36:39], v[88:91], a[120:123], v[36:39]\n"
"s_waitcnt lgkmcnt(10)\n"
"ds_read_b128 a[156:159], v1 offset:39936\n"
"v_mfma_f32_16x16x32_bf16 v[36:39], v[84:87], a[104:107], v[36:39]\n"
"s_waitcnt lgkmcnt(10)\n"
"buffer_load_dwordx4 v[80:83], v1, s[24:27], s93 offen offset:2048 sc1\n"
"ds_read_b128 a[140:143], v1 offset:35840\n"
"v_mfma_f32_16x16x32_bf16 v[32:35], v[84:87], a[100:103], v[32:35]\n"
"s_waitcnt lgkmcnt(10)\n"
"ds_read_b128 a[172:175], v1 offset:44032\n"
"v_mfma_f32_16x16x32_bf16 v[32:35], v[88:91], a[116:119], v[32:35]\n"
"s_waitcnt lgkmcnt(10)\n"
"ds_read_b128 a[188:191], v1 offset:48128\n"
"v_mfma_f32_16x16x32_bf16 v[28:31], v[88:91], a[112:115], v[28:31]\n"
"s_waitcnt lgkmcnt(10)\n"
"ds_read_b128 a[184:187], v1 offset:47104\n"
"v_mfma_f32_16x16x32_bf16 v[28:31], v[84:87], a[96:99], v[28:31]\n"
"s_waitcnt lgkmcnt(10)\n"
"buffer_load_dwordx4 v[84:87], v1, s[24:27], s93 offen offset:3072 sc1\n"
"ds_read_b128 a[168:171], v1 offset:43008\n"
"v_mfma_f32_16x16x32_bf16 v[28:31], v[92:95], a[128:131], v[28:31]\n"
"s_waitcnt lgkmcnt(10)\n"
"ds_read_b128 a[164:167], v1 offset:41984\n"
"v_mfma_f32_16x16x32_bf16 v[28:31], v[96:99], a[144:147], v[28:31]\n"
"s_waitcnt lgkmcnt(10)\n"
"ds_read_b128 a[180:183], v1 offset:46080\n"
"v_mfma_f32_16x16x32_bf16 v[32:35], v[96:99], a[148:151], v[32:35]\n"
"s_waitcnt lgkmcnt(10)\n"
"ds_read_b128 a[176:179], v1 offset:45056\n"
"v_mfma_f32_16x16x32_bf16 v[32:35], v[92:95], a[132:135], v[32:35]\n"
"s_waitcnt lgkmcnt(10)\n"
"s_add_u32 s93, s62, 49152\n"
"buffer_load_dwordx4 v[88:91], v1, s[24:27], s93 offen offset:0 sc1\n"
"ds_read_b128 a[160:163], v1 offset:40960\n"
"s_waitcnt vmcnt(15)\n"
"ds_write_b128 v6, a[232:235] offset:49152\n"
"v_mfma_f32_16x16x32_bf16 v[44:47], v[10:13], a[232:235], v[44:47]\n"
"v_mfma_f32_16x16x32_bf16 v[48:51], a[232:235], a[232:235], v[48:51]\n"
"s_waitcnt vmcnt(14)\n"
"ds_write_b128 v6, a[236:239] offset:53248\n"
"v_mfma_f32_16x16x32_bf16 v[44:47], v[10:13], a[236:239], v[44:47]\n"
"v_mfma_f32_16x16x32_bf16 v[48:51], a[236:239], a[236:239], v[48:51]\n"
"s_waitcnt vmcnt(13)\n"
"ds_write_b128 v6, a[240:243] offset:57344\n"
"v_mfma_f32_16x16x32_bf16 v[44:47], v[10:13], a[240:243], v[44:47]\n"
"v_mfma_f32_16x16x32_bf16 v[48:51], a[240:243], a[240:243], v[48:51]\n"
"s_waitcnt vmcnt(12)\n"
"ds_write_b128 v6, a[244:247] offset:61440\n"
"v_mfma_f32_16x16x32_bf16 v[44:47], v[10:13], a[244:247], v[44:47]\n"
"v_mfma_f32_16x16x32_bf16 v[48:51], a[244:247], a[244:247], v[48:51]\n"
"s_waitcnt vmcnt(10)\n"
"s_waitcnt lgkmcnt(14)\n"
"ds_write_b128 v7, a[248:251] offset:0\n"
"v_mfma_f32_16x16x32_bf16 v[44:47], v[10:13], a[248:251], v[44:47]\n"
"v_mfma_f32_16x16x32_bf16 v[48:51], a[248:251], a[248:251], v[48:51]\n"
"s_waitcnt vmcnt(8)\n"
"s_waitcnt lgkmcnt(14)\n"
"ds_write_b128 v7, a[252:255] offset:4096\n"
"v_mfma_f32_16x16x32_bf16 v[44:47], v[10:13], a[252:255], v[44:47]\n"
".Lp0_1_%=:\n"
"s_cbranch_execz .Lp0_2_%=\n"
"v_mfma_f32_16x16x32_bf16 v[48:51], a[252:255], a[252:255], v[48:51]\n"
"s_waitcnt lgkmcnt(0)\n"
"s_barrier\n"
"v_mfma_f32_16x16x32_bf16 v[36:39], v[92:95], a[136:139], v[36:39]\n"
"ds_read_b128 a[192:195], v1 offset:49152\n"
"v_mfma_f32_16x16x32_bf16 v[36:39], v[96:99], a[152:155], v[36:39]\n"
"ds_read_b128 a[208:211], v1 offset:53248\n"
"v_mfma_f32_16x16x32_bf16 v[40:43], v[96:99], a[156:159], v[40:43]\n"
"ds_read_b128 a[212:215], v1 offset:54272\n"
"v_mfma_f32_16x16x32_bf16 v[40:43], v[92:95], a[140:143], v[40:43]\n"
"buffer_load_dwordx4 v[92:95], v1, s[24:27], s93 offen offset:1024 sc1\n"
"ds_read_b128 a[196:199], v1 offset:50176\n"
"v_mfma_f32_16x16x32_bf16 v[40:43], v[100:103], a[172:175], v[40:43]\n"
"ds_read_b128 a[200:203], v1 offset:51200\n"
"v_mfma_f32_16x16x32_bf16 v[40:43], v[104:107], a[188:191], v[40:43]\n"
"ds_read_b128 a[216:219], v1 offset:55296\n"
"v_mfma_f32_16x16x32_bf16 v[36:39], v[104:107], a[184:187], v[36:39]\n"
"ds_read_b128 a[220:223], v1 offset:56320\n"
"v_mfma_f32_16x16x32_bf16 v[36:39], v[100:103], a[168:171], v[36:39]\n"
"buffer_load_dwordx4 v[96:99], v1, s[24:27], s93 offen offset:2048 sc1\n"
"ds_read_b128 a[204:207], v1 offset:52224\n"
"v_mfma_f32_16x16x32_bf16 v[32:35], v[100:103], a[164:167], v[32:35]\n"
"ds_read_b128 a[236:239], v1 offset:60416\n"
"v_mfma_f32_16x16x32_bf16 v[32:35], v[104:107], a[180:183], v[32:35]\n"
"ds_read_b128 a[252:255], v1 offset:64512\n"
"v_mfma_f32_16x16x32_bf16 v[28:31], v[104:107], a[176:179], v[28:31]\n"
"ds_read_b128 a[248:251], v1 offset:63488\n"
"v_mfma_f32_16x16x32_bf16 v[28:31], v[100:103], a[160:163], v[28:31]\n"
"s_waitcnt lgkmcnt(10)\n"
"buffer_load_dwordx4 v[100:103], v1, s[24:27], s93 offen offset:3072 sc1\n"
"ds_read_b128 a[232:235], v1 offset:59392\n"
"v_mov_b32 v14, v44\n"
"v_mov_b32 v15, v48\n"
"s_mov_b32 exec_lo, 0x200002\n"
"s_mov_b32 exec_hi, 0x20000200\n"
"v_mov_b32 v15, v49\n"
"s_mov_b32 exec_lo, 0x400004\n"
"s_mov_b32 exec_hi, 0x40000400\n"
"v_mov_b32 v15, v50\n"
"s_mov_b32 exec_lo, 0x800008\n"
"s_mov_b32 exec_hi, 0x80000800\n"
"v_mov_b32 v15, v51\n"
"s_mov_b64 exec, -1\n"
"v_mul_f32 v14, 0.001736111111111111, v14\n"
"v_mul_f32 v16, v14, v14\n"
"v_mul_f32 v15, 0.001736111111111111, v15\n"
"v_sub_f32 v15, v15, v16\n"
"v_max_f32 v15, 0, v15\n"
"v_add_f32 v15, 0x3727c5ac, v15\n"
"v_rsq_f32 v15, v15\n"
"s_nop 0\n"
"v_mul_f32 v14, -v14, v15\n"
"v_and_b32 v16, 15, v0\n"
"v_lshlrev_b32 v16, 3, v16\n"
"v_add_u32 v16, 147456, v16\n"
"s_lshl_b32 s33, s28, 7\n"
"v_add_u32 v17, s33, v16\n"
"s_mov_b32 exec_lo, 0xf0000f\n"
"s_mov_b32 exec_hi, 0xf0000f00\n"
"ds_write_b64 v17, v[14:15]\n"
"s_mov_b64 exec, -1\n"
"v_mfma_f32_16x16x32_bf16 v[28:31], v[108:111], a[192:195], v[28:31]\n"
"s_waitcnt lgkmcnt(11)\n"
"ds_read_b128 a[228:231], v1 offset:58368\n"
"v_mfma_f32_16x16x32_bf16 v[28:31], v[112:115], a[208:211], v[28:31]\n"
"s_waitcnt lgkmcnt(11)\n"
"ds_read_b128 a[244:247], v1 offset:62464\n"
"v_mfma_f32_16x16x32_bf16 v[32:35], v[112:115], a[212:215], v[32:35]\n"
"s_waitcnt lgkmcnt(11)\n"
"ds_read_b128 a[240:243], v1 offset:61440\n"
"v_mfma_f32_16x16x32_bf16 v[32:35], v[108:111], a[196:199], v[32:35]\n"
"s_waitcnt lgkmcnt(11)\n"
"s_add_u32 s93, s62, 53248\n"
"buffer_load_dwordx4 v[104:107], v1, s[24:27], s93 offen offset:0 sc1\n"
"ds_read_b128 a[224:227], v1 offset:57344\n"
"v_mfma_f32_16x16x32_bf16 v[36:39], v[108:111], a[200:203], v[36:39]\n"
"s_waitcnt lgkmcnt(11)\n"
"ds_read_b128 v[224:227], v2 offset:0\n"
"v_mfma_f32_16x16x32_bf16 v[36:39], v[112:115], a[216:219], v[36:39]\n"
"s_waitcnt lgkmcnt(11)\n"
"ds_read_b128 v[240:243], v2 offset:4096\n"
"v_mfma_f32_16x16x32_bf16 v[40:43], v[112:115], a[220:223], v[40:43]\n"
"s_waitcnt lgkmcnt(11)\n"
"ds_read_b128 v[244:247], v2 offset:5120\n"
"v_mfma_f32_16x16x32_bf16 v[40:43], v[108:111], a[204:207], v[40:43]\n"
"s_waitcnt lgkmcnt(11)\n"
"buffer_load_dwordx4 v[108:111], v1, s[24:27], s93 offen offset:1024 sc1\n"
"ds_read_b128 v[228:231], v2 offset:1024\n"
"v_mfma_f32_16x16x32_bf16 v[40:43], v[116:119], a[236:239], v[40:43]\n"
"s_waitcnt lgkmcnt(11)\n"
"ds_read_b128 v[232:235], v2 offset:2048\n"
"v_mfma_f32_16x16x32_bf16 v[40:43], v[120:123], a[252:255], v[40:43]\n"
"s_waitcnt lgkmcnt(11)\n"
"ds_read_b128 v[248:251], v2 offset:6144\n"
"v_mfma_f32_16x16x32_bf16 v[36:39], v[120:123], a[248:251], v[36:39]\n"
"s_waitcnt lgkmcnt(11)\n"
"ds_read_b128 v[252:255], v2 offset:7168\n"
"v_mfma_f32_16x16x32_bf16 v[36:39], v[116:119], a[232:235], v[36:39]\n"
"s_waitcnt lgkmcnt(10)\n"
"buffer_load_dwordx4 v[112:115], v1, s[24:27], s93 offen offset:2048 sc1\n"
"ds_read_b128 v[236:239], v2 offset:3072\n"
"v_mfma_f32_16x16x32_bf16 v[32:35], v[116:119], a[228:231], v[32:35]\n"
"s_waitcnt lgkmcnt(10)\n"
"v_mfma_f32_16x16x32_bf16 v[32:35], v[120:123], a[244:247], v[32:35]\n"
"s_waitcnt lgkmcnt(9)\n"
"v_mfma_f32_16x16x32_bf16 v[28:31], v[120:123], a[240:243], v[28:31]\n"
"s_waitcnt lgkmcnt(8)\n"
"v_mfma_f32_16x16x32_bf16 v[28:31], v[116:119], a[224:227], v[28:31]\n"
"s_waitcnt lgkmcnt(7)\n"
"buffer_load_dwordx4 v[116:119], v1, s[24:27], s93 offen offset:3072 sc1\n"
"s_barrier\n"
"v_mfma_f32_16x16x32_bf16 v[28:31], v[124:127], v[224:227], v[28:31]\n"
"s_waitcnt lgkmcnt(6)\n"
"ds_read_b64 v[6:7], v16 offset:0\n"
"ds_read_b64 v[8:9], v16 offset:128\n"
"v_mfma_f32_16x16x32_bf16 v[28:31], v[128:131], v[240:243], v[28:31]\n"
"s_waitcnt lgkmcnt(7)\n"
"ds_read_b64 v[10:11], v16 offset:256\n"
"ds_read_b64 v[12:13], v16 offset:384\n"
"v_mfma_f32_16x16x32_bf16 v[32:35], v[128:131], v[244:247], v[32:35]\n"
"s_waitcnt lgkmcnt(8)\n"
"v_mfma_f32_16x16x32_bf16 v[32:35], v[124:127], v[228:231], v[32:35]\n"
"s_waitcnt lgkmcnt(7)\n"
"s_add_u32 s93, s62, 57344\n"
"buffer_load_dwordx4 v[120:123], v1, s[24:27], s93 offen offset:0 sc1\n"
"v_mfma_f32_16x16x32_bf16 v[36:39], v[124:127], v[232:235], v[36:39]\n"
"s_waitcnt lgkmcnt(6)\n"
"v_mfma_f32_16x16x32_bf16 v[36:39], v[128:131], v[248:251], v[36:39]\n"
"s_waitcnt lgkmcnt(5)\n"
"v_mfma_f32_16x16x32_bf16 v[40:43], v[128:131], v[252:255], v[40:43]\n"
"s_waitcnt lgkmcnt(4)\n"
"v_mfma_f32_16x16x32_bf16 v[40:43], v[124:127], v[236:239], v[40:43]\n"
"buffer_load_dwordx4 v[124:127], v1, s[24:27], s93 offen offset:1024 sc1\n"
"v_mfma_f32_16x16x32_bf16 v[44:47], v[132:135], a[0:3], 0\n"
"v_mfma_f32_16x16x32_bf16 v[44:47], v[136:139], a[16:19], v[44:47]\n"
"v_mfma_f32_16x16x32_bf16 v[48:51], v[136:139], a[20:23], 0\n"
"v_mfma_f32_16x16x32_bf16 v[48:51], v[132:135], a[4:7], v[48:51]\n"
"buffer_load_dwordx4 v[128:131], v1, s[24:27], s93 offen offset:2048 sc1\n"
"v_mfma_f32_16x16x32_bf16 v[52:55], v[132:135], a[8:11], 0\n"
"ds_read_b128 v[14:17], v5 offset:0\n"
"v_mfma_f32_16x16x32_bf16 v[52:55], v[136:139], a[24:27], v[52:55]\n"
"v_mfma_f32_16x16x32_bf16 v[56:59], v[136:139], a[28:31], 0\n"
"v_mfma_f32_16x16x32_bf16 v[56:59], v[132:135], a[12:15], v[56:59]\n"
"buffer_load_dwordx4 v[132:135], v1, s[24:27], s93 offen offset:3072 sc1\n"
"v_mfma_f32_16x16x32_bf16 v[56:59], v[140:143], a[44:47], v[56:59]\n"
"v_mfma_f32_16x16x32_bf16 v[56:59], v[144:147], a[60:63], v[56:59]\n"
"s_waitcnt lgkmcnt(0)\n"
"v_mul_f32 v18, v6, v14\n"
"v_mfma_f32_16x16x32_bf16 v[52:55], v[144:147], a[56:59], v[52:55]\n"
"v_mul_f32 v19, v6, v15\n"
"v_mfma_f32_16x16x32_bf16 v[52:55], v[140:143], a[40:43], v[52:55]\n"
"s_add_u32 s93, s62, 61440\n"
"buffer_load_dwordx4 v[136:139], v1, s[24:27], s93 offen offset:0 sc1\n"
"v_mul_f32 v20, v6, v16\n"
"v_mfma_f32_16x16x32_bf16 v[48:51], v[140:143], a[36:39], v[48:51]\n"
"v_mul_f32 v21, v6, v17\n"
"v_mfma_f32_16x16x32_bf16 v[48:51], v[144:147], a[52:55], v[48:51]\n"
"v_fmac_f32 v18, v7, v28\n"
"v_mfma_f32_16x16x32_bf16 v[44:47], v[144:147], a[48:51], v[44:47]\n"
"v_fmac_f32 v19, v7, v29\n"
"v_mfma_f32_16x16x32_bf16 v[44:47], v[140:143], a[32:35], v[44:47]\n"
"buffer_load_dwordx4 v[140:143], v1, s[24:27], s93 offen offset:1024 sc1\n"
"v_fmac_f32 v20, v7, v30\n"
"v_mfma_f32_16x16x32_bf16 v[44:47], v[148:151], a[64:67], v[44:47]\n"
"v_fmac_f32 v21, v7, v31\n"
"v_mfma_f32_16x16x32_bf16 v[44:47], v[152:155], a[80:83], v[44:47]\n"
"v_cvt_pk_bf16_f32 v18, v18, v19\n"
"v_mfma_f32_16x16x32_bf16 v[48:51], v[152:155], a[84:87], v[48:51]\n"
"v_cvt_pk_bf16_f32 v19, v20, v21\n"
"v_mfma_f32_16x16x32_bf16 v[48:51], v[148:151], a[68:71], v[48:51]\n"
"buffer_load_dwordx4 v[144:147], v1, s[24:27], s93 offen offset:2048 sc1\n"
"v_pk_max_i16 v18, v18, 0\n"
"v_mfma_f32_16x16x32_bf16 v[52:55], v[148:151], a[72:75], v[52:55]\n"
"v_pk_max_i16 v19, v19, 0\n"
"v_mfma_f32_16x16x32_bf16 v[52:55], v[152:155], a[88:91], v[52:55]\n"
"ds_write_b64 v4, v[18:19] offset:0\n"
"v_mfma_f32_16x16x32_bf16 v[56:59], v[152:155], a[92:95], v[56:59]\n"
"v_mul_f32 v22, v8, v14\n"
"v_mfma_f32_16x16x32_bf16 v[56:59], v[148:151], a[76:79], v[56:59]\n"
"buffer_load_dwordx4 v[148:151], v1, s[24:27], s93 offen offset:3072 sc1\n"
"v_mul_f32 v23, v8, v15\n"
"v_mfma_f32_16x16x32_bf16 v[56:59], v[156:159], a[108:111], v[56:59]\n"
"v_mul_f32 v24, v8, v16\n"
"v_mfma_f32_16x16x32_bf16 v[56:59], v[160:163], a[124:127], v[56:59]\n"
"v_mul_f32 v25, v8, v17\n"
"v_mfma_f32_16x16x32_bf16 v[52:55], v[160:163], a[120:123], v[52:55]\n"
"v_fmac_f32 v22, v9, v32\n"
"v_mfma_f32_16x16x32_bf16 v[52:55], v[156:159], a[104:107], v[52:55]\n"
"s_add_u32 s93, s62, 65536\n"
"buffer_load_dwordx4 v[152:155], v1, s[24:27], s93 offen offset:0 sc1\n"
"v_fmac_f32 v23, v9, v33\n"
"v_mfma_f32_16x16x32_bf16 v[48:51], v[156:159], a[100:103], v[48:51]\n"
"v_fmac_f32 v24, v9, v34\n"
"v_mfma_f32_16x16x32_bf16 v[48:51], v[160:163], a[116:119], v[48:51]\n"
"v_fmac_f32 v25, v9, v35\n"
"v_mfma_f32_16x16x32_bf16 v[44:47], v[160:163], a[112:115], v[44:47]\n"
"v_cvt_pk_bf16_f32 v22, v22, v23\n"
"v_mfma_f32_16x16x32_bf16 v[44:47], v[156:159], a[96:99], v[44:47]\n"
"buffer_load_dwordx4 v[156:159], v1, s[24:27], s93 offen offset:1024 sc1\n"
"v_cvt_pk_bf16_f32 v23, v24, v25\n"
"v_mfma_f32_16x16x32_bf16 v[44:47], v[164:167], a[128:131], v[44:47]\n"
"v_pk_max_i16 v22, v22, 0\n"
"v_mfma_f32_16x16x32_bf16 v[44:47], v[168:171], a[144:147], v[44:47]\n"
"v_pk_max_i16 v23, v23, 0\n"
"v_mfma_f32_16x16x32_bf16 v[48:51], v[168:171], a[148:151], v[48:51]\n"
"ds_write_b64 v4, v[22:23] offset:1024\n"
"v_mfma_f32_16x16x32_bf16 v[48:51], v[164:167], a[132:135], v[48:51]\n"
"buffer_load_dwordx4 v[160:163], v1, s[24:27], s93 offen offset:2048 sc1\n"
"v_mul_f32 v18, v10, v14\n"
"v_mfma_f32_16x16x32_bf16 v[52:55], v[164:167], a[136:139], v[52:55]\n"
"v_mul_f32 v19, v10, v15\n"
"v_mfma_f32_16x16x32_bf16 v[52:55], v[168:171], a[152:155], v[52:55]\n"
"v_mul_f32 v20, v10, v16\n"
"v_mfma_f32_16x16x32_bf16 v[56:59], v[168:171], a[156:159], v[56:59]\n"
"v_mul_f32 v21, v10, v17\n"
"v_mfma_f32_16x16x32_bf16 v[56:59], v[164:167], a[140:143], v[56:59]\n"
"buffer_load_dwordx4 v[164:167], v1, s[24:27], s93 offen offset:3072 sc1\n"
"v_fmac_f32 v18, v11, v36\n"
"v_mfma_f32_16x16x32_bf16 v[56:59], v[172:175], a[172:175], v[56:59]\n"
"v_fmac_f32 v19, v11, v37\n"
"v_mfma_f32_16x16x32_bf16 v[56:59], v[176:179], a[188:191], v[56:59]\n"
"v_fmac_f32 v20, v11, v38\n"
"v_mfma_f32_16x16x32_bf16 v[52:55], v[176:179], a[184:187], v[52:55]\n"
"v_fmac_f32 v21, v11, v39\n"
"v_mfma_f32_16x16x32_bf16 v[52:55], v[172:175], a[168:171], v[52:55]\n"
"s_add_u32 s93, s62, 69632\n"
"buffer_load_dwordx4 v[168:171], v1, s[24:27], s93 offen offset:0 sc1\n"
"v_cvt_pk_bf16_f32 v18, v18, v19\n"
"v_mfma_f32_16x16x32_bf16 v[48:51], v[172:175], a[164:167], v[48:51]\n"
"v_cvt_pk_bf16_f32 v19, v20, v21\n"
"v_mfma_f32_16x16x32_bf16 v[48:51], v[176:179], a[180:183], v[48:51]\n"
"v_pk_max_i16 v18, v18, 0\n"
"v_mfma_f32_16x16x32_bf16 v[44:47], v[176:179], a[176:179], v[44:47]\n"
"v_pk_max_i16 v19, v19, 0\n"
"v_mfma_f32_16x16x32_bf16 v[44:47], v[172:175], a[160:163], v[44:47]\n"
"buffer_load_dwordx4 v[172:175], v1, s[24:27], s93 offen offset:1024 sc1\n"
"ds_write_b64 v4, v[18:19] offset:2048\n"
"v_mfma_f32_16x16x32_bf16 v[44:47], v[180:183], a[192:195], v[44:47]\n"
"v_mul_f32 v22, v12, v14\n"
"v_mfma_f32_16x16x32_bf16 v[44:47], v[184:187], a[208:211], v[44:47]\n"
"v_mul_f32 v23, v12, v15\n"
"v_mfma_f32_16x16x32_bf16 v[48:51], v[184:187], a[212:215], v[48:51]\n"
"v_mul_f32 v24, v12, v16\n"
"v_mfma_f32_16x16x32_bf16 v[48:51], v[180:183], a[196:199], v[48:51]\n"
"buffer_load_dwordx4 v[176:179], v1, s[24:27], s93 offen offset:2048 sc1\n"
"v_mul_f32 v25, v12, v17\n"
"v_mfma_f32_16x16x32_bf16 v[52:55], v[180:183], a[200:203], v[52:55]\n"
"v_fmac_f32 v22, v13, v40\n"
"v_mfma_f32_16x16x32_bf16 v[52:55], v[184:187], a[216:219], v[52:55]\n"
"v_fmac_f32 v23, v13, v41\n"
"v_mfma_f32_16x16x32_bf16 v[56:59], v[184:187], a[220:223], v[56:59]\n"
"v_fmac_f32 v24, v13, v42\n"
"v_mfma_f32_16x16x32_bf16 v[56:59], v[180:183], a[204:207], v[56:59]\n"
"buffer_load_dwordx4 v[180:183], v1, s[24:27], s93 offen offset:3072 sc1\n"
"v_fmac_f32 v25, v13, v43\n"
"v_mfma_f32_16x16x32_bf16 v[56:59], v[188:191], a[236:239], v[56:59]\n"
"v_cvt_pk_bf16_f32 v22, v22, v23\n"
"v_mfma_f32_16x16x32_bf16 v[56:59], v[192:195], a[252:255], v[56:59]\n"
"v_cvt_pk_bf16_f32 v23, v24, v25\n"
"v_mfma_f32_16x16x32_bf16 v[52:55], v[192:195], a[248:251], v[52:55]\n"
"v_pk_max_i16 v22, v22, 0\n"
"v_mfma_f32_16x16x32_bf16 v[52:55], v[188:191], a[232:235], v[52:55]\n"
"s_add_u32 s93, s62, 73728\n"
"buffer_load_dwordx4 v[184:187], v1, s[24:27], s93 offen offset:0 sc1\n"
"v_pk_max_i16 v23, v23, 0\n"
"v_mfma_f32_16x16x32_bf16 v[48:51], v[188:191], a[228:231], v[48:51]\n"
"ds_write_b64 v4, v[22:23] offset:3072\n"
"v_mfma_f32_16x16x32_bf16 v[48:51], v[192:195], a[244:247], v[48:51]\n"
"v_mfma_f32_16x16x32_bf16 v[44:47], v[192:195], a[240:243], v[44:47]\n"
"v_mfma_f32_16x16x32_bf16 v[44:47], v[188:191], a[224:227], v[44:47]\n"
"buffer_load_dwordx4 v[188:191], v1, s[24:27], s93 offen offset:1024 sc1\n"
"v_mfma_f32_16x16x32_bf16 v[44:47], v[196:199], v[224:227], v[44:47]\n"
"v_mfma_f32_16x16x32_bf16 v[44:47], v[200:203], v[240:243], v[44:47]\n"
"v_mfma_f32_16x16x32_bf16 v[48:51], v[200:203], v[244:247], v[48:51]\n"
"v_mfma_f32_16x16x32_bf16 v[48:51], v[196:199], v[228:231], v[48:51]\n"
"buffer_load_dwordx4 v[192:195], v1, s[24:27], s93 offen offset:2048 sc1\n"
"v_mfma_f32_16x16x32_bf16 v[52:55], v[196:199], v[232:235], v[52:55]\n"
"v_mfma_f32_16x16x32_bf16 v[52:55], v[200:203], v[248:251], v[52:55]\n"
"v_mfma_f32_16x16x32_bf16 v[56:59], v[200:203], v[252:255], v[56:59]\n"
"v_mfma_f32_16x16x32_bf16 v[56:59], v[196:199], v[236:239], v[56:59]\n"
"buffer_load_dwordx4 v[196:199], v1, s[24:27], s93 offen offset:3072 sc1\n"
"v_mfma_f32_16x16x32_bf16 v[28:31], v[204:207], a[0:3], 0\n"
"v_mfma_f32_16x16x32_bf16 v[28:31], v[208:211], a[16:19], v[28:31]\n"
"v_mfma_f32_16x16x32_bf16 v[32:35], v[208:211], a[20:23], 0\n"
"v_mfma_f32_16x16x32_bf16 v[32:35], v[204:207], a[4:7], v[32:35]\n"
"s_add_u32 s93, s62, 77824\n"
"buffer_load_dwordx4 v[200:203], v1, s[24:27], s93 offen offset:0 sc1\n"
"v_mfma_f32_16x16x32_bf16 v[36:39], v[204:207], a[8:11], 0\n"
"ds_read_b128 v[14:17], v5 offset:256\n"
"v_mfma_f32_16x16x32_bf16 v[36:39], v[208:211], a[24:27], v[36:39]\n"
"v_mfma_f32_16x16x32_bf16 v[40:43], v[208:211], a[28:31], 0\n"
"v_mfma_f32_16x16x32_bf16 v[40:43], v[204:207], a[12:15], v[40:43]\n"
"buffer_load_dwordx4 v[204:207], v1, s[24:27], s93 offen offset:1024 sc1\n"
"v_mfma_f32_16x16x32_bf16 v[40:43], v[212:215], a[44:47], v[40:43]\n"
"v_mfma_f32_16x16x32_bf16 v[40:43], v[216:219], a[60:63], v[40:43]\n"
"s_waitcnt lgkmcnt(0)\n"
"v_mul_f32 v18, v6, v14\n"
"v_mfma_f32_16x16x32_bf16 v[36:39], v[216:219], a[56:59], v[36:39]\n"
"v_mul_f32 v19, v6, v15\n"
"v_mfma_f32_16x16x32_bf16 v[36:39], v[212:215], a[40:43], v[36:39]\n"
"buffer_load_dwordx4 v[208:211], v1, s[24:27], s93 offen offset:2048 sc1\n"
"v_mul_f32 v20, v6, v16\n"
"v_mfma_f32_16x16x32_bf16 v[32:35], v[212:215], a[36:39], v[32:35]\n"
"v_mul_f32 v21, v6, v17\n"
"v_mfma_f32_16x16x32_bf16 v[32:35], v[216:219], a[52:55], v[32:35]\n"
"v_fmac_f32 v18, v7, v44\n"
"v_mfma_f32_16x16x32_bf16 v[28:31], v[216:219], a[48:51], v[28:31]\n"
"v_fmac_f32 v19, v7, v45\n"
"v_mfma_f32_16x16x32_bf16 v[28:31], v[212:215], a[32:35], v[28:31]\n"
"buffer_load_dwordx4 v[212:215], v1, s[24:27], s93 offen offset:3072 sc1\n"
"v_fmac_f32 v20, v7, v46\n"
"v_mfma_f32_16x16x32_bf16 v[28:31], v[220:223], a[64:67], v[28:31]\n"
"s_waitcnt vmcnt(38)\n"
"v_fmac_f32 v21, v7, v47\n"
"v_mfma_f32_16x16x32_bf16 v[28:31], v[60:63], a[80:83], v[28:31]\n"
"v_cvt_pk_bf16_f32 v18, v18, v19\n"
"v_mfma_f32_16x16x32_bf16 v[32:35], v[60:63], a[84:87], v[32:35]\n"
"v_cvt_pk_bf16_f32 v19, v20, v21\n"
"v_mfma_f32_16x16x32_bf16 v[32:35], v[220:223], a[68:71], v[32:35]\n"
"s_add_u32 s93, s62, 81920\n"
"buffer_load_dwordx4 v[216:219], v1, s[24:27], s93 offen offset:0 sc1\n"
"v_pk_max_i16 v18, v18, 0\n"
"v_mfma_f32_16x16x32_bf16 v[36:39], v[220:223], a[72:75], v[36:39]\n"
"v_pk_max_i16 v19, v19, 0\n"
"v_mfma_f32_16x16x32_bf16 v[36:39], v[60:63], a[88:91], v[36:39]\n"
"ds_write_b64 v4, v[18:19] offset:8192\n"
"v_mfma_f32_16x16x32_bf16 v[40:43], v[60:63], a[92:95], v[40:43]\n"
"v_mul_f32 v22, v8, v14\n"
"v_mfma_f32_16x16x32_bf16 v[40:43], v[220:223], a[76:79], v[40:43]\n"
"s_waitcnt vmcnt(38)\n"
"buffer_load_dwordx4 v[220:223], v1, s[24:27], s93 offen offset:1024 sc1\n"
"v_mul_f32 v23, v8, v15\n"
"v_mfma_f32_16x16x32_bf16 v[40:43], v[64:67], a[108:111], v[40:43]\n"
"s_waitcnt vmcnt(38)\n"
"v_mul_f32 v24, v8, v16\n"
"v_mfma_f32_16x16x32_bf16 v[40:43], v[68:71], a[124:127], v[40:43]\n"
"v_mul_f32 v25, v8, v17\n"
"v_mfma_f32_16x16x32_bf16 v[36:39], v[68:71], a[120:123], v[36:39]\n"
"v_fmac_f32 v22, v9, v48\n"
"v_mfma_f32_16x16x32_bf16 v[36:39], v[64:67], a[104:107], v[36:39]\n"
"buffer_load_dwordx4 v[60:63], v1, s[24:27], s93 offen offset:2048 sc1\n"
"v_fmac_f32 v23, v9, v49\n"
"v_mfma_f32_16x16x32_bf16 v[32:35], v[64:67], a[100:103], v[32:35]\n"
"v_fmac_f32 v24, v9, v50\n"
"v_mfma_f32_16x16x32_bf16 v[32:35], v[68:71], a[116:119], v[32:35]\n"
"v_fmac_f32 v25, v9, v51\n"
"v_mfma_f32_16x16x32_bf16 v[28:31], v[68:71], a[112:115], v[28:31]\n"
"v_cvt_pk_bf16_f32 v22, v22, v23\n"
"v_mfma_f32_16x16x32_bf16 v[28:31], v[64:67], a[96:99], v[28:31]\n"
"s_waitcnt vmcnt(38)\n"
"buffer_load_dwordx4 v[64:67], v1, s[24:27], s93 offen offset:3072 sc1\n"
"v_cvt_pk_bf16_f32 v23, v24, v25\n"
"v_mfma_f32_16x16x32_bf16 v[28:31], v[72:75], a[128:131], v[28:31]\n"
"s_waitcnt vmcnt(38)\n"
"v_pk_max_i16 v22, v22, 0\n"
"v_mfma_f32_16x16x32_bf16 v[28:31], v[76:79], a[144:147], v[28:31]\n"
"v_pk_max_i16 v23, v23, 0\n"
"v_mfma_f32_16x16x32_bf16 v[32:35], v[76:79], a[148:151], v[32:35]\n"
"ds_write_b64 v4, v[22:23] offset:9216\n"
"v_mfma_f32_16x16x32_bf16 v[32:35], v[72:75], a[132:135], v[32:35]\n"
"s_add_u32 s93, s62, 86016\n"
"buffer_load_dwordx4 v[68:71], v1, s[24:27], s93 offen offset:0 sc1\n"
"v_mul_f32 v18, v10, v14\n"
"v_mfma_f32_16x16x32_bf16 v[36:39], v[72:75], a[136:139], v[36:39]\n"
"v_mul_f32 v19, v10, v15\n"
"v_mfma_f32_16x16x32_bf16 v[36:39], v[76:79], a[152:155], v[36:39]\n"
"v_mul_f32 v20, v10, v16\n"
"v_mfma_f32_16x16x32_bf16 v[40:43], v[76:79], a[156:159], v[40:43]\n"
"v_mul_f32 v21, v10, v17\n"
"v_mfma_f32_16x16x32_bf16 v[40:43], v[72:75], a[140:143], v[40:43]\n"
"s_waitcnt vmcnt(38)\n"
"buffer_load_dwordx4 v[72:75], v1, s[24:27], s93 offen offset:1024 sc1\n"
"v_fmac_f32 v18, v11, v52\n"
"v_mfma_f32_16x16x32_bf16 v[40:43], v[80:83], a[172:175], v[40:43]\n"
"s_waitcnt vmcnt(38)\n"
"v_fmac_f32 v19, v11, v53\n"
"v_mfma_f32_16x16x32_bf16 v[40:43], v[84:87], a[188:191], v[40:43]\n"
"v_fmac_f32 v20, v11, v54\n"
"v_mfma_f32_16x16x32_bf16 v[36:39], v[84:87], a[184:187], v[36:39]\n"
"v_fmac_f32 v21, v11, v55\n"
"v_mfma_f32_16x16x32_bf16 v[36:39], v[80:83], a[168:171], v[36:39]\n"
"buffer_load_dwordx4 v[76:79], v1, s[24:27], s93 offen offset:2048 sc1\n"
"v_cvt_pk_bf16_f32 v18, v18, v19\n"
"v_mfma_f32_16x16x32_bf16 v[32:35], v[80:83], a[164:167], v[32:35]\n"
"v_cvt_pk_bf16_f32 v19, v20, v21\n"
"v_mfma_f32_16x16x32_bf16 v[32:35], v[84:87], a[180:183], v[32:35]\n"
"v_pk_max_i16 v18, v18, 0\n"
"v_mfma_f32_16x16x32_bf16 v[28:31], v[84:87], a[176:179], v[28:31]\n"
"v_pk_max_i16 v19, v19, 0\n"
"v_mfma_f32_16x16x32_bf16 v[28:31], v[80:83], a[160:163], v[28:31]\n"
"s_waitcnt vmcnt(38)\n"
"buffer_load_dwordx4 v[80:83], v1, s[24:27], s93 offen offset:3072 sc1\n"
"ds_write_b64 v4, v[18:19] offset:10240\n"
"v_mfma_f32_16x16x32_bf16 v[28:31], v[88:91], a[192:195], v[28:31]\n"
"s_waitcnt vmcnt(38)\n"
"v_mul_f32 v22, v12, v14\n"
"v_mfma_f32_16x16x32_bf16 v[28:31], v[92:95], a[208:211], v[28:31]\n"
"v_mul_f32 v23, v12, v15\n"
"v_mfma_f32_16x16x32_bf16 v[32:35], v[92:95], a[212:215], v[32:35]\n"
"v_mul_f32 v24, v12, v16\n"
"v_mfma_f32_16x16x32_bf16 v[32:35], v[88:91], a[196:199], v[32:35]\n"
"s_add_u32 s93, s62, 90112\n"
"buffer_load_dwordx4 v[84:87], v1, s[24:27], s93 offen offset:0 sc1\n"
"v_mul_f32 v25, v12, v17\n"
"v_mfma_f32_16x16x32_bf16 v[36:39], v[88:91], a[200:203], v[36:39]\n"
"v_fmac_f32 v22, v13, v56\n"
"v_mfma_f32_16x16x32_bf16 v[36:39], v[92:95], a[216:219], v[36:39]\n"
"v_fmac_f32 v23, v13, v57\n"
"v_mfma_f32_16x16x32_bf16 v[40:43], v[92:95], a[220:223], v[40:43]\n"
"v_fmac_f32 v24, v13, v58\n"
"v_mfma_f32_16x16x32_bf16 v[40:43], v[88:91], a[204:207], v[40:43]\n"
"s_waitcnt vmcnt(38)\n"
"buffer_load_dwordx4 v[88:91], v1, s[24:27], s93 offen offset:1024 sc1\n"
"v_fmac_f32 v25, v13, v59\n"
"v_mfma_f32_16x16x32_bf16 v[40:43], v[96:99], a[236:239], v[40:43]\n"
"s_waitcnt vmcnt(38)\n"
"v_cvt_pk_bf16_f32 v22, v22, v23\n"
"v_mfma_f32_16x16x32_bf16 v[40:43], v[100:103], a[252:255], v[40:43]\n"
"v_cvt_pk_bf16_f32 v23, v24, v25\n"
"v_mfma_f32_16x16x32_bf16 v[36:39], v[100:103], a[248:251], v[36:39]\n"
"v_pk_max_i16 v22, v22, 0\n"
"v_mfma_f32_16x16x32_bf16 v[36:39], v[96:99], a[232:235], v[36:39]\n"
"buffer_load_dwordx4 v[92:95], v1, s[24:27], s93 offen offset:2048 sc1\n"
"v_pk_max_i16 v23, v23, 0\n"
"v_mfma_f32_16x16x32_bf16 v[32:35], v[96:99], a[228:231], v[32:35]\n"
"ds_write_b64 v4, v[22:23] offset:11264\n"
"v_mfma_f32_16x16x32_bf16 v[32:35], v[100:103], a[244:247], v[32:35]\n"
"v_mfma_f32_16x16x32_bf16 v[28:31], v[100:103], a[240:243], v[28:31]\n"
"v_mfma_f32_16x16x32_bf16 v[28:31], v[96:99], a[224:227], v[28:31]\n"
"s_waitcnt vmcnt(38)\n"
"buffer_load_dwordx4 v[96:99], v1, s[24:27], s93 offen offset:3072 sc1\n"
"v_mfma_f32_16x16x32_bf16 v[28:31], v[104:107], v[224:227], v[28:31]\n"
"s_waitcnt vmcnt(38)\n"
"v_mfma_f32_16x16x32_bf16 v[28:31], v[108:111], v[240:243], v[28:31]\n"
"v_mfma_f32_16x16x32_bf16 v[32:35], v[108:111], v[244:247], v[32:35]\n"
"v_mfma_f32_16x16x32_bf16 v[32:35], v[104:107], v[228:231], v[32:35]\n"
"s_add_u32 s93, s62, 94208\n"
"buffer_load_dwordx4 v[100:103], v1, s[24:27], s93 offen offset:0 sc1\n"
"v_mfma_f32_16x16x32_bf16 v[36:39], v[104:107], v[232:235], v[36:39]\n"
"v_mfma_f32_16x16x32_bf16 v[36:39], v[108:111], v[248:251], v[36:39]\n"
"v_mfma_f32_16x16x32_bf16 v[40:43], v[108:111], v[252:255], v[40:43]\n"
"v_mfma_f32_16x16x32_bf16 v[40:43], v[104:107], v[236:239], v[40:43]\n"
"s_waitcnt vmcnt(38)\n"
"buffer_load_dwordx4 v[104:107], v1, s[24:27], s93 offen offset:1024 sc1\n"
"v_mfma_f32_16x16x32_bf16 v[44:47], v[112:115], a[0:3], 0\n"
"s_waitcnt vmcnt(38)\n"
"v_mfma_f32_16x16x32_bf16 v[44:47], v[116:119], a[16:19], v[44:47]\n"
"v_mfma_f32_16x16x32_bf16 v[48:51], v[116:119], a[20:23], 0\n"
"v_mfma_f32_16x16x32_bf16 v[48:51], v[112:115], a[4:7], v[48:51]\n"
"buffer_load_dwordx4 v[108:111], v1, s[24:27], s93 offen offset:2048 sc1\n"
"v_mfma_f32_16x16x32_bf16 v[52:55], v[112:115], a[8:11], 0\n"
"ds_read_b128 v[14:17], v5 offset:512\n"
"v_mfma_f32_16x16x32_bf16 v[52:55], v[116:119], a[24:27], v[52:55]\n"
"v_mfma_f32_16x16x32_bf16 v[56:59], v[116:119], a[28:31], 0\n"
"v_mfma_f32_16x16x32_bf16 v[56:59], v[112:115], a[12:15], v[56:59]\n"
"s_waitcnt vmcnt(38)\n"
"buffer_load_dwordx4 v[112:115], v1, s[24:27], s93 offen offset:3072 sc1\n"
"v_mfma_f32_16x16x32_bf16 v[56:59], v[120:123], a[44:47], v[56:59]\n"
"s_waitcnt vmcnt(38)\n"
"v_mfma_f32_16x16x32_bf16 v[56:59], v[124:127], a[60:63], v[56:59]\n"
"s_waitcnt lgkmcnt(0)\n"
"v_mul_f32 v18, v6, v14\n"
"v_mfma_f32_16x16x32_bf16 v[52:55], v[124:127], a[56:59], v[52:55]\n"
"v_mul_f32 v19, v6, v15\n"
"v_mfma_f32_16x16x32_bf16 v[52:55], v[120:123], a[40:43], v[52:55]\n"
"s_add_u32 s93, s62, 98304\n"
"buffer_load_dwordx4 v[116:119], v1, s[24:27], s93 offen offset:0 sc1\n"
"v_mul_f32 v20, v6, v16\n"
"v_mfma_f32_16x16x32_bf16 v[48:51], v[120:123], a[36:39], v[48:51]\n"
"v_mul_f32 v21, v6, v17\n"
"v_mfma_f32_16x16x32_bf16 v[48:51], v[124:127], a[52:55], v[48:51]\n"
"v_fmac_f32 v18, v7, v28\n"
"v_mfma_f32_16x16x32_bf16 v[44:47], v[124:127], a[48:51], v[44:47]\n"
"v_fmac_f32 v19, v7, v29\n"
"v_mfma_f32_16x16x32_bf16 v[44:47], v[120:123], a[32:35], v[44:47]\n"
"s_waitcnt vmcnt(38)\n"
"buffer_load_dwordx4 v[120:123], v1, s[24:27], s93 offen offset:1024 sc1\n"
"v_fmac_f32 v20, v7, v30\n"
"v_mfma_f32_16x16x32_bf16 v[44:47], v[128:131], a[64:67], v[44:47]\n"
"s_waitcnt vmcnt(38)\n"
"v_fmac_f32 v21, v7, v31\n"
"v_mfma_f32_16x16x32_bf16 v[44:47], v[132:135], a[80:83], v[44:47]\n"
"v_cvt_pk_bf16_f32 v18, v18, v19\n"
"v_mfma_f32_16x16x32_bf16 v[48:51], v[132:135], a[84:87], v[48:51]\n"
"v_cvt_pk_bf16_f32 v19, v20, v21\n"
"v_mfma_f32_16x16x32_bf16 v[48:51], v[128:131], a[68:71], v[48:51]\n"
"buffer_load_dwordx4 v[124:127], v1, s[24:27], s93 offen offset:2048 sc1\n"
"v_pk_max_i16 v18, v18, 0\n"
"v_mfma_f32_16x16x32_bf16 v[52:55], v[128:131], a[72:75], v[52:55]\n"
"v_pk_max_i16 v19, v19, 0\n"
"v_mfma_f32_16x16x32_bf16 v[52:55], v[132:135], a[88:91], v[52:55]\n"
"ds_write_b64 v4, v[18:19] offset:16384\n"
"v_mfma_f32_16x16x32_bf16 v[56:59], v[132:135], a[92:95], v[56:59]\n"
"v_mul_f32 v22, v8, v14\n"
"v_mfma_f32_16x16x32_bf16 v[56:59], v[128:131], a[76:79], v[56:59]\n"
"s_waitcnt vmcnt(38)\n"
"buffer_load_dwordx4 v[128:131], v1, s[24:27], s93 offen offset:3072 sc1\n"
"v_mul_f32 v23, v8, v15\n"
"v_mfma_f32_16x16x32_bf16 v[56:59], v[136:139], a[108:111], v[56:59]\n"
"s_waitcnt vmcnt(38)\n"
"v_mul_f32 v24, v8, v16\n"
"v_mfma_f32_16x16x32_bf16 v[56:59], v[140:143], a[124:127], v[56:59]\n"
"v_mul_f32 v25, v8, v17\n"
"v_mfma_f32_16x16x32_bf16 v[52:55], v[140:143], a[120:123], v[52:55]\n"
"v_fmac_f32 v22, v9, v32\n"
"v_mfma_f32_16x16x32_bf16 v[52:55], v[136:139], a[104:107], v[52:55]\n"
"s_add_u32 s93, s62, 102400\n"
"buffer_load_dwordx4 v[132:135], v1, s[24:27], s93 offen offset:0 sc1\n"
"v_fmac_f32 v23, v9, v33\n"
"v_mfma_f32_16x16x32_bf16 v[48:51], v[136:139], a[100:103], v[48:51]\n"
"v_fmac_f32 v24, v9, v34\n"
"v_mfma_f32_16x16x32_bf16 v[48:51], v[140:143], a[116:119], v[48:51]\n"
"v_fmac_f32 v25, v9, v35\n"
"v_mfma_f32_16x16x32_bf16 v[44:47], v[140:143], a[112:115], v[44:47]\n"
"v_cvt_pk_bf16_f32 v22, v22, v23\n"
"v_mfma_f32_16x16x32_bf16 v[44:47], v[136:139], a[96:99], v[44:47]\n"
"s_waitcnt vmcnt(38)\n"
"buffer_load_dwordx4 v[136:139], v1, s[24:27], s93 offen offset:1024 sc1\n"
"v_cvt_pk_bf16_f32 v23, v24, v25\n"
"v_mfma_f32_16x16x32_bf16 v[44:47], v[144:147], a[128:131], v[44:47]\n"
"s_waitcnt vmcnt(38)\n"
"v_pk_max_i16 v22, v22, 0\n"
"v_mfma_f32_16x16x32_bf16 v[44:47], v[148:151], a[144:147], v[44:47]\n"
"v_pk_max_i16 v23, v23, 0\n"
"v_mfma_f32_16x16x32_bf16 v[48:51], v[148:151], a[148:151], v[48:51]\n"
"ds_write_b64 v4, v[22:23] offset:17408\n"
"v_mfma_f32_16x16x32_bf16 v[48:51], v[144:147], a[132:135], v[48:51]\n"
"buffer_load_dwordx4 v[140:143], v1, s[24:27], s93 offen offset:2048 sc1\n"
"v_mul_f32 v18, v10, v14\n"
"v_mfma_f32_16x16x32_bf16 v[52:55], v[144:147], a[136:139], v[52:55]\n"
"v_mul_f32 v19, v10, v15\n"
"v_mfma_f32_16x16x32_bf16 v[52:55], v[148:151], a[152:155], v[52:55]\n"
"v_mul_f32 v20, v10, v16\n"
"v_mfma_f32_16x16x32_bf16 v[56:59], v[148:151], a[156:159], v[56:59]\n"
"v_mul_f32 v21, v10, v17\n"
"v_mfma_f32_16x16x32_bf16 v[56:59], v[144:147], a[140:143], v[56:59]\n"
"s_waitcnt vmcnt(38)\n"
"buffer_load_dwordx4 v[144:147], v1, s[24:27], s93 offen offset:3072 sc1\n"
"v_fmac_f32 v18, v11, v36\n"
"v_mfma_f32_16x16x32_bf16 v[56:59], v[152:155], a[172:175], v[56:59]\n"
"s_waitcnt vmcnt(38)\n"
"v_fmac_f32 v19, v11, v37\n"
"v_mfma_f32_16x16x32_bf16 v[56:59], v[156:159], a[188:191], v[56:59]\n"
"v_fmac_f32 v20, v11, v38\n"
"v_mfma_f32_16x16x32_bf16 v[52:55], v[156:159], a[184:187], v[52:55]\n"
"v_fmac_f32 v21, v11, v39\n"
"v_mfma_f32_16x16x32_bf16 v[52:55], v[152:155], a[168:171], v[52:55]\n"
"s_add_u32 s93, s62, 106496\n"
"buffer_load_dwordx4 v[148:151], v1, s[24:27], s93 offen offset:0 sc1\n"
"v_cvt_pk_bf16_f32 v18, v18, v19\n"
"v_mfma_f32_16x16x32_bf16 v[48:51], v[152:155], a[164:167], v[48:51]\n"
"v_cvt_pk_bf16_f32 v19, v20, v21\n"
"v_mfma_f32_16x16x32_bf16 v[48:51], v[156:159], a[180:183], v[48:51]\n"
"v_pk_max_i16 v18, v18, 0\n"
"v_mfma_f32_16x16x32_bf16 v[44:47], v[156:159], a[176:179], v[44:47]\n"
"v_pk_max_i16 v19, v19, 0\n"
"v_mfma_f32_16x16x32_bf16 v[44:47], v[152:155], a[160:163], v[44:47]\n"
"s_waitcnt vmcnt(38)\n"
"buffer_load_dwordx4 v[152:155], v1, s[24:27], s93 offen offset:1024 sc1\n"
"ds_write_b64 v4, v[18:19] offset:18432\n"
"v_mfma_f32_16x16x32_bf16 v[44:47], v[160:163], a[192:195], v[44:47]\n"
"s_waitcnt vmcnt(38)\n"
"v_mul_f32 v22, v12, v14\n"
"v_mfma_f32_16x16x32_bf16 v[44:47], v[164:167], a[208:211], v[44:47]\n"
"v_mul_f32 v23, v12, v15\n"
"v_mfma_f32_16x16x32_bf16 v[48:51], v[164:167], a[212:215], v[48:51]\n"
"v_mul_f32 v24, v12, v16\n"
"v_mfma_f32_16x16x32_bf16 v[48:51], v[160:163], a[196:199], v[48:51]\n"
"buffer_load_dwordx4 v[156:159], v1, s[24:27], s93 offen offset:2048 sc1\n"
"v_mul_f32 v25, v12, v17\n"
"v_mfma_f32_16x16x32_bf16 v[52:55], v[160:163], a[200:203], v[52:55]\n"
"v_fmac_f32 v22, v13, v40\n"
"v_mfma_f32_16x16x32_bf16 v[52:55], v[164:167], a[216:219], v[52:55]\n"
"v_fmac_f32 v23, v13, v41\n"
"v_mfma_f32_16x16x32_bf16 v[56:59], v[164:167], a[220:223], v[56:59]\n"
"v_fmac_f32 v24, v13, v42\n"
"v_mfma_f32_16x16x32_bf16 v[56:59], v[160:163], a[204:207], v[56:59]\n"
"s_waitcnt vmcnt(38)\n"
"buffer_load_dwordx4 v[160:163], v1, s[24:27], s93 offen offset:3072 sc1\n"
"v_fmac_f32 v25, v13, v43\n"
"v_mfma_f32_16x16x32_bf16 v[56:59], v[168:171], a[236:239], v[56:59]\n"
"s_waitcnt vmcnt(38)\n"
"v_cvt_pk_bf16_f32 v22, v22, v23\n"
"v_mfma_f32_16x16x32_bf16 v[56:59], v[172:175], a[252:255], v[56:59]\n"
"v_cvt_pk_bf16_f32 v23, v24, v25\n"
"v_mfma_f32_16x16x32_bf16 v[52:55], v[172:175], a[248:251], v[52:55]\n"
"v_pk_max_i16 v22, v22, 0\n"
"v_mfma_f32_16x16x32_bf16 v[52:55], v[168:171], a[232:235], v[52:55]\n"
"s_add_u32 s93, s62, 110592\n"
"buffer_load_dwordx4 v[164:167], v1, s[24:27], s93 offen offset:0 sc1\n"
"v_pk_max_i16 v23, v23, 0\n"
"v_mfma_f32_16x16x32_bf16 v[48:51], v[168:171], a[228:231], v[48:51]\n"
"ds_write_b64 v4, v[22:23] offset:19456\n"
"v_mfma_f32_16x16x32_bf16 v[48:51], v[172:175], a[244:247], v[48:51]\n"
"v_mfma_f32_16x16x32_bf16 v[44:47], v[172:175], a[240:243], v[44:47]\n"
".Lp1_1_%=:\n"
"s_cbranch_execz .Lp1_2_%=\n"
"v_mfma_f32_16x16x32_bf16 v[44:47], v[168:171], a[224:227], v[44:47]\n"
"s_waitcnt vmcnt(38)\n"
"buffer_load_dwordx4 v[168:171], v1, s[24:27], s93 offen offset:1024 sc1\n"
"v_mfma_f32_16x16x32_bf16 v[44:47], v[176:179], v[224:227], v[44:47]\n"
"s_waitcnt vmcnt(38)\n"
"v_mfma_f32_16x16x32_bf16 v[44:47], v[180:183], v[240:243], v[44:47]\n"
"v_mfma_f32_16x16x32_bf16 v[48:51], v[180:183], v[244:247], v[48:51]\n"
"v_mfma_f32_16x16x32_bf16 v[48:51], v[176:179], v[228:231], v[48:51]\n"
"buffer_load_dwordx4 v[172:175], v1, s[24:27], s93 offen offset:2048 sc1\n"
"v_mfma_f32_16x16x32_bf16 v[52:55], v[176:179], v[232:235], v[52:55]\n"
"v_mfma_f32_16x16x32_bf16 v[52:55], v[180:183], v[248:251], v[52:55]\n"
"v_mfma_f32_16x16x32_bf16 v[56:59], v[180:183], v[252:255], v[56:59]\n"
"v_mfma_f32_16x16x32_bf16 v[56:59], v[176:179], v[236:239], v[56:59]\n"
"s_waitcnt vmcnt(38)\n"
"buffer_load_dwordx4 v[176:179], v1, s[24:27], s93 offen offset:3072 sc1\n"
"v_mfma_f32_16x16x32_bf16 v[28:31], v[184:187], a[0:3], 0\n"
"s_waitcnt vmcnt(38)\n"
"v_mfma_f32_16x16x32_bf16 v[28:31], v[188:191], a[16:19], v[28:31]\n"
"v_mfma_f32_16x16x32_bf16 v[32:35], v[188:191], a[20:23], 0\n"
"v_mfma_f32_16x16x32_bf16 v[32:35], v[184:187], a[4:7], v[32:35]\n"
"s_add_u32 s93, s62, 114688\n"
"buffer_load_dwordx4 v[180:183], v1, s[24:27], s93 offen offset:0 sc1\n"
"v_mfma_f32_16x16x32_bf16 v[36:39], v[184:187], a[8:11], 0\n"
"ds_read_b128 v[14:17], v5 offset:768\n"
"v_mfma_f32_16x16x32_bf16 v[36:39], v[188:191], a[24:27], v[36:39]\n"
"v_mfma_f32_16x16x32_bf16 v[40:43], v[188:191], a[28:31], 0\n"
"v_mfma_f32_16x16x32_bf16 v[40:43], v[184:187], a[12:15], v[40:43]\n"
"s_waitcnt vmcnt(38)\n"
"buffer_load_dwordx4 v[184:187], v1, s[24:27], s93 offen offset:1024 sc1\n"
"v_mfma_f32_16x16x32_bf16 v[40:43], v[192:195], a[44:47], v[40:43]\n"
"s_waitcnt vmcnt(38)\n"
"v_mfma_f32_16x16x32_bf16 v[40:43], v[196:199], a[60:63], v[40:43]\n"
"s_waitcnt lgkmcnt(0)\n"
"v_mul_f32 v18, v6, v14\n"
"v_mfma_f32_16x16x32_bf16 v[36:39], v[196:199], a[56:59], v[36:39]\n"
"v_mul_f32 v19, v6, v15\n"
"v_mfma_f32_16x16x32_bf16 v[36:39], v[192:195], a[40:43], v[36:39]\n"
"buffer_load_dwordx4 v[188:191], v1, s[24:27], s93 offen offset:2048 sc1\n"
"v_mul_f32 v20, v6, v16\n"
"v_mfma_f32_16x16x32_bf16 v[32:35], v[192:195], a[36:39], v[32:35]\n"
"v_mul_f32 v21, v6, v17\n"
"v_mfma_f32_16x16x32_bf16 v[32:35], v[196:199], a[52:55], v[32:35]\n"
"v_fmac_f32 v18, v7, v44\n"
"v_mfma_f32_16x16x32_bf16 v[28:31], v[196:199], a[48:51], v[28:31]\n"
"v_fmac_f32 v19, v7, v45\n"
"v_mfma_f32_16x16x32_bf16 v[28:31], v[192:195], a[32:35], v[28:31]\n"
"s_waitcnt vmcnt(38)\n"
"buffer_load_dwordx4 v[192:195], v1, s[24:27], s93 offen offset:3072 sc1\n"
"v_fmac_f32 v20, v7, v46\n"
"v_mfma_f32_16x16x32_bf16 v[28:31], v[200:203], a[64:67], v[28:31]\n"
"s_waitcnt vmcnt(38)\n"
"v_fmac_f32 v21, v7, v47\n"
"v_mfma_f32_16x16x32_bf16 v[28:31], v[204:207], a[80:83], v[28:31]\n"
"v_cvt_pk_bf16_f32 v18, v18, v19\n"
"v_mfma_f32_16x16x32_bf16 v[32:35], v[204:207], a[84:87], v[32:35]\n"
"v_cvt_pk_bf16_f32 v19, v20, v21\n"
"v_mfma_f32_16x16x32_bf16 v[32:35], v[200:203], a[68:71], v[32:35]\n"
"s_add_u32 s93, s62, 118784\n"
"buffer_load_dwordx4 v[196:199], v1, s[24:27], s93 offen offset:0 sc1\n"
"v_pk_max_i16 v18, v18, 0\n"
"v_mfma_f32_16x16x32_bf16 v[36:39], v[200:203], a[72:75], v[36:39]\n"
"v_pk_max_i16 v19, v19, 0\n"
"v_mfma_f32_16x16x32_bf16 v[36:39], v[204:207], a[88:91], v[36:39]\n"
"ds_write_b64 v4, v[18:19] offset:24576\n"
"v_mfma_f32_16x16x32_bf16 v[40:43], v[204:207], a[92:95], v[40:43]\n"
"v_mul_f32 v22, v8, v14\n"
"v_mfma_f32_16x16x32_bf16 v[40:43], v[200:203], a[76:79], v[40:43]\n"
"s_waitcnt vmcnt(38)\n"
"buffer_load_dwordx4 v[200:203], v1, s[24:27], s93 offen offset:1024 sc1\n"
"v_mul_f32 v23, v8, v15\n"
"v_mfma_f32_16x16x32_bf16 v[40:43], v[208:211], a[108:111], v[40:43]\n"
"s_waitcnt vmcnt(38)\n"
"v_mul_f32 v24, v8, v16\n"
"v_mfma_f32_16x16x32_bf16 v[40:43], v[212:215], a[124:127], v[40:43]\n"
"v_mul_f32 v25, v8, v17\n"
"v_mfma_f32_16x16x32_bf16 v[36:39], v[212:215], a[120:123], v[36:39]\n"
"v_fmac_f32 v22, v9, v48\n"
"v_mfma_f32_16x16x32_bf16 v[36:39], v[208:211], a[104:107], v[36:39]\n"
"buffer_load_dwordx4 v[204:207], v1, s[24:27], s93 offen offset:2048 sc1\n"
"v_fmac_f32 v23, v9, v49\n"
"v_mfma_f32_16x16x32_bf16 v[32:35], v[208:211], a[100:103], v[32:35]\n"
"v_fmac_f32 v24, v9, v50\n"
"v_mfma_f32_16x16x32_bf16 v[32:35], v[212:215], a[116:119], v[32:35]\n"
"v_fmac_f32 v25, v9, v51\n"
"v_mfma_f32_16x16x32_bf16 v[28:31], v[212:215], a[112:115], v[28:31]\n"
"v_cvt_pk_bf16_f32 v22, v22, v23\n"
"v_mfma_f32_16x16x32_bf16 v[28:31], v[208:211], a[96:99], v[28:31]\n"
"s_waitcnt vmcnt(38)\n"
"buffer_load_dwordx4 v[208:211], v1, s[24:27], s93 offen offset:3072 sc1\n"
"v_cvt_pk_bf16_f32 v23, v24, v25\n"
"v_mfma_f32_16x16x32_bf16 v[28:31], v[216:219], a[128:131], v[28:31]\n"
"s_waitcnt vmcnt(38)\n"
"v_pk_max_i16 v22, v22, 0\n"
"v_mfma_f32_16x16x32_bf16 v[28:31], v[220:223], a[144:147], v[28:31]\n"
"v_pk_max_i16 v23, v23, 0\n"
"v_mfma_f32_16x16x32_bf16 v[32:35], v[220:223], a[148:151], v[32:35]\n"
"ds_write_b64 v4, v[22:23] offset:25600\n"
"v_mfma_f32_16x16x32_bf16 v[32:35], v[216:219], a[132:135], v[32:35]\n"
"s_add_u32 s93, s62, 122880\n"
"buffer_load_dwordx4 v[212:215], v1, s[24:27], s93 offen offset:0 sc1\n"
"v_mul_f32 v18, v10, v14\n"
"v_mfma_f32_16x16x32_bf16 v[36:39], v[216:219], a[136:139], v[36:39]\n"
"v_mul_f32 v19, v10, v15\n"
"v_mfma_f32_16x16x32_bf16 v[36:39], v[220:223], a[152:155], v[36:39]\n"
"v_mul_f32 v20, v10, v16\n"
"v_mfma_f32_16x16x32_bf16 v[40:43], v[220:223], a[156:159], v[40:43]\n"
"v_mul_f32 v21, v10, v17\n"
"v_mfma_f32_16x16x32_bf16 v[40:43], v[216:219], a[140:143], v[40:43]\n"
"s_waitcnt vmcnt(38)\n"
"buffer_load_dwordx4 v[216:219], v1, s[24:27], s93 offen offset:1024 sc1\n"
"v_fmac_f32 v18, v11, v52\n"
"v_mfma_f32_16x16x32_bf16 v[40:43], v[60:63], a[172:175], v[40:43]\n"
"s_waitcnt vmcnt(38)\n"
"v_fmac_f32 v19, v11, v53\n"
"v_mfma_f32_16x16x32_bf16 v[40:43], v[64:67], a[188:191], v[40:43]\n"
"v_fmac_f32 v20, v11, v54\n"
"v_mfma_f32_16x16x32_bf16 v[36:39], v[64:67], a[184:187], v[36:39]\n"
"v_fmac_f32 v21, v11, v55\n"
"v_mfma_f32_16x16x32_bf16 v[36:39], v[60:63], a[168:171], v[36:39]\n"
"buffer_load_dwordx4 v[220:223], v1, s[24:27], s93 offen offset:2048 sc1\n"
"v_cvt_pk_bf16_f32 v18, v18, v19\n"
"v_mfma_f32_16x16x32_bf16 v[32:35], v[60:63], a[164:167], v[32:35]\n"
"v_cvt_pk_bf16_f32 v19, v20, v21\n"
"v_mfma_f32_16x16x32_bf16 v[32:35], v[64:67], a[180:183], v[32:35]\n"
"v_pk_max_i16 v18, v18, 0\n"
"v_mfma_f32_16x16x32_bf16 v[28:31], v[64:67], a[176:179], v[28:31]\n"
"v_pk_max_i16 v19, v19, 0\n"
"v_mfma_f32_16x16x32_bf16 v[28:31], v[60:63], a[160:163], v[28:31]\n"
"s_waitcnt vmcnt(38)\n"
"buffer_load_dwordx4 v[60:63], v1, s[24:27], s93 offen offset:3072 sc1\n"
"ds_write_b64 v4, v[18:19] offset:26624\n"
"v_mfma_f32_16x16x32_bf16 v[28:31], v[68:71], a[192:195], v[28:31]\n"
"s_waitcnt vmcnt(38)\n"
"v_mul_f32 v22, v12, v14\n"
"v_mfma_f32_16x16x32_bf16 v[28:31], v[72:75], a[208:211], v[28:31]\n"
"v_mul_f32 v23, v12, v15\n"
"v_mfma_f32_16x16x32_bf16 v[32:35], v[72:75], a[212:215], v[32:35]\n"
"v_mul_f32 v24, v12, v16\n"
"v_mfma_f32_16x16x32_bf16 v[32:35], v[68:71], a[196:199], v[32:35]\n"
"s_add_u32 s93, s62, 126976\n"
"buffer_load_dwordx4 v[64:67], v1, s[24:27], s93 offen offset:0 sc1\n"
"v_mul_f32 v25, v12, v17\n"
"v_mfma_f32_16x16x32_bf16 v[36:39], v[68:71], a[200:203], v[36:39]\n"
"v_fmac_f32 v22, v13, v56\n"
"v_mfma_f32_16x16x32_bf16 v[36:39], v[72:75], a[216:219], v[36:39]\n"
"v_fmac_f32 v23, v13, v57\n"
"v_mfma_f32_16x16x32_bf16 v[40:43], v[72:75], a[220:223], v[40:43]\n"
"v_fmac_f32 v24, v13, v58\n"
"v_mfma_f32_16x16x32_bf16 v[40:43], v[68:71], a[204:207], v[40:43]\n"
"s_waitcnt vmcnt(38)\n"
"buffer_load_dwordx4 v[68:71], v1, s[24:27], s93 offen offset:1024 sc1\n"
"v_fmac_f32 v25, v13, v59\n"
"v_mfma_f32_16x16x32_bf16 v[40:43], v[76:79], a[236:239], v[40:43]\n"
"s_waitcnt vmcnt(38)\n"
"v_cvt_pk_bf16_f32 v22, v22, v23\n"
"v_mfma_f32_16x16x32_bf16 v[40:43], v[80:83], a[252:255], v[40:43]\n"
"v_cvt_pk_bf16_f32 v23, v24, v25\n"
"v_mfma_f32_16x16x32_bf16 v[36:39], v[80:83], a[248:251], v[36:39]\n"
"v_pk_max_i16 v22, v22, 0\n"
"v_mfma_f32_16x16x32_bf16 v[36:39], v[76:79], a[232:235], v[36:39]\n"
"buffer_load_dwordx4 v[72:75], v1, s[24:27], s93 offen offset:2048 sc1\n"
"v_pk_max_i16 v23, v23, 0\n"
"v_mfma_f32_16x16x32_bf16 v[32:35], v[76:79], a[228:231], v[32:35]\n"
"ds_write_b64 v4, v[22:23] offset:27648\n"
"v_mfma_f32_16x16x32_bf16 v[32:35], v[80:83], a[244:247], v[32:35]\n"
"v_mfma_f32_16x16x32_bf16 v[28:31], v[80:83], a[240:243], v[28:31]\n"
"v_mfma_f32_16x16x32_bf16 v[28:31], v[76:79], a[224:227], v[28:31]\n"
"s_waitcnt vmcnt(38)\n"
"buffer_load_dwordx4 v[76:79], v1, s[24:27], s93 offen offset:3072 sc1\n"
"v_mfma_f32_16x16x32_bf16 v[28:31], v[84:87], v[224:227], v[28:31]\n"
"s_waitcnt vmcnt(38)\n"
"v_mfma_f32_16x16x32_bf16 v[28:31], v[88:91], v[240:243], v[28:31]\n"
"v_mfma_f32_16x16x32_bf16 v[32:35], v[88:91], v[244:247], v[32:35]\n"
"v_mfma_f32_16x16x32_bf16 v[32:35], v[84:87], v[228:231], v[32:35]\n"
"s_add_u32 s93, s62, 131072\n"
"buffer_load_dwordx4 v[80:83], v1, s[24:27], s93 offen offset:0 sc1\n"
"v_mfma_f32_16x16x32_bf16 v[36:39], v[84:87], v[232:235], v[36:39]\n"
"v_mfma_f32_16x16x32_bf16 v[36:39], v[88:91], v[248:251], v[36:39]\n"
"v_mfma_f32_16x16x32_bf16 v[40:43], v[88:91], v[252:255], v[40:43]\n"
"v_mfma_f32_16x16x32_bf16 v[40:43], v[84:87], v[236:239], v[40:43]\n"
"s_waitcnt vmcnt(38)\n"
"buffer_load_dwordx4 v[84:87], v1, s[24:27], s93 offen offset:1024 sc1\n"
"v_mfma_f32_16x16x32_bf16 v[44:47], v[92:95], a[0:3], 0\n"
"s_waitcnt vmcnt(38)\n"
"v_mfma_f32_16x16x32_bf16 v[44:47], v[96:99], a[16:19], v[44:47]\n"
"v_mfma_f32_16x16x32_bf16 v[48:51], v[96:99], a[20:23], 0\n"
"v_mfma_f32_16x16x32_bf16 v[48:51], v[92:95], a[4:7], v[48:51]\n"
"buffer_load_dwordx4 v[88:91], v1, s[24:27], s93 offen offset:2048 sc1\n"
"v_mfma_f32_16x16x32_bf16 v[52:55], v[92:95], a[8:11], 0\n"
"ds_read_b128 v[14:17], v5 offset:1024\n"
"v_mfma_f32_16x16x32_bf16 v[52:55], v[96:99], a[24:27], v[52:55]\n"
"v_mfma_f32_16x16x32_bf16 v[56:59], v[96:99], a[28:31], 0\n"
"v_mfma_f32_16x16x32_bf16 v[56:59], v[92:95], a[12:15], v[56:59]\n"
"s_waitcnt vmcnt(38)\n"
"buffer_load_dwordx4 v[92:95], v1, s[24:27], s93 offen offset:3072 sc1\n"
"v_mfma_f32_16x16x32_bf16 v[56:59], v[100:103], a[44:47], v[56:59]\n"
"s_waitcnt vmcnt(38)\n"
"v_mfma_f32_16x16x32_bf16 v[56:59], v[104:107], a[60:63], v[56:59]\n"
"s_waitcnt lgkmcnt(0)\n"
"v_mul_f32 v18, v6, v14\n"
"v_mfma_f32_16x16x32_bf16 v[52:55], v[104:107], a[56:59], v[52:55]\n"
"v_mul_f32 v19, v6, v15\n"
"v_mfma_f32_16x16x32_bf16 v[52:55], v[100:103], a[40:43], v[52:55]\n"
"s_add_u32 s93, s62, 135168\n"
"buffer_load_dwordx4 v[96:99], v1, s[24:27], s93 offen offset:0 sc1\n"
"v_mul_f32 v20, v6, v16\n"
"v_mfma_f32_16x16x32_bf16 v[48:51], v[100:103], a[36:39], v[48:51]\n"
"v_mul_f32 v21, v6, v17\n"
"v_mfma_f32_16x16x32_bf16 v[48:51], v[104:107], a[52:55], v[48:51]\n"
"v_fmac_f32 v18, v7, v28\n"
"v_mfma_f32_16x16x32_bf16 v[44:47], v[104:107], a[48:51], v[44:47]\n"
"v_fmac_f32 v19, v7, v29\n"
"v_mfma_f32_16x16x32_bf16 v[44:47], v[100:103], a[32:35], v[44:47]\n"
"s_waitcnt vmcnt(38)\n"
"buffer_load_dwordx4 v[100:103], v1, s[24:27], s93 offen offset:1024 sc1\n"
"v_fmac_f32 v20, v7, v30\n"
"v_mfma_f32_16x16x32_bf16 v[44:47], v[108:111], a[64:67], v[44:47]\n"
"s_waitcnt vmcnt(38)\n"
"v_fmac_f32 v21, v7, v31\n"
"v_mfma_f32_16x16x32_bf16 v[44:47], v[112:115], a[80:83], v[44:47]\n"
"v_cvt_pk_bf16_f32 v18, v18, v19\n"
"v_mfma_f32_16x16x32_bf16 v[48:51], v[112:115], a[84:87], v[48:51]\n"
"v_cvt_pk_bf16_f32 v19, v20, v21\n"
"v_mfma_f32_16x16x32_bf16 v[48:51], v[108:111], a[68:71], v[48:51]\n"
"buffer_load_dwordx4 v[104:107], v1, s[24:27], s93 offen offset:2048 sc1\n"
"v_pk_max_i16 v18, v18, 0\n"
"v_mfma_f32_16x16x32_bf16 v[52:55], v[108:111], a[72:75], v[52:55]\n"
"v_pk_max_i16 v19, v19, 0\n"
"v_mfma_f32_16x16x32_bf16 v[52:55], v[112:115], a[88:91], v[52:55]\n"
"ds_write_b64 v4, v[18:19] offset:32768\n"
"v_mfma_f32_16x16x32_bf16 v[56:59], v[112:115], a[92:95], v[56:59]\n"
"v_mul_f32 v22, v8, v14\n"
"v_mfma_f32_16x16x32_bf16 v[56:59], v[108:111], a[76:79], v[56:59]\n"
"s_waitcnt vmcnt(38)\n"
"buffer_load_dwordx4 v[108:111], v1, s[24:27], s93 offen offset:3072 sc1\n"
"v_mul_f32 v23, v8, v15\n"
"v_mfma_f32_16x16x32_bf16 v[56:59], v[116:119], a[108:111], v[56:59]\n"
"s_waitcnt vmcnt(38)\n"
"v_mul_f32 v24, v8, v16\n"
"v_mfma_f32_16x16x32_bf16 v[56:59], v[120:123], a[124:127], v[56:59]\n"
"v_mul_f32 v25, v8, v17\n"
"v_mfma_f32_16x16x32_bf16 v[52:55], v[120:123], a[120:123], v[52:55]\n"
"v_fmac_f32 v22, v9, v32\n"
"v_mfma_f32_16x16x32_bf16 v[52:55], v[116:119], a[104:107], v[52:55]\n"
"s_add_u32 s93, s62, 139264\n"
"buffer_load_dwordx4 v[112:115], v1, s[24:27], s93 offen offset:0 sc1\n"
"v_fmac_f32 v23, v9, v33\n"
"v_mfma_f32_16x16x32_bf16 v[48:51], v[116:119], a[100:103], v[48:51]\n"
"v_fmac_f32 v24, v9, v34\n"
"v_mfma_f32_16x16x32_bf16 v[48:51], v[120:123], a[116:119], v[48:51]\n"
"v_fmac_f32 v25, v9, v35\n"
"v_mfma_f32_16x16x32_bf16 v[44:47], v[120:123], a[112:115], v[44:47]\n"
"v_cvt_pk_bf16_f32 v22, v22, v23\n"
"v_mfma_f32_16x16x32_bf16 v[44:47], v[116:119], a[96:99], v[44:47]\n"
"s_waitcnt vmcnt(38)\n"
"buffer_load_dwordx4 v[116:119], v1, s[24:27], s93 offen offset:1024 sc1\n"
"v_cvt_pk_bf16_f32 v23, v24, v25\n"
"v_mfma_f32_16x16x32_bf16 v[44:47], v[124:127], a[128:131], v[44:47]\n"
"s_waitcnt vmcnt(38)\n"
"v_pk_max_i16 v22, v22, 0\n"
"v_mfma_f32_16x16x32_bf16 v[44:47], v[128:131], a[144:147], v[44:47]\n"
"v_pk_max_i16 v23, v23, 0\n"
"v_mfma_f32_16x16x32_bf16 v[48:51], v[128:131], a[148:151], v[48:51]\n"
"ds_write_b64 v4, v[22:23] offset:33792\n"
"v_mfma_f32_16x16x32_bf16 v[48:51], v[124:127], a[132:135], v[48:51]\n"
"buffer_load_dwordx4 v[120:123], v1, s[24:27], s93 offen offset:2048 sc1\n"
"v_mul_f32 v18, v10, v14\n"
"v_mfma_f32_16x16x32_bf16 v[52:55], v[124:127], a[136:139], v[52:55]\n"
"v_mul_f32 v19, v10, v15\n"
"v_mfma_f32_16x16x32_bf16 v[52:55], v[128:131], a[152:155], v[52:55]\n"
"v_mul_f32 v20, v10, v16\n"
"v_mfma_f32_16x16x32_bf16 v[56:59], v[128:131], a[156:159], v[56:59]\n"
"v_mul_f32 v21, v10, v17\n"
"v_mfma_f32_16x16x32_bf16 v[56:59], v[124:127], a[140:143], v[56:59]\n"
"s_waitcnt vmcnt(38)\n"
"buffer_load_dwordx4 v[124:127], v1, s[24:27], s93 offen offset:3072 sc1\n"
"v_fmac_f32 v18, v11, v36\n"
"v_mfma_f32_16x16x32_bf16 v[56:59], v[132:135], a[172:175], v[56:59]\n"
"s_waitcnt vmcnt(38)\n"
"v_fmac_f32 v19, v11, v37\n"
"v_mfma_f32_16x16x32_bf16 v[56:59], v[136:139], a[188:191], v[56:59]\n"
"v_fmac_f32 v20, v11, v38\n"
"v_mfma_f32_16x16x32_bf16 v[52:55], v[136:139], a[184:187], v[52:55]\n"
"v_fmac_f32 v21, v11, v39\n"
"v_mfma_f32_16x16x32_bf16 v[52:55], v[132:135], a[168:171], v[52:55]\n"
"s_add_u32 s93, s62, 143360\n"
"buffer_load_dwordx4 v[128:131], v1, s[24:27], s93 offen offset:0 sc1\n"
"v_cvt_pk_bf16_f32 v18, v18, v19\n"
"v_mfma_f32_16x16x32_bf16 v[48:51], v[132:135], a[164:167], v[48:51]\n"
"v_cvt_pk_bf16_f32 v19, v20, v21\n"
"v_mfma_f32_16x16x32_bf16 v[48:51], v[136:139], a[180:183], v[48:51]\n"
"v_pk_max_i16 v18, v18, 0\n"
"v_mfma_f32_16x16x32_bf16 v[44:47], v[136:139], a[176:179], v[44:47]\n"
"v_pk_max_i16 v19, v19, 0\n"
"v_mfma_f32_16x16x32_bf16 v[44:47], v[132:135], a[160:163], v[44:47]\n"
"s_waitcnt vmcnt(38)\n"
"buffer_load_dwordx4 v[132:135], v1, s[24:27], s93 offen offset:1024 sc1\n"
"ds_write_b64 v4, v[18:19] offset:34816\n"
"v_mfma_f32_16x16x32_bf16 v[44:47], v[140:143], a[192:195], v[44:47]\n"
"s_waitcnt vmcnt(38)\n"
"v_mul_f32 v22, v12, v14\n"
"v_mfma_f32_16x16x32_bf16 v[44:47], v[144:147], a[208:211], v[44:47]\n"
"v_mul_f32 v23, v12, v15\n"
"v_mfma_f32_16x16x32_bf16 v[48:51], v[144:147], a[212:215], v[48:51]\n"
"v_mul_f32 v24, v12, v16\n"
"v_mfma_f32_16x16x32_bf16 v[48:51], v[140:143], a[196:199], v[48:51]\n"
"buffer_load_dwordx4 v[136:139], v1, s[24:27], s93 offen offset:2048 sc1\n"
"v_mul_f32 v25, v12, v17\n"
"v_mfma_f32_16x16x32_bf16 v[52:55], v[140:143], a[200:203], v[52:55]\n"
"v_fmac_f32 v22, v13, v40\n"
"v_mfma_f32_16x16x32_bf16 v[52:55], v[144:147], a[216:219], v[52:55]\n"
"v_fmac_f32 v23, v13, v41\n"
"v_mfma_f32_16x16x32_bf16 v[56:59], v[144:147], a[220:223], v[56:59]\n"
"v_fmac_f32 v24, v13, v42\n"
"v_mfma_f32_16x16x32_bf16 v[56:59], v[140:143], a[204:207], v[56:59]\n"
"s_waitcnt vmcnt(38)\n"
"buffer_load_dwordx4 v[140:143], v1, s[24:27], s93 offen offset:3072 sc1\n"
"v_fmac_f32 v25, v13, v43\n"
"v_mfma_f32_16x16x32_bf16 v[56:59], v[148:151], a[236:239], v[56:59]\n"
"s_waitcnt vmcnt(38)\n"
"v_cvt_pk_bf16_f32 v22, v22, v23\n"
"v_mfma_f32_16x16x32_bf16 v[56:59], v[152:155], a[252:255], v[56:59]\n"
"v_cvt_pk_bf16_f32 v23, v24, v25\n"
"v_mfma_f32_16x16x32_bf16 v[52:55], v[152:155], a[248:251], v[52:55]\n"
"v_pk_max_i16 v22, v22, 0\n"
"v_mfma_f32_16x16x32_bf16 v[52:55], v[148:151], a[232:235], v[52:55]\n"
"s_add_u32 s93, s62, 147456\n"
"buffer_load_dwordx4 v[144:147], v1, s[24:27], s93 offen offset:0 sc1\n"
"v_pk_max_i16 v23, v23, 0\n"
"v_mfma_f32_16x16x32_bf16 v[48:51], v[148:151], a[228:231], v[48:51]\n"
"ds_write_b64 v4, v[22:23] offset:35840\n"
"v_mfma_f32_16x16x32_bf16 v[48:51], v[152:155], a[244:247], v[48:51]\n"
"v_mfma_f32_16x16x32_bf16 v[44:47], v[152:155], a[240:243], v[44:47]\n"
"v_mfma_f32_16x16x32_bf16 v[44:47], v[148:151], a[224:227], v[44:47]\n"
"s_waitcnt vmcnt(38)\n"
"buffer_load_dwordx4 v[148:151], v1, s[24:27], s93 offen offset:1024 sc1\n"
"v_mfma_f32_16x16x32_bf16 v[44:47], v[156:159], v[224:227], v[44:47]\n"
"s_waitcnt vmcnt(38)\n"
"v_mfma_f32_16x16x32_bf16 v[44:47], v[160:163], v[240:243], v[44:47]\n"
"v_mfma_f32_16x16x32_bf16 v[48:51], v[160:163], v[244:247], v[48:51]\n"
"v_mfma_f32_16x16x32_bf16 v[48:51], v[156:159], v[228:231], v[48:51]\n"
"buffer_load_dwordx4 v[152:155], v1, s[24:27], s93 offen offset:2048 sc1\n"
"v_mfma_f32_16x16x32_bf16 v[52:55], v[156:159], v[232:235], v[52:55]\n"
"v_mfma_f32_16x16x32_bf16 v[52:55], v[160:163], v[248:251], v[52:55]\n"
"v_mfma_f32_16x16x32_bf16 v[56:59], v[160:163], v[252:255], v[56:59]\n"
"v_mfma_f32_16x16x32_bf16 v[56:59], v[156:159], v[236:239], v[56:59]\n"
"s_waitcnt vmcnt(38)\n"
"buffer_load_dwordx4 v[156:159], v1, s[24:27], s93 offen offset:3072 sc1\n"
"v_mfma_f32_16x16x32_bf16 v[28:31], v[164:167], a[0:3], 0\n"
"s_waitcnt vmcnt(38)\n"
"v_mfma_f32_16x16x32_bf16 v[28:31], v[168:171], a[16:19], v[28:31]\n"
"v_mfma_f32_16x16x32_bf16 v[32:35], v[168:171], a[20:23], 0\n"
"v_mfma_f32_16x16x32_bf16 v[32:35], v[164:167], a[4:7], v[32:35]\n"
"s_add_u32 s93, s62, 151552\n"
"buffer_load_dwordx4 v[160:163], v1, s[24:27], s93 offen offset:0 sc1\n"
"v_mfma_f32_16x16x32_bf16 v[36:39], v[164:167], a[8:11], 0\n"
"ds_read_b128 v[14:17], v5 offset:1280\n"
"v_mfma_f32_16x16x32_bf16 v[36:39], v[168:171], a[24:27], v[36:39]\n"
"v_mfma_f32_16x16x32_bf16 v[40:43], v[168:171], a[28:31], 0\n"
"v_mfma_f32_16x16x32_bf16 v[40:43], v[164:167], a[12:15], v[40:43]\n"
"s_waitcnt vmcnt(38)\n"
"buffer_load_dwordx4 v[164:167], v1, s[24:27], s93 offen offset:1024 sc1\n"
"v_mfma_f32_16x16x32_bf16 v[40:43], v[172:175], a[44:47], v[40:43]\n"
"s_waitcnt vmcnt(38)\n"
"v_mfma_f32_16x16x32_bf16 v[40:43], v[176:179], a[60:63], v[40:43]\n"
"s_waitcnt lgkmcnt(0)\n"
"v_mul_f32 v18, v6, v14\n"
"v_mfma_f32_16x16x32_bf16 v[36:39], v[176:179], a[56:59], v[36:39]\n"
"v_mul_f32 v19, v6, v15\n"
"v_mfma_f32_16x16x32_bf16 v[36:39], v[172:175], a[40:43], v[36:39]\n"
"buffer_load_dwordx4 v[168:171], v1, s[24:27], s93 offen offset:2048 sc1\n"
"v_mul_f32 v20, v6, v16\n"
"v_mfma_f32_16x16x32_bf16 v[32:35], v[172:175], a[36:39], v[32:35]\n"
"v_mul_f32 v21, v6, v17\n"
"v_mfma_f32_16x16x32_bf16 v[32:35], v[176:179], a[52:55], v[32:35]\n"
"v_fmac_f32 v18, v7, v44\n"
"v_mfma_f32_16x16x32_bf16 v[28:31], v[176:179], a[48:51], v[28:31]\n"
"v_fmac_f32 v19, v7, v45\n"
"v_mfma_f32_16x16x32_bf16 v[28:31], v[172:175], a[32:35], v[28:31]\n"
"s_waitcnt vmcnt(38)\n"
"buffer_load_dwordx4 v[172:175], v1, s[24:27], s93 offen offset:3072 sc1\n"
"v_fmac_f32 v20, v7, v46\n"
"v_mfma_f32_16x16x32_bf16 v[28:31], v[180:183], a[64:67], v[28:31]\n"
"s_waitcnt vmcnt(38)\n"
"v_fmac_f32 v21, v7, v47\n"
"v_mfma_f32_16x16x32_bf16 v[28:31], v[184:187], a[80:83], v[28:31]\n"
"v_cvt_pk_bf16_f32 v18, v18, v19\n"
"v_mfma_f32_16x16x32_bf16 v[32:35], v[184:187], a[84:87], v[32:35]\n"
"v_cvt_pk_bf16_f32 v19, v20, v21\n"
"v_mfma_f32_16x16x32_bf16 v[32:35], v[180:183], a[68:71], v[32:35]\n"
"s_add_u32 s93, s62, 155648\n"
"buffer_load_dwordx4 v[176:179], v1, s[24:27], s93 offen offset:0 sc1\n"
"v_pk_max_i16 v18, v18, 0\n"
"v_mfma_f32_16x16x32_bf16 v[36:39], v[180:183], a[72:75], v[36:39]\n"
"v_pk_max_i16 v19, v19, 0\n"
"v_mfma_f32_16x16x32_bf16 v[36:39], v[184:187], a[88:91], v[36:39]\n"
"ds_write_b64 v4, v[18:19] offset:40960\n"
"v_mfma_f32_16x16x32_bf16 v[40:43], v[184:187], a[92:95], v[40:43]\n"
"v_mul_f32 v22, v8, v14\n"
"v_mfma_f32_16x16x32_bf16 v[40:43], v[180:183], a[76:79], v[40:43]\n"
"s_waitcnt vmcnt(38)\n"
"buffer_load_dwordx4 v[180:183], v1, s[24:27], s93 offen offset:1024 sc1\n"
"v_mul_f32 v23, v8, v15\n"
"v_mfma_f32_16x16x32_bf16 v[40:43], v[188:191], a[108:111], v[40:43]\n"
"s_waitcnt vmcnt(38)\n"
"v_mul_f32 v24, v8, v16\n"
"v_mfma_f32_16x16x32_bf16 v[40:43], v[192:195], a[124:127], v[40:43]\n"
"v_mul_f32 v25, v8, v17\n"
"v_mfma_f32_16x16x32_bf16 v[36:39], v[192:195], a[120:123], v[36:39]\n"
"v_fmac_f32 v22, v9, v48\n"
"v_mfma_f32_16x16x32_bf16 v[36:39], v[188:191], a[104:107], v[36:39]\n"
"buffer_load_dwordx4 v[184:187], v1, s[24:27], s93 offen offset:2048 sc1\n"
"v_fmac_f32 v23, v9, v49\n"
"v_mfma_f32_16x16x32_bf16 v[32:35], v[188:191], a[100:103], v[32:35]\n"
"v_fmac_f32 v24, v9, v50\n"
"v_mfma_f32_16x16x32_bf16 v[32:35], v[192:195], a[116:119], v[32:35]\n"
"v_fmac_f32 v25, v9, v51\n"
"v_mfma_f32_16x16x32_bf16 v[28:31], v[192:195], a[112:115], v[28:31]\n"
"v_cvt_pk_bf16_f32 v22, v22, v23\n"
"v_mfma_f32_16x16x32_bf16 v[28:31], v[188:191], a[96:99], v[28:31]\n"
"s_waitcnt vmcnt(38)\n"
"buffer_load_dwordx4 v[188:191], v1, s[24:27], s93 offen offset:3072 sc1\n"
"v_cvt_pk_bf16_f32 v23, v24, v25\n"
"v_mfma_f32_16x16x32_bf16 v[28:31], v[196:199], a[128:131], v[28:31]\n"
"s_waitcnt vmcnt(38)\n"
"v_pk_max_i16 v22, v22, 0\n"
"v_mfma_f32_16x16x32_bf16 v[28:31], v[200:203], a[144:147], v[28:31]\n"
"v_pk_max_i16 v23, v23, 0\n"
"v_mfma_f32_16x16x32_bf16 v[32:35], v[200:203], a[148:151], v[32:35]\n"
"ds_write_b64 v4, v[22:23] offset:41984\n"
"v_mfma_f32_16x16x32_bf16 v[32:35], v[196:199], a[132:135], v[32:35]\n"
"s_add_u32 s93, s62, 159744\n"
"buffer_load_dwordx4 v[192:195], v1, s[24:27], s93 offen offset:0 sc1\n"
"v_mul_f32 v18, v10, v14\n"
"v_mfma_f32_16x16x32_bf16 v[36:39], v[196:199], a[136:139], v[36:39]\n"
"v_mul_f32 v19, v10, v15\n"
"v_mfma_f32_16x16x32_bf16 v[36:39], v[200:203], a[152:155], v[36:39]\n"
"v_mul_f32 v20, v10, v16\n"
"v_mfma_f32_16x16x32_bf16 v[40:43], v[200:203], a[156:159], v[40:43]\n"
"v_mul_f32 v21, v10, v17\n"
"v_mfma_f32_16x16x32_bf16 v[40:43], v[196:199], a[140:143], v[40:43]\n"
"s_waitcnt vmcnt(38)\n"
"buffer_load_dwordx4 v[196:199], v1, s[24:27], s93 offen offset:1024 sc1\n"
"v_fmac_f32 v18, v11, v52\n"
"v_mfma_f32_16x16x32_bf16 v[40:43], v[204:207], a[172:175], v[40:43]\n"
"s_waitcnt vmcnt(38)\n"
"v_fmac_f32 v19, v11, v53\n"
"v_mfma_f32_16x16x32_bf16 v[40:43], v[208:211], a[188:191], v[40:43]\n"
"v_fmac_f32 v20, v11, v54\n"
"v_mfma_f32_16x16x32_bf16 v[36:39], v[208:211], a[184:187], v[36:39]\n"
"v_fmac_f32 v21, v11, v55\n"
"v_mfma_f32_16x16x32_bf16 v[36:39], v[204:207], a[168:171], v[36:39]\n"
"buffer_load_dwordx4 v[200:203], v1, s[24:27], s93 offen offset:2048 sc1\n"
"v_cvt_pk_bf16_f32 v18, v18, v19\n"
"v_mfma_f32_16x16x32_bf16 v[32:35], v[204:207], a[164:167], v[32:35]\n"
"v_cvt_pk_bf16_f32 v19, v20, v21\n"
"v_mfma_f32_16x16x32_bf16 v[32:35], v[208:211], a[180:183], v[32:35]\n"
"v_pk_max_i16 v18, v18, 0\n"
"v_mfma_f32_16x16x32_bf16 v[28:31], v[208:211], a[176:179], v[28:31]\n"
"v_pk_max_i16 v19, v19, 0\n"
"v_mfma_f32_16x16x32_bf16 v[28:31], v[204:207], a[160:163], v[28:31]\n"
"s_waitcnt vmcnt(38)\n"
"buffer_load_dwordx4 v[204:207], v1, s[24:27], s93 offen offset:3072 sc1\n"
"ds_write_b64 v4, v[18:19] offset:43008\n"
"v_mfma_f32_16x16x32_bf16 v[28:31], v[212:215], a[192:195], v[28:31]\n"
"s_waitcnt vmcnt(38)\n"
"v_mul_f32 v22, v12, v14\n"
"v_mfma_f32_16x16x32_bf16 v[28:31], v[216:219], a[208:211], v[28:31]\n"
"v_mul_f32 v23, v12, v15\n"
"v_mfma_f32_16x16x32_bf16 v[32:35], v[216:219], a[212:215], v[32:35]\n"
"v_mul_f32 v24, v12, v16\n"
"v_mfma_f32_16x16x32_bf16 v[32:35], v[212:215], a[196:199], v[32:35]\n"
"s_add_u32 s93, s62, 163840\n"
"buffer_load_dwordx4 v[208:211], v1, s[24:27], s93 offen offset:0 sc1\n"
"v_mul_f32 v25, v12, v17\n"
"v_mfma_f32_16x16x32_bf16 v[36:39], v[212:215], a[200:203], v[36:39]\n"
"v_fmac_f32 v22, v13, v56\n"
"v_mfma_f32_16x16x32_bf16 v[36:39], v[216:219], a[216:219], v[36:39]\n"
"v_fmac_f32 v23, v13, v57\n"
"v_mfma_f32_16x16x32_bf16 v[40:43], v[216:219], a[220:223], v[40:43]\n"
"v_fmac_f32 v24, v13, v58\n"
"v_mfma_f32_16x16x32_bf16 v[40:43], v[212:215], a[204:207], v[40:43]\n"
"s_waitcnt vmcnt(38)\n"
"buffer_load_dwordx4 v[212:215], v1, s[24:27], s93 offen offset:1024 sc1\n"
"v_fmac_f32 v25, v13, v59\n"
"v_mfma_f32_16x16x32_bf16 v[40:43], v[220:223], a[236:239], v[40:43]\n"
"s_waitcnt vmcnt(38)\n"
"v_cvt_pk_bf16_f32 v22, v22, v23\n"
"v_mfma_f32_16x16x32_bf16 v[40:43], v[60:63], a[252:255], v[40:43]\n"
"v_cvt_pk_bf16_f32 v23, v24, v25\n"
"v_mfma_f32_16x16x32_bf16 v[36:39], v[60:63], a[248:251], v[36:39]\n"
"v_pk_max_i16 v22, v22, 0\n"
"v_mfma_f32_16x16x32_bf16 v[36:39], v[220:223], a[232:235], v[36:39]\n"
"buffer_load_dwordx4 v[216:219], v1, s[24:27], s93 offen offset:2048 sc1\n"
"v_pk_max_i16 v23, v23, 0\n"
"v_mfma_f32_16x16x32_bf16 v[32:35], v[220:223], a[228:231], v[32:35]\n"
"ds_write_b64 v4, v[22:23] offset:44032\n"
"v_mfma_f32_16x16x32_bf16 v[32:35], v[60:63], a[244:247], v[32:35]\n"
"v_mfma_f32_16x16x32_bf16 v[28:31], v[60:63], a[240:243], v[28:31]\n"
"v_mfma_f32_16x16x32_bf16 v[28:31], v[220:223], a[224:227], v[28:31]\n"
"s_waitcnt vmcnt(38)\n"
"buffer_load_dwordx4 v[220:223], v1, s[24:27], s93 offen offset:3072 sc1\n"
"v_mfma_f32_16x16x32_bf16 v[28:31], v[64:67], v[224:227], v[28:31]\n"
"s_waitcnt vmcnt(38)\n"
"v_mfma_f32_16x16x32_bf16 v[28:31], v[68:71], v[240:243], v[28:31]\n"
"v_mfma_f32_16x16x32_bf16 v[32:35], v[68:71], v[244:247], v[32:35]\n"
"v_mfma_f32_16x16x32_bf16 v[32:35], v[64:67], v[228:231], v[32:35]\n"
"s_add_u32 s93, s62, 167936\n"
"buffer_load_dwordx4 v[60:63], v1, s[24:27], s93 offen offset:0 sc1\n"
"v_mfma_f32_16x16x32_bf16 v[36:39], v[64:67], v[232:235], v[36:39]\n"
"v_mfma_f32_16x16x32_bf16 v[36:39], v[68:71], v[248:251], v[36:39]\n"
"v_mfma_f32_16x16x32_bf16 v[40:43], v[68:71], v[252:255], v[40:43]\n"
"v_mfma_f32_16x16x32_bf16 v[40:43], v[64:67], v[236:239], v[40:43]\n"
"s_waitcnt vmcnt(38)\n"
"buffer_load_dwordx4 v[64:67], v1, s[24:27], s93 offen offset:1024 sc1\n"
"v_mfma_f32_16x16x32_bf16 v[44:47], v[72:75], a[0:3], 0\n"
"s_waitcnt vmcnt(38)\n"
"v_mfma_f32_16x16x32_bf16 v[44:47], v[76:79], a[16:19], v[44:47]\n"
"v_mfma_f32_16x16x32_bf16 v[48:51], v[76:79], a[20:23], 0\n"
"v_mfma_f32_16x16x32_bf16 v[48:51], v[72:75], a[4:7], v[48:51]\n"
"buffer_load_dwordx4 v[68:71], v1, s[24:27], s93 offen offset:2048 sc1\n"
"v_mfma_f32_16x16x32_bf16 v[52:55], v[72:75], a[8:11], 0\n"
"ds_read_b128 v[14:17], v5 offset:1536\n"
"v_mfma_f32_16x16x32_bf16 v[52:55], v[76:79], a[24:27], v[52:55]\n"
"v_mfma_f32_16x16x32_bf16 v[56:59], v[76:79], a[28:31], 0\n"
"v_mfma_f32_16x16x32_bf16 v[56:59], v[72:75], a[12:15], v[56:59]\n"
"s_waitcnt vmcnt(38)\n"
"buffer_load_dwordx4 v[72:75], v1, s[24:27], s93 offen offset:3072 sc1\n"
"v_mfma_f32_16x16x32_bf16 v[56:59], v[80:83], a[44:47], v[56:59]\n"
"s_waitcnt vmcnt(38)\n"
"v_mfma_f32_16x16x32_bf16 v[56:59], v[84:87], a[60:63], v[56:59]\n"
"s_waitcnt lgkmcnt(0)\n"
"v_mul_f32 v18, v6, v14\n"
"v_mfma_f32_16x16x32_bf16 v[52:55], v[84:87], a[56:59], v[52:55]\n"
"v_mul_f32 v19, v6, v15\n"
"v_mfma_f32_16x16x32_bf16 v[52:55], v[80:83], a[40:43], v[52:55]\n"
"s_add_u32 s93, s62, 172032\n"
"buffer_load_dwordx4 v[76:79], v1, s[24:27], s93 offen offset:0 sc1\n"
"v_mul_f32 v20, v6, v16\n"
"v_mfma_f32_16x16x32_bf16 v[48:51], v[80:83], a[36:39], v[48:51]\n"
"v_mul_f32 v21, v6, v17\n"
"v_mfma_f32_16x16x32_bf16 v[48:51], v[84:87], a[52:55], v[48:51]\n"
"v_fmac_f32 v18, v7, v28\n"
"v_mfma_f32_16x16x32_bf16 v[44:47], v[84:87], a[48:51], v[44:47]\n"
"v_fmac_f32 v19, v7, v29\n"
"v_mfma_f32_16x16x32_bf16 v[44:47], v[80:83], a[32:35], v[44:47]\n"
"s_waitcnt vmcnt(38)\n"
"buffer_load_dwordx4 v[80:83], v1, s[24:27], s93 offen offset:1024 sc1\n"
"v_fmac_f32 v20, v7, v30\n"
"v_mfma_f32_16x16x32_bf16 v[44:47], v[88:91], a[64:67], v[44:47]\n"
"s_waitcnt vmcnt(38)\n"
"v_fmac_f32 v21, v7, v31\n"
"v_mfma_f32_16x16x32_bf16 v[44:47], v[92:95], a[80:83], v[44:47]\n"
"v_cvt_pk_bf16_f32 v18, v18, v19\n"
"v_mfma_f32_16x16x32_bf16 v[48:51], v[92:95], a[84:87], v[48:51]\n"
"v_cvt_pk_bf16_f32 v19, v20, v21\n"
"v_mfma_f32_16x16x32_bf16 v[48:51], v[88:91], a[68:71], v[48:51]\n"
"buffer_load_dwordx4 v[84:87], v1, s[24:27], s93 offen offset:2048 sc1\n"
"v_pk_max_i16 v18, v18, 0\n"
"v_mfma_f32_16x16x32_bf16 v[52:55], v[88:91], a[72:75], v[52:55]\n"
"v_pk_max_i16 v19, v19, 0\n"
"v_mfma_f32_16x16x32_bf16 v[52:55], v[92:95], a[88:91], v[52:55]\n"
"ds_write_b64 v4, v[18:19] offset:49152\n"
"v_mfma_f32_16x16x32_bf16 v[56:59], v[92:95], a[92:95], v[56:59]\n"
"v_mul_f32 v22, v8, v14\n"
"v_mfma_f32_16x16x32_bf16 v[56:59], v[88:91], a[76:79], v[56:59]\n"
"s_waitcnt vmcnt(38)\n"
"buffer_load_dwordx4 v[88:91], v1, s[24:27], s93 offen offset:3072 sc1\n"
"v_mul_f32 v23, v8, v15\n"
"v_mfma_f32_16x16x32_bf16 v[56:59], v[96:99], a[108:111], v[56:59]\n"
"s_waitcnt vmcnt(38)\n"
"v_mul_f32 v24, v8, v16\n"
"v_mfma_f32_16x16x32_bf16 v[56:59], v[100:103], a[124:127], v[56:59]\n"
"v_mul_f32 v25, v8, v17\n"
"v_mfma_f32_16x16x32_bf16 v[52:55], v[100:103], a[120:123], v[52:55]\n"
"v_fmac_f32 v22, v9, v32\n"
"v_mfma_f32_16x16x32_bf16 v[52:55], v[96:99], a[104:107], v[52:55]\n"
"s_add_u32 s93, s62, 176128\n"
"buffer_load_dwordx4 v[92:95], v1, s[24:27], s93 offen offset:0 sc1\n"
"v_fmac_f32 v23, v9, v33\n"
"v_mfma_f32_16x16x32_bf16 v[48:51], v[96:99], a[100:103], v[48:51]\n"
"v_fmac_f32 v24, v9, v34\n"
"v_mfma_f32_16x16x32_bf16 v[48:51], v[100:103], a[116:119], v[48:51]\n"
"v_fmac_f32 v25, v9, v35\n"
"v_mfma_f32_16x16x32_bf16 v[44:47], v[100:103], a[112:115], v[44:47]\n"
"v_cvt_pk_bf16_f32 v22, v22, v23\n"
"v_mfma_f32_16x16x32_bf16 v[44:47], v[96:99], a[96:99], v[44:47]\n"
"s_waitcnt vmcnt(38)\n"
"buffer_load_dwordx4 v[96:99], v1, s[24:27], s93 offen offset:1024 sc1\n"
"v_cvt_pk_bf16_f32 v23, v24, v25\n"
"v_mfma_f32_16x16x32_bf16 v[44:47], v[104:107], a[128:131], v[44:47]\n"
"s_waitcnt vmcnt(38)\n"
"v_pk_max_i16 v22, v22, 0\n"
"v_mfma_f32_16x16x32_bf16 v[44:47], v[108:111], a[144:147], v[44:47]\n"
"v_pk_max_i16 v23, v23, 0\n"
"v_mfma_f32_16x16x32_bf16 v[48:51], v[108:111], a[148:151], v[48:51]\n"
"ds_write_b64 v4, v[22:23] offset:50176\n"
"v_mfma_f32_16x16x32_bf16 v[48:51], v[104:107], a[132:135], v[48:51]\n"
"buffer_load_dwordx4 v[100:103], v1, s[24:27], s93 offen offset:2048 sc1\n"
"v_mul_f32 v18, v10, v14\n"
"v_mfma_f32_16x16x32_bf16 v[52:55], v[104:107], a[136:139], v[52:55]\n"
"v_mul_f32 v19, v10, v15\n"
"v_mfma_f32_16x16x32_bf16 v[52:55], v[108:111], a[152:155], v[52:55]\n"
".Lp2_1_%=:\n"
"s_cbranch_execz .Lp2_2_%=\n"
"v_mul_f32 v20, v10, v16\n"
"v_mfma_f32_16x16x32_bf16 v[56:59], v[108:111], a[156:159], v[56:59]\n"
"v_mul_f32 v21, v10, v17\n"
"v_mfma_f32_16x16x32_bf16 v[56:59], v[104:107], a[140:143], v[56:59]\n"
"s_waitcnt vmcnt(38)\n"
"buffer_load_dwordx4 v[104:107], v1, s[24:27], s93 offen offset:3072 sc1\n"
"v_fmac_f32 v18, v11, v36\n"
"v_mfma_f32_16x16x32_bf16 v[56:59], v[112:115], a[172:175], v[56:59]\n"
"s_waitcnt vmcnt(38)\n"
"v_fmac_f32 v19, v11, v37\n"
"v_mfma_f32_16x16x32_bf16 v[56:59], v[116:119], a[188:191], v[56:59]\n"
"v_fmac_f32 v20, v11, v38\n"
"v_mfma_f32_16x16x32_bf16 v[52:55], v[116:119], a[184:187], v[52:55]\n"
"v_fmac_f32 v21, v11, v39\n"
"v_mfma_f32_16x16x32_bf16 v[52:55], v[112:115], a[168:171], v[52:55]\n"
"s_add_u32 s93, s62, 180224\n"
"buffer_load_dwordx4 v[108:111], v1, s[24:27], s93 offen offset:0 sc1\n"
"v_cvt_pk_bf16_f32 v18, v18, v19\n"
"v_mfma_f32_16x16x32_bf16 v[48:51], v[112:115], a[164:167], v[48:51]\n"
"v_cvt_pk_bf16_f32 v19, v20, v21\n"
"v_mfma_f32_16x16x32_bf16 v[48:51], v[116:119], a[180:183], v[48:51]\n"
"v_pk_max_i16 v18, v18, 0\n"
"v_mfma_f32_16x16x32_bf16 v[44:47], v[116:119], a[176:179], v[44:47]\n"
"v_pk_max_i16 v19, v19, 0\n"
"v_mfma_f32_16x16x32_bf16 v[44:47], v[112:115], a[160:163], v[44:47]\n"
"s_waitcnt vmcnt(38)\n"
"buffer_load_dwordx4 v[112:115], v1, s[24:27], s93 offen offset:1024 sc1\n"
"ds_write_b64 v4, v[18:19] offset:51200\n"
"v_mfma_f32_16x16x32_bf16 v[44:47], v[120:123], a[192:195], v[44:47]\n"
"s_waitcnt vmcnt(38)\n"
"v_mul_f32 v22, v12, v14\n"
"v_mfma_f32_16x16x32_bf16 v[44:47], v[124:127], a[208:211], v[44:47]\n"
"v_mul_f32 v23, v12, v15\n"
"v_mfma_f32_16x16x32_bf16 v[48:51], v[124:127], a[212:215], v[48:51]\n"
"v_mul_f32 v24, v12, v16\n"
"v_mfma_f32_16x16x32_bf16 v[48:51], v[120:123], a[196:199], v[48:51]\n"
"buffer_load_dwordx4 v[116:119], v1, s[24:27], s93 offen offset:2048 sc1\n"
"v_mul_f32 v25, v12, v17\n"
"v_mfma_f32_16x16x32_bf16 v[52:55], v[120:123], a[200:203], v[52:55]\n"
"v_fmac_f32 v22, v13, v40\n"
"v_mfma_f32_16x16x32_bf16 v[52:55], v[124:127], a[216:219], v[52:55]\n"
"v_fmac_f32 v23, v13, v41\n"
"v_mfma_f32_16x16x32_bf16 v[56:59], v[124:127], a[220:223], v[56:59]\n"
"v_fmac_f32 v24, v13, v42\n"
"v_mfma_f32_16x16x32_bf16 v[56:59], v[120:123], a[204:207], v[56:59]\n"
"s_waitcnt vmcnt(38)\n"
"buffer_load_dwordx4 v[120:123], v1, s[24:27], s93 offen offset:3072 sc1\n"
"v_fmac_f32 v25, v13, v43\n"
"v_mfma_f32_16x16x32_bf16 v[56:59], v[128:131], a[236:239], v[56:59]\n"
"s_waitcnt vmcnt(38)\n"
"v_cvt_pk_bf16_f32 v22, v22, v23\n"
"v_mfma_f32_16x16x32_bf16 v[56:59], v[132:135], a[252:255], v[56:59]\n"
"v_cvt_pk_bf16_f32 v23, v24, v25\n"
"v_mfma_f32_16x16x32_bf16 v[52:55], v[132:135], a[248:251], v[52:55]\n"
"v_pk_max_i16 v22, v22, 0\n"
"v_mfma_f32_16x16x32_bf16 v[52:55], v[128:131], a[232:235], v[52:55]\n"
"s_add_u32 s93, s62, 184320\n"
"buffer_load_dwordx4 v[124:127], v1, s[24:27], s93 offen offset:0 sc1\n"
"v_pk_max_i16 v23, v23, 0\n"
"v_mfma_f32_16x16x32_bf16 v[48:51], v[128:131], a[228:231], v[48:51]\n"
"ds_write_b64 v4, v[22:23] offset:52224\n"
"v_mfma_f32_16x16x32_bf16 v[48:51], v[132:135], a[244:247], v[48:51]\n"
"v_mfma_f32_16x16x32_bf16 v[44:47], v[132:135], a[240:243], v[44:47]\n"
"v_mfma_f32_16x16x32_bf16 v[44:47], v[128:131], a[224:227], v[44:47]\n"
"s_waitcnt vmcnt(38)\n"
"buffer_load_dwordx4 v[128:131], v1, s[24:27], s93 offen offset:1024 sc1\n"
"v_mfma_f32_16x16x32_bf16 v[44:47], v[136:139], v[224:227], v[44:47]\n"
"s_waitcnt vmcnt(38)\n"
"v_mfma_f32_16x16x32_bf16 v[44:47], v[140:143], v[240:243], v[44:47]\n"
"v_mfma_f32_16x16x32_bf16 v[48:51], v[140:143], v[244:247], v[48:51]\n"
"v_mfma_f32_16x16x32_bf16 v[48:51], v[136:139], v[228:231], v[48:51]\n"
"buffer_load_dwordx4 v[132:135], v1, s[24:27], s93 offen offset:2048 sc1\n"
"v_mfma_f32_16x16x32_bf16 v[52:55], v[136:139], v[232:235], v[52:55]\n"
"v_mfma_f32_16x16x32_bf16 v[52:55], v[140:143], v[248:251], v[52:55]\n"
"v_mfma_f32_16x16x32_bf16 v[56:59], v[140:143], v[252:255], v[56:59]\n"
"v_mfma_f32_16x16x32_bf16 v[56:59], v[136:139], v[236:239], v[56:59]\n"
"s_waitcnt vmcnt(38)\n"
"buffer_load_dwordx4 v[136:139], v1, s[24:27], s93 offen offset:3072 sc1\n"
"s_waitcnt lgkmcnt(0)\n"
"s_barrier\n"
"v_mfma_f32_16x16x32_bf16 v[28:31], v[144:147], a[0:3], 0\n"
"s_waitcnt vmcnt(38)\n"
"ds_read_b128 a[0:3], v2 offset:8192\n"
"v_mfma_f32_16x16x32_bf16 v[28:31], v[148:151], a[16:19], v[28:31]\n"
"ds_read_b128 a[16:19], v2 offset:12288\n"
"v_mfma_f32_16x16x32_bf16 v[32:35], v[148:151], a[20:23], 0\n"
"ds_read_b128 a[20:23], v2 offset:13312\n"
"v_mfma_f32_16x16x32_bf16 v[32:35], v[144:147], a[4:7], v[32:35]\n"
"s_add_u32 s93, s62, 188416\n"
"buffer_load_dwordx4 v[140:143], v1, s[24:27], s93 offen offset:0 sc1\n"
"ds_read_b128 a[4:7], v2 offset:9216\n"
"v_mfma_f32_16x16x32_bf16 v[36:39], v[144:147], a[8:11], 0\n"
"ds_read_b128 a[8:11], v2 offset:10240\n"
"ds_read_b128 v[14:17], v5 offset:1792\n"
"v_mfma_f32_16x16x32_bf16 v[36:39], v[148:151], a[24:27], v[36:39]\n"
"ds_read_b128 a[24:27], v2 offset:14336\n"
"v_mfma_f32_16x16x32_bf16 v[40:43], v[148:151], a[28:31], 0\n"
"ds_read_b128 a[28:31], v2 offset:15360\n"
"v_mfma_f32_16x16x32_bf16 v[40:43], v[144:147], a[12:15], v[40:43]\n"
"s_waitcnt vmcnt(38)\n"
"buffer_load_dwordx4 v[144:147], v1, s[24:27], s93 offen offset:1024 sc1\n"
"ds_read_b128 a[12:15], v2 offset:11264\n"
"v_mfma_f32_16x16x32_bf16 v[40:43], v[152:155], a[44:47], v[40:43]\n"
"s_waitcnt vmcnt(38)\n"
"ds_read_b128 a[44:47], v2 offset:19456\n"
"v_mfma_f32_16x16x32_bf16 v[40:43], v[156:159], a[60:63], v[40:43]\n"
"ds_read_b128 a[60:63], v2 offset:23552\n"
"v_mfma_f32_16x16x32_bf16 v[36:39], v[156:159], a[56:59], v[36:39]\n"
"ds_read_b128 a[56:59], v2 offset:22528\n"
"s_waitcnt lgkmcnt(6)\n"
"v_mul_f32 v18, v6, v14\n"
"v_mfma_f32_16x16x32_bf16 v[36:39], v[152:155], a[40:43], v[36:39]\n"
"buffer_load_dwordx4 v[148:151], v1, s[24:27], s93 offen offset:2048 sc1\n"
"ds_read_b128 a[40:43], v2 offset:18432\n"
"v_mfma_f32_16x16x32_bf16 v[32:35], v[152:155], a[36:39], v[32:35]\n"
"ds_read_b128 a[36:39], v2 offset:17408\n"
"v_mul_f32 v19, v6, v15\n"
"v_mfma_f32_16x16x32_bf16 v[32:35], v[156:159], a[52:55], v[32:35]\n"
"ds_read_b128 a[52:55], v2 offset:21504\n"
"v_mul_f32 v20, v6, v16\n"
"v_mfma_f32_16x16x32_bf16 v[28:31], v[156:159], a[48:51], v[28:31]\n"
"ds_read_b128 a[48:51], v2 offset:20480\n"
"v_mul_f32 v21, v6, v17\n"
"v_mfma_f32_16x16x32_bf16 v[28:31], v[152:155], a[32:35], v[28:31]\n"
"s_waitcnt vmcnt(38)\n"
"buffer_load_dwordx4 v[152:155], v1, s[24:27], s93 offen offset:3072 sc1\n"
"ds_read_b128 a[32:35], v2 offset:16384\n"
"v_mfma_f32_16x16x32_bf16 v[28:31], v[160:163], a[64:67], v[28:31]\n"
"s_waitcnt vmcnt(38)\n"
"ds_read_b128 a[64:67], v2 offset:24576\n"
"v_fmac_f32 v18, v7, v44\n"
"v_mfma_f32_16x16x32_bf16 v[28:31], v[164:167], a[80:83], v[28:31]\n"
"ds_read_b128 a[80:83], v2 offset:28672\n"
"v_fmac_f32 v19, v7, v45\n"
"v_mfma_f32_16x16x32_bf16 v[32:35], v[164:167], a[84:87], v[32:35]\n"
"ds_read_b128 a[84:87], v2 offset:29696\n"
"v_fmac_f32 v20, v7, v46\n"
"v_mfma_f32_16x16x32_bf16 v[32:35], v[160:163], a[68:71], v[32:35]\n"
"s_add_u32 s93, s62, 192512\n"
"buffer_load_dwordx4 v[156:159], v1, s[24:27], s93 offen offset:0 sc1\n"
"ds_read_b128 a[68:71], v2 offset:25600\n"
"v_mfma_f32_16x16x32_bf16 v[36:39], v[160:163], a[72:75], v[36:39]\n"
"s_waitcnt lgkmcnt(14)\n"
"ds_read_b128 a[72:75], v2 offset:26624\n"
"v_fmac_f32 v21, v7, v47\n"
"v_mfma_f32_16x16x32_bf16 v[36:39], v[164:167], a[88:91], v[36:39]\n"
"s_waitcnt lgkmcnt(14)\n"
"ds_read_b128 a[88:91], v2 offset:30720\n"
"v_cvt_pk_bf16_f32 v18, v18, v19\n"
"v_mfma_f32_16x16x32_bf16 v[40:43], v[164:167], a[92:95], v[40:43]\n"
"s_waitcnt lgkmcnt(14)\n"
"ds_read_b128 a[92:95], v2 offset:31744\n"
"v_cvt_pk_bf16_f32 v19, v20, v21\n"
"v_mfma_f32_16x16x32_bf16 v[40:43], v[160:163], a[76:79], v[40:43]\n"
"s_waitcnt vmcnt(38)\n"
"buffer_load_dwordx4 v[160:163], v1, s[24:27], s93 offen offset:1024 sc1\n"
"s_waitcnt lgkmcnt(14)\n"
"ds_read_b128 a[76:79], v2 offset:27648\n"
"v_mfma_f32_16x16x32_bf16 v[40:43], v[168:171], a[108:111], v[40:43]\n"
"s_waitcnt vmcnt(38)\n"
"s_waitcnt lgkmcnt(14)\n"
"ds_read_b128 a[108:111], v2 offset:35840\n"
"v_pk_max_i16 v18, v18, 0\n"
"v_mfma_f32_16x16x32_bf16 v[40:43], v[172:175], a[124:127], v[40:43]\n"
"s_waitcnt lgkmcnt(14)\n"
"ds_read_b128 a[124:127], v2 offset:39936\n"
"v_pk_max_i16 v19, v19, 0\n"
"v_mfma_f32_16x16x32_bf16 v[36:39], v[172:175], a[120:123], v[36:39]\n"
"s_waitcnt lgkmcnt(14)\n"
"ds_read_b128 a[120:123], v2 offset:38912\n"
"s_waitcnt lgkmcnt(14)\n"
"ds_write_b64 v4, v[18:19] offset:57344\n"
"v_mfma_f32_16x16x32_bf16 v[36:39], v[168:171], a[104:107], v[36:39]\n"
"buffer_load_dwordx4 v[164:167], v1, s[24:27], s93 offen offset:2048 sc1\n"
"s_waitcnt lgkmcnt(14)\n"
"ds_read_b128 a[104:107], v2 offset:34816\n"
"v_mfma_f32_16x16x32_bf16 v[32:35], v[168:171], a[100:103], v[32:35]\n"
"s_waitcnt lgkmcnt(14)\n"
"ds_read_b128 a[100:103], v2 offset:33792\n"
"v_mul_f32 v22, v8, v14\n"
"v_mfma_f32_16x16x32_bf16 v[32:35], v[172:175], a[116:119], v[32:35]\n"
"s_waitcnt lgkmcnt(14)\n"
"ds_read_b128 a[116:119], v2 offset:37888\n"
"v_mul_f32 v23, v8, v15\n"
"v_mfma_f32_16x16x32_bf16 v[28:31], v[172:175], a[112:115], v[28:31]\n"
"s_waitcnt lgkmcnt(14)\n"
"ds_read_b128 a[112:115], v2 offset:36864\n"
"v_mul_f32 v24, v8, v16\n"
"v_mfma_f32_16x16x32_bf16 v[28:31], v[168:171], a[96:99], v[28:31]\n"
"s_waitcnt vmcnt(38)\n"
"buffer_load_dwordx4 v[168:171], v1, s[24:27], s93 offen offset:3072 sc1\n"
"s_waitcnt lgkmcnt(14)\n"
"ds_read_b128 a[96:99], v2 offset:32768\n"
"v_mfma_f32_16x16x32_bf16 v[28:31], v[176:179], a[128:131], v[28:31]\n"
"s_waitcnt vmcnt(38)\n"
"s_waitcnt lgkmcnt(14)\n"
"ds_read_b128 a[128:131], v2 offset:40960\n"
"v_mul_f32 v25, v8, v17\n"
"v_mfma_f32_16x16x32_bf16 v[28:31], v[180:183], a[144:147], v[28:31]\n"
"s_waitcnt lgkmcnt(14)\n"
"ds_read_b128 a[144:147], v2 offset:45056\n"
"v_fmac_f32 v22, v9, v48\n"
"v_mfma_f32_16x16x32_bf16 v[32:35], v[180:183], a[148:151], v[32:35]\n"
"s_waitcnt lgkmcnt(14)\n"
"ds_read_b128 a[148:151], v2 offset:46080\n"
"v_fmac_f32 v23, v9, v49\n"
"v_mfma_f32_16x16x32_bf16 v[32:35], v[176:179], a[132:135], v[32:35]\n"
"s_add_u32 s93, s62, 196608\n"
"buffer_load_dwordx4 v[172:175], v1, s[24:27], s93 offen offset:0 sc1\n"
"s_waitcnt lgkmcnt(14)\n"
"ds_read_b128 a[132:135], v2 offset:41984\n"
"v_mfma_f32_16x16x32_bf16 v[36:39], v[176:179], a[136:139], v[36:39]\n"
"s_waitcnt lgkmcnt(14)\n"
"ds_read_b128 a[136:139], v2 offset:43008\n"
"v_fmac_f32 v24, v9, v50\n"
"v_mfma_f32_16x16x32_bf16 v[36:39], v[180:183], a[152:155], v[36:39]\n"
"s_waitcnt lgkmcnt(14)\n"
"ds_read_b128 a[152:155], v2 offset:47104\n"
"v_fmac_f32 v25, v9, v51\n"
"v_mfma_f32_16x16x32_bf16 v[40:43], v[180:183], a[156:159], v[40:43]\n"
"s_waitcnt lgkmcnt(14)\n"
"ds_read_b128 a[156:159], v2 offset:48128\n"
"v_cvt_pk_bf16_f32 v22, v22, v23\n"
"v_mfma_f32_16x16x32_bf16 v[40:43], v[176:179], a[140:143], v[40:43]\n"
"s_waitcnt vmcnt(38)\n"
"buffer_load_dwordx4 v[176:179], v1, s[24:27], s93 offen offset:1024 sc1\n"
"s_waitcnt lgkmcnt(14)\n"
"ds_read_b128 a[140:143], v2 offset:44032\n"
"v_mfma_f32_16x16x32_bf16 v[40:43], v[184:187], a[172:175], v[40:43]\n"
"s_waitcnt vmcnt(38)\n"
"s_waitcnt lgkmcnt(14)\n"
"ds_read_b128 a[172:175], v2 offset:52224\n"
"v_cvt_pk_bf16_f32 v23, v24, v25\n"
"v_mfma_f32_16x16x32_bf16 v[40:43], v[188:191], a[188:191], v[40:43]\n"
"s_waitcnt lgkmcnt(14)\n"
"ds_read_b128 a[188:191], v2 offset:56320\n"
"v_pk_max_i16 v22, v22, 0\n"
"v_mfma_f32_16x16x32_bf16 v[36:39], v[188:191], a[184:187], v[36:39]\n"
"s_waitcnt lgkmcnt(14)\n"
"ds_read_b128 a[184:187], v2 offset:55296\n"
"v_pk_max_i16 v23, v23, 0\n"
"v_mfma_f32_16x16x32_bf16 v[36:39], v[184:187], a[168:171], v[36:39]\n"
"buffer_load_dwordx4 v[180:183], v1, s[24:27], s93 offen offset:2048 sc1\n"
"s_waitcnt lgkmcnt(14)\n"
"ds_read_b128 a[168:171], v2 offset:51200\n"
"v_mfma_f32_16x16x32_bf16 v[32:35], v[184:187], a[164:167], v[32:35]\n"
"s_waitcnt lgkmcnt(14)\n"
"ds_read_b128 a[164:167], v2 offset:50176\n"
"s_waitcnt lgkmcnt(14)\n"
"ds_write_b64 v4, v[22:23] offset:58368\n"
"v_mfma_f32_16x16x32_bf16 v[32:35], v[188:191], a[180:183], v[32:35]\n"
"s_waitcnt lgkmcnt(14)\n"
"ds_read_b128 a[180:183], v2 offset:54272\n"
"v_mul_f32 v18, v10, v14\n"
"v_mfma_f32_16x16x32_bf16 v[28:31], v[188:191], a[176:179], v[28:31]\n"
"s_waitcnt lgkmcnt(14)\n"
"ds_read_b128 a[176:179], v2 offset:53248\n"
"v_mul_f32 v19, v10, v15\n"
"v_mfma_f32_16x16x32_bf16 v[28:31], v[184:187], a[160:163], v[28:31]\n"
"s_waitcnt vmcnt(38)\n"
"buffer_load_dwordx4 v[184:187], v1, s[24:27], s93 offen offset:3072 sc1\n"
"s_waitcnt lgkmcnt(14)\n"
"ds_read_b128 a[160:163], v2 offset:49152\n"
"v_mfma_f32_16x16x32_bf16 v[28:31], v[192:195], a[192:195], v[28:31]\n"
"s_waitcnt vmcnt(38)\n"
"s_waitcnt lgkmcnt(14)\n"
"ds_read_b128 a[192:195], v2 offset:57344\n"
"v_mul_f32 v20, v10, v16\n"
"v_mfma_f32_16x16x32_bf16 v[28:31], v[196:199], a[208:211], v[28:31]\n"
"s_waitcnt lgkmcnt(14)\n"
"ds_read_b128 a[208:211], v2 offset:61440\n"
"v_mul_f32 v21, v10, v17\n"
"v_mfma_f32_16x16x32_bf16 v[32:35], v[196:199], a[212:215], v[32:35]\n"
"s_waitcnt lgkmcnt(14)\n"
"ds_read_b128 a[212:215], v2 offset:62464\n"
"v_fmac_f32 v18, v11, v52\n"
"v_mfma_f32_16x16x32_bf16 v[32:35], v[192:195], a[196:199], v[32:35]\n"
"s_add_u32 s93, s62, 200704\n"
"buffer_load_dwordx4 v[188:191], v1, s[24:27], s93 offen offset:0 sc1\n"
"s_waitcnt lgkmcnt(14)\n"
"ds_read_b128 a[196:199], v2 offset:58368\n"
"v_mfma_f32_16x16x32_bf16 v[36:39], v[192:195], a[200:203], v[36:39]\n"
"s_waitcnt lgkmcnt(14)\n"
"ds_read_b128 a[200:203], v2 offset:59392\n"
"v_fmac_f32 v19, v11, v53\n"
"v_mfma_f32_16x16x32_bf16 v[36:39], v[196:199], a[216:219], v[36:39]\n"
"s_waitcnt lgkmcnt(14)\n"
"ds_read_b128 a[216:219], v2 offset:63488\n"
"v_fmac_f32 v20, v11, v54\n"
"v_mfma_f32_16x16x32_bf16 v[40:43], v[196:199], a[220:223], v[40:43]\n"
"s_waitcnt lgkmcnt(14)\n"
"ds_read_b128 a[220:223], v2 offset:64512\n"
"v_fmac_f32 v21, v11, v55\n"
"v_mfma_f32_16x16x32_bf16 v[40:43], v[192:195], a[204:207], v[40:43]\n"
"s_waitcnt vmcnt(38)\n"
"buffer_load_dwordx4 v[192:195], v1, s[24:27], s93 offen offset:1024 sc1\n"
"s_waitcnt lgkmcnt(14)\n"
"ds_read_b128 a[204:207], v2 offset:60416\n"
"v_mfma_f32_16x16x32_bf16 v[40:43], v[200:203], a[236:239], v[40:43]\n"
"s_waitcnt vmcnt(38)\n"
"v_cvt_pk_bf16_f32 v18, v18, v19\n"
"v_cvt_pk_bf16_f32 v19, v20, v21\n"
"v_mfma_f32_16x16x32_bf16 v[40:43], v[204:207], a[252:255], v[40:43]\n"
"v_pk_max_i16 v18, v18, 0\n"
"v_pk_max_i16 v19, v19, 0\n"
"v_mfma_f32_16x16x32_bf16 v[36:39], v[204:207], a[248:251], v[36:39]\n"
"s_waitcnt lgkmcnt(14)\n"
"ds_write_b64 v4, v[18:19] offset:59392\n"
"v_mul_f32 v22, v12, v14\n"
"v_mfma_f32_16x16x32_bf16 v[36:39], v[200:203], a[232:235], v[36:39]\n"
"buffer_load_dwordx4 v[196:199], v1, s[24:27], s93 offen offset:2048 sc1\n"
"v_mul_f32 v23, v12, v15\n"
"v_mfma_f32_16x16x32_bf16 v[32:35], v[200:203], a[228:231], v[32:35]\n"
"v_mul_f32 v24, v12, v16\n"
"v_mul_f32 v25, v12, v17\n"
"v_mfma_f32_16x16x32_bf16 v[32:35], v[204:207], a[244:247], v[32:35]\n"
"v_fmac_f32 v22, v13, v56\n"
"v_fmac_f32 v23, v13, v57\n"
"v_mfma_f32_16x16x32_bf16 v[28:31], v[204:207], a[240:243], v[28:31]\n"
"v_fmac_f32 v24, v13, v58\n"
"v_fmac_f32 v25, v13, v59\n"
"v_mfma_f32_16x16x32_bf16 v[28:31], v[200:203], a[224:227], v[28:31]\n"
"s_waitcnt vmcnt(38)\n"
"buffer_load_dwordx4 v[200:203], v1, s[24:27], s93 offen offset:3072 sc1\n"
"v_cvt_pk_bf16_f32 v22, v22, v23\n"
"v_mfma_f32_16x16x32_bf16 v[28:31], v[208:211], v[224:227], v[28:31]\n"
"s_waitcnt vmcnt(38)\n"
"v_cvt_pk_bf16_f32 v23, v24, v25\n"
"v_pk_max_i16 v22, v22, 0\n"
"v_mfma_f32_16x16x32_bf16 v[28:31], v[212:215], v[240:243], v[28:31]\n"
"v_pk_max_i16 v23, v23, 0\n"
"s_waitcnt lgkmcnt(14)\n"
"ds_write_b64 v4, v[22:23] offset:60416\n"
"v_mfma_f32_16x16x32_bf16 v[32:35], v[212:215], v[244:247], v[32:35]\n"
"v_mfma_f32_16x16x32_bf16 v[32:35], v[208:211], v[228:231], v[32:35]\n"
"s_add_u32 s93, s62, 204800\n"
"buffer_load_dwordx4 v[204:207], v1, s[24:27], s93 offen offset:0 sc1\n"
"v_mfma_f32_16x16x32_bf16 v[36:39], v[208:211], v[232:235], v[36:39]\n"
"v_mfma_f32_16x16x32_bf16 v[36:39], v[212:215], v[248:251], v[36:39]\n"
"v_mfma_f32_16x16x32_bf16 v[40:43], v[212:215], v[252:255], v[40:43]\n"
"v_mfma_f32_16x16x32_bf16 v[40:43], v[208:211], v[236:239], v[40:43]\n"
"s_waitcnt vmcnt(38)\n"
"buffer_load_dwordx4 v[208:211], v1, s[24:27], s93 offen offset:1024 sc1\n"
"s_waitcnt lgkmcnt(0)\n"
"s_barrier\n"
"v_mfma_f32_16x16x32_bf16 v[44:47], v[216:219], a[0:3], 0\n"
"s_waitcnt vmcnt(38)\n"
"ds_read_b128 a[236:239], v3 offset:3072\n"
"v_mfma_f32_16x16x32_bf16 v[44:47], v[220:223], a[16:19], v[44:47]\n"
"ds_read_b128 a[252:255], v3 offset:7168\n"
"v_mfma_f32_16x16x32_bf16 v[48:51], v[220:223], a[20:23], 0\n"
"ds_read_b128 a[248:251], v3 offset:6144\n"
"v_mfma_f32_16x16x32_bf16 v[48:51], v[216:219], a[4:7], v[48:51]\n"
"buffer_load_dwordx4 v[212:215], v1, s[24:27], s93 offen offset:2048 sc1\n"
"ds_read_b128 a[232:235], v3 offset:2048\n"
"v_mfma_f32_16x16x32_bf16 v[52:55], v[216:219], a[8:11], 0\n"
"ds_read_b128 a[228:231], v3 offset:1024\n"
"ds_read_b128 v[14:17], v5 offset:2048\n"
"v_mfma_f32_16x16x32_bf16 v[52:55], v[220:223], a[24:27], v[52:55]\n"
"ds_read_b128 a[244:247], v3 offset:5120\n"
"v_add_u32 v4, 0x10000, v4\n"
"v_mfma_f32_16x16x32_bf16 v[56:59], v[220:223], a[28:31], 0\n"
"ds_read_b128 a[240:243], v3 offset:4096\n"
"v_mfma_f32_16x16x32_bf16 v[56:59], v[216:219], a[12:15], v[56:59]\n"
"s_waitcnt vmcnt(38)\n"
"buffer_load_dwordx4 v[216:219], v1, s[24:27], s93 offen offset:3072 sc1\n"
"ds_read_b128 a[224:227], v3 offset:0\n"
"v_mfma_f32_16x16x32_bf16 v[56:59], v[60:63], a[44:47], v[56:59]\n"
"s_waitcnt vmcnt(38)\n"
"v_mfma_f32_16x16x32_bf16 v[56:59], v[64:67], a[60:63], v[56:59]\n"
"s_waitcnt lgkmcnt(3)\n"
"v_mul_f32 v18, v6, v14\n"
"v_mfma_f32_16x16x32_bf16 v[52:55], v[64:67], a[56:59], v[52:55]\n"
"v_mul_f32 v19, v6, v15\n"
"v_mul_f32 v20, v6, v16\n"
"v_mfma_f32_16x16x32_bf16 v[52:55], v[60:63], a[40:43], v[52:55]\n"
"s_add_u32 s93, s62, 208896\n"
"buffer_load_dwordx4 v[220:223], v1, s[24:27], s93 offen offset:0 sc1\n"
"v_mul_f32 v21, v6, v17\n"
"v_mfma_f32_16x16x32_bf16 v[48:51], v[60:63], a[36:39], v[48:51]\n"
"v_fmac_f32 v18, v7, v28\n"
"v_fmac_f32 v19, v7, v29\n"
"v_mfma_f32_16x16x32_bf16 v[48:51], v[64:67], a[52:55], v[48:51]\n"
"v_fmac_f32 v20, v7, v30\n"
"v_fmac_f32 v21, v7, v31\n"
"v_mfma_f32_16x16x32_bf16 v[44:47], v[64:67], a[48:51], v[44:47]\n"
"v_cvt_pk_bf16_f32 v18, v18, v19\n"
"v_cvt_pk_bf16_f32 v19, v20, v21\n"
"v_mfma_f32_16x16x32_bf16 v[44:47], v[60:63], a[32:35], v[44:47]\n"
"s_waitcnt vmcnt(38)\n"
"buffer_load_dwordx4 v[60:63], v1, s[24:27], s93 offen offset:1024 sc1\n"
"v_pk_max_i16 v18, v18, 0\n"
"v_mfma_f32_16x16x32_bf16 v[44:47], v[68:71], a[64:67], v[44:47]\n"
"s_waitcnt vmcnt(38)\n"
"v_pk_max_i16 v19, v19, 0\n"
"ds_write_b64 v4, v[18:19] offset:0\n"
"v_mfma_f32_16x16x32_bf16 v[44:47], v[72:75], a[80:83], v[44:47]\n"
"v_mul_f32 v22, v8, v14\n"
"v_mul_f32 v23, v8, v15\n"
"v_mfma_f32_16x16x32_bf16 v[48:51], v[72:75], a[84:87], v[48:51]\n"
"v_mul_f32 v24, v8, v16\n"
"v_mul_f32 v25, v8, v17\n"
"v_mfma_f32_16x16x32_bf16 v[48:51], v[68:71], a[68:71], v[48:51]\n"
"buffer_load_dwordx4 v[64:67], v1, s[24:27], s93 offen offset:2048 sc1\n"
"v_fmac_f32 v22, v9, v32\n"
"v_mfma_f32_16x16x32_bf16 v[52:55], v[68:71], a[72:75], v[52:55]\n"
"v_fmac_f32 v23, v9, v33\n"
"v_fmac_f32 v24, v9, v34\n"
"v_mfma_f32_16x16x32_bf16 v[52:55], v[72:75], a[88:91], v[52:55]\n"
"v_fmac_f32 v25, v9, v35\n"
"v_cvt_pk_bf16_f32 v22, v22, v23\n"
"v_mfma_f32_16x16x32_bf16 v[56:59], v[72:75], a[92:95], v[56:59]\n"
"v_cvt_pk_bf16_f32 v23, v24, v25\n"
"v_pk_max_i16 v22, v22, 0\n"
"v_mfma_f32_16x16x32_bf16 v[56:59], v[68:71], a[76:79], v[56:59]\n"
"s_waitcnt vmcnt(38)\n"
"buffer_load_dwordx4 v[68:71], v1, s[24:27], s93 offen offset:3072 sc1\n"
"v_pk_max_i16 v23, v23, 0\n"
"v_mfma_f32_16x16x32_bf16 v[56:59], v[76:79], a[108:111], v[56:59]\n"
"s_waitcnt vmcnt(38)\n"
"ds_write_b64 v4, v[22:23] offset:1024\n"
"v_mul_f32 v18, v10, v14\n"
"v_mfma_f32_16x16x32_bf16 v[56:59], v[80:83], a[124:127], v[56:59]\n"
"v_mul_f32 v19, v10, v15\n"
"v_mul_f32 v20, v10, v16\n"
"v_mfma_f32_16x16x32_bf16 v[52:55], v[80:83], a[120:123], v[52:55]\n"
"v_mul_f32 v21, v10, v17\n"
"v_fmac_f32 v18, v11, v36\n"
"v_mfma_f32_16x16x32_bf16 v[52:55], v[76:79], a[104:107], v[52:55]\n"
"s_add_u32 s93, s62, 212992\n"
"buffer_load_dwordx4 v[72:75], v1, s[24:27], s93 offen offset:0 sc1\n"
"v_fmac_f32 v19, v11, v37\n"
"v_mfma_f32_16x16x32_bf16 v[48:51], v[76:79], a[100:103], v[48:51]\n"
"v_fmac_f32 v20, v11, v38\n"
"v_fmac_f32 v21, v11, v39\n"
"v_mfma_f32_16x16x32_bf16 v[48:51], v[80:83], a[116:119], v[48:51]\n"
"v_cvt_pk_bf16_f32 v18, v18, v19\n"
"v_cvt_pk_bf16_f32 v19, v20, v21\n"
"v_mfma_f32_16x16x32_bf16 v[44:47], v[80:83], a[112:115], v[44:47]\n"
"v_pk_max_i16 v18, v18, 0\n"
"v_pk_max_i16 v19, v19, 0\n"
"v_mfma_f32_16x16x32_bf16 v[44:47], v[76:79], a[96:99], v[44:47]\n"
"s_waitcnt vmcnt(38)\n"
"buffer_load_dwordx4 v[76:79], v1, s[24:27], s93 offen offset:1024 sc1\n"
"ds_write_b64 v4, v[18:19] offset:2048\n"
"v_mfma_f32_16x16x32_bf16 v[44:47], v[84:87], a[128:131], v[44:47]\n"
"s_waitcnt vmcnt(38)\n"
"v_mul_f32 v22, v12, v14\n"
"v_mul_f32 v23, v12, v15\n"
"v_mfma_f32_16x16x32_bf16 v[44:47], v[88:91], a[144:147], v[44:47]\n"
"v_mul_f32 v24, v12, v16\n"
"v_mul_f32 v25, v12, v17\n"
"v_mfma_f32_16x16x32_bf16 v[48:51], v[88:91], a[148:151], v[48:51]\n"
"v_fmac_f32 v22, v13, v40\n"
"v_fmac_f32 v23, v13, v41\n"
"v_mfma_f32_16x16x32_bf16 v[48:51], v[84:87], a[132:135], v[48:51]\n"
"buffer_load_dwordx4 v[80:83], v1, s[24:27], s93 offen offset:2048 sc1\n"
"v_fmac_f32 v24, v13, v42\n"
"v_mfma_f32_16x16x32_bf16 v[52:55], v[84:87], a[136:139], v[52:55]\n"
"v_fmac_f32 v25, v13, v43\n"
"v_cvt_pk_bf16_f32 v22, v22, v23\n"
"v_mfma_f32_16x16x32_bf16 v[52:55], v[88:91], a[152:155], v[52:55]\n"
"v_cvt_pk_bf16_f32 v23, v24, v25\n"
"v_pk_max_i16 v22, v22, 0\n"
"v_mfma_f32_16x16x32_bf16 v[56:59], v[88:91], a[156:159], v[56:59]\n"
"v_pk_max_i16 v23, v23, 0\n"
"ds_write_b64 v4, v[22:23] offset:3072\n"
"v_mfma_f32_16x16x32_bf16 v[56:59], v[84:87], a[140:143], v[56:59]\n"
"s_waitcnt vmcnt(38)\n"
"buffer_load_dwordx4 v[84:87], v1, s[24:27], s93 offen offset:3072 sc1\n"
"s_waitcnt lgkmcnt(0)\n"
"s_barrier\n"
"v_mfma_f32_16x16x32_bf16 v[56:59], v[92:95], a[172:175], v[56:59]\n"
"s_waitcnt vmcnt(38)\n"
"ds_read_b128 v[224:227], v3 offset:8192\n"
"v_mfma_f32_16x16x32_bf16 v[56:59], v[96:99], a[188:191], v[56:59]\n"
"ds_read_b128 v[240:243], v3 offset:12288\n"
"v_mfma_f32_16x16x32_bf16 v[52:55], v[96:99], a[184:187], v[52:55]\n"
"ds_read_b128 v[244:247], v3 offset:13312\n"
"v_mfma_f32_16x16x32_bf16 v[52:55], v[92:95], a[168:171], v[52:55]\n"
"s_add_u32 s93, s62, 217088\n"
"buffer_load_dwordx4 v[88:91], v1, s[24:27], s93 offen offset:0 sc1\n"
"ds_read_b128 v[228:231], v3 offset:9216\n"
"v_mfma_f32_16x16x32_bf16 v[48:51], v[92:95], a[164:167], v[48:51]\n"
"ds_read_b128 v[232:235], v3 offset:10240\n"
"v_mfma_f32_16x16x32_bf16 v[48:51], v[96:99], a[180:183], v[48:51]\n"
"ds_read_b128 v[248:251], v3 offset:14336\n"
"v_mfma_f32_16x16x32_bf16 v[44:47], v[96:99], a[176:179], v[44:47]\n"
"ds_read_b128 v[252:255], v3 offset:15360\n"
"v_mfma_f32_16x16x32_bf16 v[44:47], v[92:95], a[160:163], v[44:47]\n"
"s_waitcnt vmcnt(38)\n"
"buffer_load_dwordx4 v[92:95], v1, s[24:27], s93 offen offset:1024 sc1\n"
"ds_read_b128 v[236:239], v3 offset:11264\n"
"v_mfma_f32_16x16x32_bf16 v[44:47], v[100:103], a[192:195], v[44:47]\n"
"s_waitcnt vmcnt(38)\n"
"v_mfma_f32_16x16x32_bf16 v[44:47], v[104:107], a[208:211], v[44:47]\n"
"v_mfma_f32_16x16x32_bf16 v[48:51], v[104:107], a[212:215], v[48:51]\n"
"v_mfma_f32_16x16x32_bf16 v[48:51], v[100:103], a[196:199], v[48:51]\n"
"buffer_load_dwordx4 v[96:99], v1, s[24:27], s93 offen offset:2048 sc1\n"
"v_mfma_f32_16x16x32_bf16 v[52:55], v[100:103], a[200:203], v[52:55]\n"
"v_mfma_f32_16x16x32_bf16 v[52:55], v[104:107], a[216:219], v[52:55]\n"
"v_mfma_f32_16x16x32_bf16 v[56:59], v[104:107], a[220:223], v[56:59]\n"
"v_mfma_f32_16x16x32_bf16 v[56:59], v[100:103], a[204:207], v[56:59]\n"
"s_waitcnt vmcnt(38)\n"
"buffer_load_dwordx4 v[100:103], v1, s[24:27], s93 offen offset:3072 sc1\n"
"v_mfma_f32_16x16x32_bf16 v[56:59], v[108:111], a[236:239], v[56:59]\n"
"s_waitcnt vmcnt(38)\n"
"v_mfma_f32_16x16x32_bf16 v[56:59], v[112:115], a[252:255], v[56:59]\n"
"v_mfma_f32_16x16x32_bf16 v[52:55], v[112:115], a[248:251], v[52:55]\n"
"v_mfma_f32_16x16x32_bf16 v[52:55], v[108:111], a[232:235], v[52:55]\n"
"s_add_u32 s93, s62, 221184\n"
"buffer_load_dwordx4 v[104:107], v1, s[24:27], s93 offen offset:0 sc1\n"
"v_mfma_f32_16x16x32_bf16 v[48:51], v[108:111], a[228:231], v[48:51]\n"
"v_mfma_f32_16x16x32_bf16 v[48:51], v[112:115], a[244:247], v[48:51]\n"
"v_mfma_f32_16x16x32_bf16 v[44:47], v[112:115], a[240:243], v[44:47]\n"
"v_mfma_f32_16x16x32_bf16 v[44:47], v[108:111], a[224:227], v[44:47]\n"
"s_waitcnt vmcnt(38) lgkmcnt(7)\n"
"buffer_load_dwordx4 v[108:111], v1, s[24:27], s93 offen offset:1024 sc1\n"
"v_mfma_f32_16x16x32_bf16 v[44:47], v[116:119], v[224:227], v[44:47]\n"
"s_waitcnt vmcnt(38) lgkmcnt(6)\n"
"v_mfma_f32_16x16x32_bf16 v[44:47], v[120:123], v[240:243], v[44:47]\n"
"s_waitcnt lgkmcnt(5)\n"
"v_mfma_f32_16x16x32_bf16 v[48:51], v[120:123], v[244:247], v[48:51]\n"
"s_waitcnt lgkmcnt(4)\n"
"v_mfma_f32_16x16x32_bf16 v[48:51], v[116:119], v[228:231], v[48:51]\n"
"s_waitcnt lgkmcnt(3)\n"
"buffer_load_dwordx4 v[112:115], v1, s[24:27], s93 offen offset:2048 sc1\n"
"v_mfma_f32_16x16x32_bf16 v[52:55], v[116:119], v[232:235], v[52:55]\n"
"s_waitcnt lgkmcnt(2)\n"
"v_mfma_f32_16x16x32_bf16 v[52:55], v[120:123], v[248:251], v[52:55]\n"
"s_waitcnt lgkmcnt(1)\n"
"v_mfma_f32_16x16x32_bf16 v[56:59], v[120:123], v[252:255], v[56:59]\n"
"s_waitcnt lgkmcnt(0)\n"
"v_mfma_f32_16x16x32_bf16 v[56:59], v[116:119], v[236:239], v[56:59]\n"
"s_waitcnt vmcnt(38)\n"
"buffer_load_dwordx4 v[116:119], v1, s[24:27], s93 offen offset:3072 sc1\n"
"s_barrier\n"
"v_mfma_f32_16x16x32_bf16 v[28:31], v[124:127], a[0:3], 0\n"
"s_waitcnt vmcnt(38)\n"
"v_mfma_f32_16x16x32_bf16 v[28:31], v[128:131], a[16:19], v[28:31]\n"
"v_mfma_f32_16x16x32_bf16 v[32:35], v[128:131], a[20:23], 0\n"
"v_mfma_f32_16x16x32_bf16 v[32:35], v[124:127], a[4:7], v[32:35]\n"
"s_add_u32 s93, s62, 225280\n"
"buffer_load_dwordx4 v[120:123], v1, s[24:27], s93 offen offset:0 sc1\n"
"v_mfma_f32_16x16x32_bf16 v[36:39], v[124:127], a[8:11], 0\n"
"v_cvt_pk_bf16_f32 v18, v44, v45\n"
"v_mfma_f32_16x16x32_bf16 v[36:39], v[128:131], a[24:27], v[36:39]\n"
"v_cvt_pk_bf16_f32 v19, v46, v47\n"
"v_mfma_f32_16x16x32_bf16 v[40:43], v[128:131], a[28:31], 0\n"
"v_cvt_pk_bf16_f32 v20, v48, v49\n"
"v_mfma_f32_16x16x32_bf16 v[40:43], v[124:127], a[12:15], v[40:43]\n"
"s_waitcnt vmcnt(38)\n"
"buffer_load_dwordx4 v[124:127], v1, s[24:27], s93 offen offset:1024 sc1\n"
"v_cvt_pk_bf16_f32 v21, v50, v51\n"
"v_mfma_f32_16x16x32_bf16 v[40:43], v[132:135], a[44:47], v[40:43]\n"
"s_waitcnt vmcnt(38)\n"
"s_add_u32 s65, s63, s67\n"
"buffer_store_dwordx4 v[18:21], v1, s[24:27], s65 offen sc1\n"
"v_mfma_f32_16x16x32_bf16 v[40:43], v[136:139], a[60:63], v[40:43]\n"
"v_cvt_pk_bf16_f32 v22, v52, v53\n"
"v_mfma_f32_16x16x32_bf16 v[36:39], v[136:139], a[56:59], v[36:39]\n"
"v_cvt_pk_bf16_f32 v23, v54, v55\n"
"v_mfma_f32_16x16x32_bf16 v[36:39], v[132:135], a[40:43], v[36:39]\n"
"buffer_load_dwordx4 v[128:131], v1, s[24:27], s93 offen offset:2048 sc1\n"
"v_cvt_pk_bf16_f32 v24, v56, v57\n"
"v_mfma_f32_16x16x32_bf16 v[32:35], v[132:135], a[36:39], v[32:35]\n"
"v_cvt_pk_bf16_f32 v25, v58, v59\n"
"v_mfma_f32_16x16x32_bf16 v[32:35], v[136:139], a[52:55], v[32:35]\n"
"s_add_u32 s65, s65, 36864\n"
"buffer_store_dwordx4 v[22:25], v1, s[24:27], s65 offen sc1\n"
"v_mfma_f32_16x16x32_bf16 v[28:31], v[136:139], a[48:51], v[28:31]\n"
"s_add_u32 s67, s60, 2\n"
"s_and_b32 s67, s67, 7\n"
"s_lshl_b32 s67, s67, 12\n"
"v_mfma_f32_16x16x32_bf16 v[28:31], v[132:135], a[32:35], v[28:31]\n"
"s_waitcnt vmcnt(40)\n"
"buffer_load_dwordx4 v[132:135], v1, s[24:27], s93 offen offset:3072 sc1\n"
"v_mfma_f32_16x16x32_bf16 v[28:31], v[140:143], a[64:67], v[28:31]\n"
"s_waitcnt vmcnt(40)\n"
"s_lshr_b32 s33, s67, 10\n"
"s_add_u32 s33, s33, s28\n"
"s_mul_i32 s34, s33, 29\n"
"s_lshr_b32 s34, s34, 8\n"
"v_mfma_f32_16x16x32_bf16 v[28:31], v[144:147], a[80:83], v[28:31]\n"
"v_mfma_f32_16x16x32_bf16 v[32:35], v[144:147], a[84:87], v[32:35]\n"
"s_mul_i32 s35, s34, 9\n"
"s_sub_u32 s33, s33, s35\n"
"v_mfma_f32_16x16x32_bf16 v[32:35], v[140:143], a[68:71], v[32:35]\n"
"s_add_u32 s93, s62, 229376\n"
"buffer_load_dwordx4 v[136:139], v1, s[24:27], s93 offen offset:0 sc1\n"
"v_mfma_f32_16x16x32_bf16 v[36:39], v[140:143], a[72:75], v[36:39]\n"
"s_lshl_b32 s31, s34, 3\n"
"s_add_u32 s35, s33, s31\n"
"s_add_u32 s34, s34, 32\n"
"v_mfma_f32_16x16x32_bf16 v[36:39], v[144:147], a[88:91], v[36:39]\n"
"v_mfma_f32_16x16x32_bf16 v[40:43], v[144:147], a[92:95], v[40:43]\n"
"s_cmp_eq_u32 s33, 8\n"
"s_cselect_b32 s35, s34, s35\n"
"s_lshr_b32 s33, s35, 1\n"
"v_mfma_f32_16x16x32_bf16 v[40:43], v[140:143], a[76:79], v[40:43]\n"
"s_waitcnt vmcnt(40)\n"
"buffer_load_dwordx4 v[140:143], v1, s[24:27], s93 offen offset:1024 sc1\n"
"s_barrier\n"
"v_mfma_f32_16x16x32_bf16 v[40:43], v[148:151], a[108:111], v[40:43]\n"
"s_waitcnt vmcnt(40)\n"
"s_lshl_b32 s33, s33, 12\n"
"v_add_u32 v6, s33, v1\n"
"v_mfma_f32_16x16x32_bf16 v[40:43], v[152:155], a[124:127], v[40:43]\n"
"v_mfma_f32_16x16x32_bf16 v[36:39], v[152:155], a[120:123], v[36:39]\n"
"s_bitcmp1_b32 s35, 0\n"
"s_cselect_b32 s33, 0, -1\n"
"s_cselect_b32 s34, -1, 0\n"
"v_mfma_f32_16x16x32_bf16 v[36:39], v[148:151], a[104:107], v[36:39]\n"
"buffer_load_dwordx4 v[144:147], v1, s[24:27], s93 offen offset:2048 sc1\n"
"v_mfma_f32_16x16x32_bf16 v[32:35], v[148:151], a[100:103], v[32:35]\n"
".Lp0_2_%=:\n"
"s_cbranch_execz .Lp0_3_%=\n"
"v_mfma_f32_16x16x32_bf16 v[32:35], v[152:155], a[116:119], v[32:35]\n"
"v_mfma_f32_16x16x32_bf16 v[28:31], v[152:155], a[112:115], v[28:31]\n"
"ds_read_b128 v[44:47], v6 offset:0\n"
"v_mfma_f32_16x16x32_bf16 v[28:31], v[148:151], a[96:99], v[28:31]\n"
"s_waitcnt vmcnt(40)\n"
"buffer_load_dwordx4 v[148:151], v1, s[24:27], s93 offen offset:3072 sc1\n"
"ds_read_b128 v[48:51], v6 offset:1024\n"
"v_mfma_f32_16x16x32_bf16 v[28:31], v[156:159], a[128:131], v[28:31]\n"
"s_waitcnt vmcnt(40)\n"
"ds_read_b128 v[52:55], v6 offset:2048\n"
"v_mfma_f32_16x16x32_bf16 v[28:31], v[160:163], a[144:147], v[28:31]\n"
"ds_read_b128 v[56:59], v6 offset:3072\n"
"v_mfma_f32_16x16x32_bf16 v[32:35], v[160:163], a[148:151], v[32:35]\n"
"v_and_b32 v14, s33, v26\n"
"v_mfma_f32_16x16x32_bf16 v[32:35], v[156:159], a[132:135], v[32:35]\n"
"v_and_b32 v15, s33, v27\n"
"v_mfma_f32_16x16x32_bf16 v[36:39], v[156:159], a[136:139], v[36:39]\n"
"v_and_b32 v16, s34, v26\n"
"v_mfma_f32_16x16x32_bf16 v[36:39], v[160:163], a[152:155], v[36:39]\n"
"v_and_b32 v17, s34, v27\n"
"v_mfma_f32_16x16x32_bf16 v[40:43], v[160:163], a[156:159], v[40:43]\n"
"v_mfma_f32_16x16x32_bf16 v[40:43], v[156:159], a[140:143], v[40:43]\n"
"s_waitcnt vmcnt(39)\n"
"s_add_u32 s93, s62, 233472\n"
"buffer_load_dwordx4 v[152:155], v1, s[24:27], s93 offen offset:0 sc1\n"
"v_mfma_f32_16x16x32_bf16 v[40:43], v[164:167], a[172:175], v[40:43]\n"
"s_waitcnt vmcnt(39)\n"
"v_mfma_f32_16x16x32_bf16 v[40:43], v[168:171], a[188:191], v[40:43]\n"
"v_mfma_f32_16x16x32_bf16 v[36:39], v[168:171], a[184:187], v[36:39]\n"
"v_mfma_f32_16x16x32_bf16 v[36:39], v[164:167], a[168:171], v[36:39]\n"
"buffer_load_dwordx4 v[156:159], v1, s[24:27], s93 offen offset:1024 sc1\n"
"v_mfma_f32_16x16x32_bf16 v[32:35], v[164:167], a[164:167], v[32:35]\n"
"v_mfma_f32_16x16x32_bf16 v[32:35], v[168:171], a[180:183], v[32:35]\n"
"v_mfma_f32_16x16x32_bf16 v[28:31], v[168:171], a[176:179], v[28:31]\n"
"s_waitcnt lgkmcnt(3)\n"
"v_mfma_f32_16x16x32_bf16 v[28:31], v[14:17], v[44:47], v[28:31]\n"
"v_mfma_f32_16x16x32_bf16 v[28:31], v[164:167], a[160:163], v[28:31]\n"
"s_waitcnt vmcnt(39)\n"
"buffer_load_dwordx4 v[160:163], v1, s[24:27], s93 offen offset:2048 sc1\n"
"s_barrier\n"
"v_mfma_f32_16x16x32_bf16 v[28:31], v[172:175], a[192:195], v[28:31]\n"
"s_waitcnt vmcnt(39)\n"
"s_waitcnt lgkmcnt(2)\n"
"v_mfma_f32_16x16x32_bf16 v[32:35], v[14:17], v[48:51], v[32:35]\n"
"v_mfma_f32_16x16x32_bf16 v[28:31], v[176:179], a[208:211], v[28:31]\n"
"v_mfma_f32_16x16x32_bf16 v[32:35], v[176:179], a[212:215], v[32:35]\n"
"s_waitcnt lgkmcnt(1)\n"
"v_mfma_f32_16x16x32_bf16 v[36:39], v[14:17], v[52:55], v[36:39]\n"
"v_mfma_f32_16x16x32_bf16 v[32:35], v[172:175], a[196:199], v[32:35]\n"
"v_mfma_f32_16x16x32_bf16 v[36:39], v[172:175], a[200:203], v[36:39]\n"
"s_waitcnt lgkmcnt(0)\n"
"v_mfma_f32_16x16x32_bf16 v[40:43], v[14:17], v[56:59], v[40:43]\n"
"v_mfma_f32_16x16x32_bf16 v[36:39], v[176:179], a[216:219], v[36:39]\n"
"v_mfma_f32_16x16x32_bf16 v[40:43], v[176:179], a[220:223], v[40:43]\n"
"v_mov_b32 v4, s96\n"
"v_mfma_f32_16x16x32_bf16 v[40:43], v[172:175], a[204:207], v[40:43]\n"
"s_waitcnt vmcnt(38)\n"
"buffer_load_dwordx4 v[164:167], v1, s[24:27], s93 offen offset:3072 sc1\n"
"v_mov_b32 v5, s97\n"
"v_mfma_f32_16x16x32_bf16 v[40:43], v[180:183], a[236:239], v[40:43]\n"
"s_waitcnt vmcnt(38)\n"
"v_mov_b32 v8, 0\n"
"v_mfma_f32_16x16x32_bf16 v[40:43], v[184:187], a[252:255], v[40:43]\n"
"v_mov_b32 v9, 0\n"
"v_mfma_f32_16x16x32_bf16 v[36:39], v[184:187], a[248:251], v[36:39]\n"
"v_mov_b32 v10, v26\n"
"v_mfma_f32_16x16x32_bf16 v[36:39], v[180:183], a[232:235], v[36:39]\n"
"s_add_u32 s93, s62, 237568\n"
"buffer_load_dwordx4 v[168:171], v1, s[24:27], s93 offen offset:0 sc1\n"
"v_mov_b32 v11, v27\n"
"v_mfma_f32_16x16x32_bf16 v[32:35], v[180:183], a[228:231], v[32:35]\n"
"v_mov_b32 v12, 0\n"
"v_mfma_f32_16x16x32_bf16 v[32:35], v[184:187], a[244:247], v[32:35]\n"
"v_mov_b32 v13, 0\n"
"v_mfma_f32_16x16x32_bf16 v[28:31], v[184:187], a[240:243], v[28:31]\n"
"v_mfma_f32_16x16x32_bf16 v[28:31], v[180:183], a[224:227], v[28:31]\n"
"s_waitcnt vmcnt(38)\n"
"buffer_load_dwordx4 v[172:175], v1, s[24:27], s93 offen offset:1024 sc1\n"
"v_mfma_f32_16x16x32_bf16 v[28:31], v[188:191], v[224:227], v[28:31]\n"
"s_waitcnt vmcnt(38)\n"
"v_mfma_f32_16x16x32_bf16 v[28:31], v[192:195], v[240:243], v[28:31]\n"
"v_mfma_f32_16x16x32_bf16 v[32:35], v[192:195], v[244:247], v[32:35]\n"
"v_mfma_f32_16x16x32_bf16 v[32:35], v[188:191], v[228:231], v[32:35]\n"
"buffer_load_dwordx4 v[176:179], v1, s[24:27], s93 offen offset:2048 sc1\n"
"v_mfma_f32_16x16x32_bf16 v[36:39], v[188:191], v[232:235], v[36:39]\n"
"s_add_u32 s33, s60, 1\n"
"s_and_b32 s33, s33, 7\n"
"s_mul_i32 s33, s33, 512\n"
"s_add_u32 s33, s33, s54\n"
"s_waitcnt vmcnt(12)\n"
"s_mov_b64 exec, 1\n"
"buffer_store_dwordx2 v[4:5], off, s[24:27], s33 offset:0 sc1\n"
"s_mov_b64 exec, -1\n"
"v_mfma_f32_16x16x32_bf16 v[36:39], v[192:195], v[248:251], v[36:39]\n"
"v_mfma_f32_16x16x32_bf16 v[40:43], v[192:195], v[252:255], v[40:43]\n"
"v_mfma_f32_16x16x32_bf16 v[40:43], v[188:191], v[236:239], v[40:43]\n"
"s_barrier\n"
"v_mfma_f32_16x16x32_bf16 v[44:47], v[196:199], a[0:3], 0\n"
"v_mfma_f32_16x16x32_bf16 v[44:47], v[200:203], a[16:19], v[44:47]\n"
"v_mfma_f32_16x16x32_bf16 v[48:51], v[200:203], a[20:23], 0\n"
"v_mfma_f32_16x16x32_bf16 v[48:51], v[196:199], a[4:7], v[48:51]\n"
"buffer_load_dwordx4 v[180:183], v1, s[24:27], s93 offen offset:3072 sc1\n"
"v_mfma_f32_16x16x32_bf16 v[52:55], v[196:199], a[8:11], 0\n"
"v_cvt_pk_bf16_f32 v18, v28, v29\n"
"v_mfma_f32_16x16x32_bf16 v[52:55], v[200:203], a[24:27], v[52:55]\n"
"v_cvt_pk_bf16_f32 v19, v30, v31\n"
"v_mfma_f32_16x16x32_bf16 v[56:59], v[200:203], a[28:31], 0\n"
"v_cvt_pk_bf16_f32 v20, v32, v33\n"
"v_mfma_f32_16x16x32_bf16 v[56:59], v[196:199], a[12:15], v[56:59]\n"
"s_add_u32 s93, s62, 241664\n"
"buffer_load_dwordx4 v[184:187], v1, s[24:27], s93 offen offset:0 sc1\n"
"v_cvt_pk_bf16_f32 v21, v34, v35\n"
"v_mfma_f32_16x16x32_bf16 v[56:59], v[204:207], a[44:47], v[56:59]\n"
"s_add_u32 s65, s63, s67\n"
"buffer_store_dwordx4 v[18:21], v1, s[24:27], s65 offen sc1\n"
"v_mfma_f32_16x16x32_bf16 v[56:59], v[208:211], a[60:63], v[56:59]\n"
"v_cvt_pk_bf16_f32 v22, v36, v37\n"
"v_mfma_f32_16x16x32_bf16 v[52:55], v[208:211], a[56:59], v[52:55]\n"
"v_cvt_pk_bf16_f32 v23, v38, v39\n"
"v_mfma_f32_16x16x32_bf16 v[52:55], v[204:207], a[40:43], v[52:55]\n"
"buffer_load_dwordx4 v[188:191], v1, s[24:27], s93 offen offset:1024 sc1\n"
"v_cvt_pk_bf16_f32 v24, v40, v41\n"
"v_mfma_f32_16x16x32_bf16 v[48:51], v[204:207], a[36:39], v[48:51]\n"
"v_cvt_pk_bf16_f32 v25, v42, v43\n"
"v_mfma_f32_16x16x32_bf16 v[48:51], v[208:211], a[52:55], v[48:51]\n"
"s_add_u32 s65, s65, 36864\n"
"buffer_store_dwordx4 v[22:25], v1, s[24:27], s65 offen sc1\n"
"v_mfma_f32_16x16x32_bf16 v[44:47], v[208:211], a[48:51], v[44:47]\n"
"s_mov_b32 s67, 32768\n"
"v_mfma_f32_16x16x32_bf16 v[44:47], v[204:207], a[32:35], v[44:47]\n"
"buffer_load_dwordx4 v[192:195], v1, s[24:27], s93 offen offset:2048 sc1\n"
"v_mfma_f32_16x16x32_bf16 v[44:47], v[212:215], a[64:67], v[44:47]\n"
"s_lshr_b32 s33, s67, 10\n"
"s_add_u32 s33, s33, s28\n"
"s_mul_i32 s34, s33, 29\n"
"s_lshr_b32 s34, s34, 8\n"
"v_mfma_f32_16x16x32_bf16 v[44:47], v[216:219], a[80:83], v[44:47]\n"
"v_mfma_f32_16x16x32_bf16 v[48:51], v[216:219], a[84:87], v[48:51]\n"
"s_mul_i32 s35, s34, 9\n"
"s_sub_u32 s33, s33, s35\n"
"v_mfma_f32_16x16x32_bf16 v[48:51], v[212:215], a[68:71], v[48:51]\n"
"v_mfma_f32_16x16x32_bf16 v[52:55], v[212:215], a[72:75], v[52:55]\n"
"s_lshl_b32 s31, s34, 3\n"
"s_add_u32 s35, s33, s31\n"
"s_add_u32 s34, s34, 32\n"
"v_mfma_f32_16x16x32_bf16 v[52:55], v[216:219], a[88:91], v[52:55]\n"
"v_mfma_f32_16x16x32_bf16 v[56:59], v[216:219], a[92:95], v[56:59]\n"
"s_cmp_eq_u32 s33, 8\n"
"s_cselect_b32 s35, s34, s35\n"
"s_lshr_b32 s33, s35, 1\n"
"v_mfma_f32_16x16x32_bf16 v[56:59], v[212:215], a[76:79], v[56:59]\n"
"buffer_load_dwordx4 v[196:199], v1, s[24:27], s93 offen offset:3072 sc1\n"
"s_barrier\n"
"v_mfma_f32_16x16x32_bf16 v[56:59], v[220:223], a[108:111], v[56:59]\n"
"s_lshl_b32 s33, s33, 12\n"
"v_add_u32 v6, s33, v1\n"
"v_mfma_f32_16x16x32_bf16 v[56:59], v[60:63], a[124:127], v[56:59]\n"
"v_mfma_f32_16x16x32_bf16 v[52:55], v[60:63], a[120:123], v[52:55]\n"
"s_bitcmp1_b32 s35, 0\n"
"s_cselect_b32 s33, 0, -1\n"
"s_cselect_b32 s34, -1, 0\n"
"v_mfma_f32_16x16x32_bf16 v[52:55], v[220:223], a[104:107], v[52:55]\n"
"s_add_u32 s93, s62, 245760\n"
"buffer_load_dwordx4 v[200:203], v1, s[24:27], s93 offen offset:0 sc1\n"
"v_mfma_f32_16x16x32_bf16 v[48:51], v[220:223], a[100:103], v[48:51]\n"
"s_cmp_eq_u32 s60, 7\n"
"s_cselect_b32 s33, s33, 0\n"
"s_cselect_b32 s34, s34, 0\n"
"v_mfma_f32_16x16x32_bf16 v[48:51], v[60:63], a[116:119], v[48:51]\n"
"v_mfma_f32_16x16x32_bf16 v[44:47], v[60:63], a[112:115], v[44:47]\n"
"ds_read_b128 v[28:31], v6 offset:0\n"
"v_mfma_f32_16x16x32_bf16 v[44:47], v[220:223], a[96:99], v[44:47]\n"
"buffer_load_dwordx4 v[204:207], v1, s[24:27], s93 offen offset:1024 sc1\n"
"ds_read_b128 v[32:35], v6 offset:1024\n"
"v_mfma_f32_16x16x32_bf16 v[44:47], v[64:67], a[128:131], v[44:47]\n"
"ds_read_b128 v[36:39], v6 offset:2048\n"
"v_mfma_f32_16x16x32_bf16 v[44:47], v[68:71], a[144:147], v[44:47]\n"
"ds_read_b128 v[40:43], v6 offset:3072\n"
"v_mfma_f32_16x16x32_bf16 v[48:51], v[68:71], a[148:151], v[48:51]\n"
"v_and_b32 v14, s33, v26\n"
"v_mfma_f32_16x16x32_bf16 v[48:51], v[64:67], a[132:135], v[48:51]\n"
"v_and_b32 v15, s33, v27\n"
"v_mfma_f32_16x16x32_bf16 v[52:55], v[64:67], a[136:139], v[52:55]\n"
"v_and_b32 v16, s34, v26\n"
"v_mfma_f32_16x16x32_bf16 v[52:55], v[68:71], a[152:155], v[52:55]\n"
"v_and_b32 v17, s34, v27\n"
"v_mfma_f32_16x16x32_bf16 v[56:59], v[68:71], a[156:159], v[56:59]\n"
"v_mfma_f32_16x16x32_bf16 v[56:59], v[64:67], a[140:143], v[56:59]\n"
"buffer_load_dwordx4 v[60:63], v1, s[24:27], s93 offen offset:2048 sc1\n"
"v_mfma_f32_16x16x32_bf16 v[56:59], v[72:75], a[172:175], v[56:59]\n"
"v_mfma_f32_16x16x32_bf16 v[56:59], v[76:79], a[188:191], v[56:59]\n"
"v_mfma_f32_16x16x32_bf16 v[52:55], v[76:79], a[184:187], v[52:55]\n"
"v_mfma_f32_16x16x32_bf16 v[52:55], v[72:75], a[168:171], v[52:55]\n"
"buffer_load_dwordx4 v[64:67], v1, s[24:27], s93 offen offset:3072 sc1\n"
"v_mfma_f32_16x16x32_bf16 v[48:51], v[72:75], a[164:167], v[48:51]\n"
"v_mfma_f32_16x16x32_bf16 v[48:51], v[76:79], a[180:183], v[48:51]\n"
"v_mfma_f32_16x16x32_bf16 v[44:47], v[76:79], a[176:179], v[44:47]\n"
"s_waitcnt lgkmcnt(3)\n"
"v_mfma_f32_16x16x32_bf16 v[44:47], v[14:17], v[28:31], v[44:47]\n"
"v_mfma_f32_16x16x32_bf16 v[44:47], v[72:75], a[160:163], v[44:47]\n"
"s_add_u32 s93, s62, 249856\n"
"buffer_load_dwordx4 v[68:71], v1, s[24:27], s93 offen offset:0 sc1\n"
"s_barrier\n"
"v_mfma_f32_16x16x32_bf16 v[44:47], v[80:83], a[192:195], v[44:47]\n"
"s_waitcnt lgkmcnt(2)\n"
"v_mfma_f32_16x16x32_bf16 v[48:51], v[14:17], v[32:35], v[48:51]\n"
"v_mfma_f32_16x16x32_bf16 v[44:47], v[84:87], a[208:211], v[44:47]\n"
"v_mfma_f32_16x16x32_bf16 v[48:51], v[84:87], a[212:215], v[48:51]\n"
"s_waitcnt lgkmcnt(1)\n"
"v_mfma_f32_16x16x32_bf16 v[52:55], v[14:17], v[36:39], v[52:55]\n"
"v_mfma_f32_16x16x32_bf16 v[48:51], v[80:83], a[196:199], v[48:51]\n"
"buffer_load_dwordx4 v[72:75], v1, s[24:27], s93 offen offset:1024 sc1\n"
"v_mfma_f32_16x16x32_bf16 v[52:55], v[80:83], a[200:203], v[52:55]\n"
"s_waitcnt lgkmcnt(0)\n"
"v_mfma_f32_16x16x32_bf16 v[56:59], v[14:17], v[40:43], v[56:59]\n"
"v_mfma_f32_16x16x32_bf16 v[52:55], v[84:87], a[216:219], v[52:55]\n"
"v_mfma_f32_16x16x32_bf16 v[56:59], v[84:87], a[220:223], v[56:59]\n"
"v_mfma_f32_16x16x32_bf16 v[56:59], v[80:83], a[204:207], v[56:59]\n"
"v_mfma_f32_16x16x32_bf16 v[56:59], v[88:91], a[236:239], v[56:59]\n"
"v_bfe_u32 v7, v0, 2, 2\n"
"v_mfma_f32_16x16x32_bf16 v[56:59], v[92:95], a[252:255], v[56:59]\n"
"v_lshrrev_b32 v6, 4, v0\n"
"v_mfma_f32_16x16x32_bf16 v[52:55], v[92:95], a[248:251], v[52:55]\n"
"v_lshl_add_u32 v7, v7, 2, v6\n"
"v_mfma_f32_16x16x32_bf16 v[52:55], v[88:91], a[232:235], v[52:55]\n"
"buffer_load_dwordx4 v[76:79], v1, s[24:27], s93 offen offset:2048 sc1\n"
"v_and_b32 v6, 3, v0\n"
"v_mfma_f32_16x16x32_bf16 v[48:51], v[88:91], a[228:231], v[48:51]\n"
"v_lshl_add_u32 v7, v7, 2, v6\n"
"v_mfma_f32_16x16x32_bf16 v[48:51], v[92:95], a[244:247], v[48:51]\n"
"v_lshlrev_b32 v7, 4, v7\n"
"v_mfma_f32_16x16x32_bf16 v[44:47], v[92:95], a[240:243], v[44:47]\n"
"v_mfma_f32_16x16x32_bf16 v[44:47], v[88:91], a[224:227], v[44:47]\n"
"buffer_load_dwordx4 v[80:83], v1, s[24:27], s93 offen offset:3072 sc1\n"
"v_mfma_f32_16x16x32_bf16 v[44:47], v[96:99], v[224:227], v[44:47]\n"
"v_mfma_f32_16x16x32_bf16 v[44:47], v[100:103], v[240:243], v[44:47]\n"
"v_mfma_f32_16x16x32_bf16 v[48:51], v[100:103], v[244:247], v[48:51]\n"
"v_mfma_f32_16x16x32_bf16 v[48:51], v[96:99], v[228:231], v[48:51]\n"
"s_add_u32 s93, s62, 253952\n"
"buffer_load_dwordx4 v[84:87], v1, s[24:27], s93 offen offset:0 sc1\n"
"v_mfma_f32_16x16x32_bf16 v[52:55], v[96:99], v[232:235], v[52:55]\n"
"s_add_u32 s33, s60, 2\n"
"s_and_b32 s33, s33, 7\n"
"s_mul_i32 s33, s33, 512\n"
"s_add_u32 s33, s33, s54\n"
"s_waitcnt vmcnt(11)\n"
"s_mov_b64 exec, 1\n"
"buffer_store_dwordx2 v[4:5], off, s[24:27], s33 offset:8 sc1\n"
"s_mov_b64 exec, -1\n"
"v_mfma_f32_16x16x32_bf16 v[52:55], v[100:103], v[248:251], v[52:55]\n"
"v_mfma_f32_16x16x32_bf16 v[56:59], v[100:103], v[252:255], v[56:59]\n"
"v_mfma_f32_16x16x32_bf16 v[56:59], v[96:99], v[236:239], v[56:59]\n"
"buffer_load_dwordx4 v[88:91], v1, s[24:27], s93 offen offset:1024 sc1\n"
"s_barrier\n"
"v_mfma_f32_16x16x32_bf16 v[28:31], v[104:107], a[0:3], 0\n"
"v_mfma_f32_16x16x32_bf16 v[28:31], v[108:111], a[16:19], v[28:31]\n"
"v_mfma_f32_16x16x32_bf16 v[32:35], v[108:111], a[20:23], 0\n"
"v_mfma_f32_16x16x32_bf16 v[32:35], v[104:107], a[4:7], v[32:35]\n"
"v_mfma_f32_16x16x32_bf16 v[36:39], v[104:107], a[8:11], 0\n"
"v_cvt_pk_bf16_f32 v18, v44, v45\n"
"v_mfma_f32_16x16x32_bf16 v[36:39], v[108:111], a[24:27], v[36:39]\n"
"v_cvt_pk_bf16_f32 v19, v46, v47\n"
"v_mfma_f32_16x16x32_bf16 v[40:43], v[108:111], a[28:31], 0\n"
"v_cvt_pk_bf16_f32 v20, v48, v49\n"
"v_mfma_f32_16x16x32_bf16 v[40:43], v[104:107], a[12:15], v[40:43]\n"
"buffer_load_dwordx4 v[92:95], v1, s[24:27], s93 offen offset:2048 sc1\n"
"v_cvt_pk_bf16_f32 v21, v50, v51\n"
"v_mfma_f32_16x16x32_bf16 v[40:43], v[112:115], a[44:47], v[40:43]\n"
"s_add_u32 s65, s63, s67\n"
"buffer_store_dwordx4 v[18:21], v7, s[24:27], s65 offen sc1\n"
"v_mfma_f32_16x16x32_bf16 v[40:43], v[116:119], a[60:63], v[40:43]\n"
"v_cvt_pk_bf16_f32 v22, v52, v53\n"
"v_mfma_f32_16x16x32_bf16 v[36:39], v[116:119], a[56:59], v[36:39]\n"
"v_cvt_pk_bf16_f32 v23, v54, v55\n"
"v_mfma_f32_16x16x32_bf16 v[36:39], v[112:115], a[40:43], v[36:39]\n"
"buffer_load_dwordx4 v[96:99], v1, s[24:27], s93 offen offset:3072 sc1\n"
"v_cvt_pk_bf16_f32 v24, v56, v57\n"
"v_mfma_f32_16x16x32_bf16 v[32:35], v[112:115], a[36:39], v[32:35]\n"
"v_cvt_pk_bf16_f32 v25, v58, v59\n"
"v_mfma_f32_16x16x32_bf16 v[32:35], v[116:119], a[52:55], v[32:35]\n"
"s_add_u32 s65, s65, 36864\n"
"buffer_store_dwordx4 v[22:25], v7, s[24:27], s65 offen sc1\n"
"v_mfma_f32_16x16x32_bf16 v[28:31], v[116:119], a[48:51], v[28:31]\n"
"s_add_u32 s67, s60, 3\n"
"s_and_b32 s67, s67, 7\n"
"s_lshl_b32 s67, s67, 12\n"
"v_mfma_f32_16x16x32_bf16 v[28:31], v[112:115], a[32:35], v[28:31]\n"
"s_add_u32 s93, s62, 258048\n"
"buffer_load_dwordx4 v[100:103], v1, s[24:27], s93 offen offset:0 sc1\n"
"v_mfma_f32_16x16x32_bf16 v[28:31], v[120:123], a[64:67], v[28:31]\n"
"v_mfma_f32_16x16x32_bf16 v[28:31], v[124:127], a[80:83], v[28:31]\n"
"v_mfma_f32_16x16x32_bf16 v[32:35], v[124:127], a[84:87], v[32:35]\n"
"v_mfma_f32_16x16x32_bf16 v[32:35], v[120:123], a[68:71], v[32:35]\n"
"v_mfma_f32_16x16x32_bf16 v[36:39], v[120:123], a[72:75], v[36:39]\n"
"v_mfma_f32_16x16x32_bf16 v[36:39], v[124:127], a[88:91], v[36:39]\n"
"v_mfma_f32_16x16x32_bf16 v[40:43], v[124:127], a[92:95], v[40:43]\n"
"v_mfma_f32_16x16x32_bf16 v[40:43], v[120:123], a[76:79], v[40:43]\n"
"buffer_load_dwordx4 v[104:107], v1, s[24:27], s93 offen offset:1024 sc1\n"
"s_barrier\n"
"v_mfma_f32_16x16x32_bf16 v[40:43], v[128:131], a[108:111], v[40:43]\n"
"v_mfma_f32_16x16x32_bf16 v[40:43], v[132:135], a[124:127], v[40:43]\n"
"v_mfma_f32_16x16x32_bf16 v[36:39], v[132:135], a[120:123], v[36:39]\n"
"v_mfma_f32_16x16x32_bf16 v[36:39], v[128:131], a[104:107], v[36:39]\n"
"buffer_load_dwordx4 v[108:111], v1, s[24:27], s93 offen offset:2048 sc1\n"
"v_mfma_f32_16x16x32_bf16 v[32:35], v[128:131], a[100:103], v[32:35]\n"
"v_mfma_f32_16x16x32_bf16 v[32:35], v[132:135], a[116:119], v[32:35]\n"
"v_mfma_f32_16x16x32_bf16 v[28:31], v[132:135], a[112:115], v[28:31]\n"
"v_mfma_f32_16x16x32_bf16 v[28:31], v[128:131], a[96:99], v[28:31]\n"
"buffer_load_dwordx4 v[112:115], v1, s[24:27], s93 offen offset:3072 sc1\n"
"v_mfma_f32_16x16x32_bf16 v[28:31], v[136:139], a[128:131], v[28:31]\n"
"v_mfma_f32_16x16x32_bf16 v[28:31], v[140:143], a[144:147], v[28:31]\n"
"v_mfma_f32_16x16x32_bf16 v[32:35], v[140:143], a[148:151], v[32:35]\n"
"v_mfma_f32_16x16x32_bf16 v[32:35], v[136:139], a[132:135], v[32:35]\n"
"s_add_u32 s93, s62, 262144\n"
"buffer_load_dwordx4 v[116:119], v1, s[24:27], s93 offen offset:0 sc1\n"
"v_mfma_f32_16x16x32_bf16 v[36:39], v[136:139], a[136:139], v[36:39]\n"
"v_mfma_f32_16x16x32_bf16 v[36:39], v[140:143], a[152:155], v[36:39]\n"
"v_mfma_f32_16x16x32_bf16 v[40:43], v[140:143], a[156:159], v[40:43]\n"
"v_mfma_f32_16x16x32_bf16 v[40:43], v[136:139], a[140:143], v[40:43]\n"
"s_waitcnt lgkmcnt(0)\n"
"s_barrier\n"
"v_mfma_f32_16x16x32_bf16 v[40:43], v[144:147], a[172:175], v[40:43]\n"
"v_lshrrev_b32 v2, 3, v0\n"
"v_mfma_f32_16x16x32_bf16 v[40:43], v[148:151], a[188:191], v[40:43]\n"
"v_mul_u32_u24 v2, 1152, v2\n"
"v_mfma_f32_16x16x32_bf16 v[36:39], v[148:151], a[184:187], v[36:39]\n"
"v_and_b32 v3, 7, v0\n"
"v_mfma_f32_16x16x32_bf16 v[36:39], v[144:147], a[168:171], v[36:39]\n"
"buffer_load_dwordx4 v[120:123], v1, s[24:27], s93 offen offset:1024 sc1\n"
"v_lshrrev_b32 v7, 4, v0\n"
"v_mfma_f32_16x16x32_bf16 v[32:35], v[144:147], a[164:167], v[32:35]\n"
"v_xor_b32 v3, v3, v7\n"
"v_mfma_f32_16x16x32_bf16 v[32:35], v[148:151], a[180:183], v[32:35]\n"
"v_xor_b32 v7, 4, v3\n"
"v_mfma_f32_16x16x32_bf16 v[28:31], v[148:151], a[176:179], v[28:31]\n"
"v_lshl_add_u32 v7, v7, 4, v2\n"
"v_mfma_f32_16x16x32_bf16 v[28:31], v[144:147], a[160:163], v[28:31]\n"
"buffer_load_dwordx4 v[124:127], v1, s[24:27], s93 offen offset:2048 sc1\n"
"v_lshl_add_u32 v2, v3, 4, v2\n"
"s_barrier\n"
"v_mfma_f32_16x16x32_bf16 v[28:31], v[152:155], a[192:195], v[28:31]\n"
"v_add_u32 v3, 9216, v7\n"
"v_mfma_f32_16x16x32_bf16 v[28:31], v[156:159], a[208:211], v[28:31]\n"
"v_mfma_f32_16x16x32_bf16 v[32:35], v[156:159], a[212:215], v[32:35]\n"
"v_mfma_f32_16x16x32_bf16 v[32:35], v[152:155], a[196:199], v[32:35]\n"
"buffer_load_dwordx4 v[128:131], v1, s[24:27], s93 offen offset:3072 sc1\n"
"v_mfma_f32_16x16x32_bf16 v[36:39], v[152:155], a[200:203], v[36:39]\n"
"v_mfma_f32_16x16x32_bf16 v[36:39], v[156:159], a[216:219], v[36:39]\n"
"v_mfma_f32_16x16x32_bf16 v[40:43], v[156:159], a[220:223], v[40:43]\n"
"v_mfma_f32_16x16x32_bf16 v[40:43], v[152:155], a[204:207], v[40:43]\n"
"v_mfma_f32_16x16x32_bf16 v[40:43], v[160:163], a[236:239], v[40:43]\n"
"v_mfma_f32_16x16x32_bf16 v[40:43], v[164:167], a[252:255], v[40:43]\n"
"v_mfma_f32_16x16x32_bf16 v[36:39], v[164:167], a[248:251], v[36:39]\n"
"v_mfma_f32_16x16x32_bf16 v[36:39], v[160:163], a[232:235], v[36:39]\n"
"s_add_u32 s93, s62, 266240\n"
"buffer_load_dwordx4 v[132:135], v1, s[24:27], s93 offen offset:0 sc1\n"
"v_mfma_f32_16x16x32_bf16 v[32:35], v[160:163], a[228:231], v[32:35]\n"
"v_mfma_f32_16x16x32_bf16 v[32:35], v[164:167], a[244:247], v[32:35]\n"
"v_mfma_f32_16x16x32_bf16 v[28:31], v[164:167], a[240:243], v[28:31]\n"
"v_mfma_f32_16x16x32_bf16 v[28:31], v[160:163], a[224:227], v[28:31]\n"
"buffer_load_dwordx4 v[136:139], v1, s[24:27], s93 offen offset:1024 sc1\n"
"v_mfma_f32_16x16x32_bf16 v[28:31], v[168:171], v[224:227], v[28:31]\n"
"v_mfma_f32_16x16x32_bf16 v[28:31], v[172:175], v[240:243], v[28:31]\n"
"v_mfma_f32_16x16x32_bf16 v[32:35], v[172:175], v[244:247], v[32:35]\n"
"v_mfma_f32_16x16x32_bf16 v[32:35], v[168:171], v[228:231], v[32:35]\n"
"buffer_load_dwordx4 v[140:143], v1, s[24:27], s93 offen offset:2048 sc1\n"
"v_mfma_f32_16x16x32_bf16 v[36:39], v[168:171], v[232:235], v[36:39]\n"
"s_lshl3_add_u32 s33, s60, s49\n"
"s_waitcnt vmcnt(11)\n"
"s_mov_b64 exec, 1\n"
"buffer_store_dwordx2 v[4:5], off, s[24:27], s33 offset:0 sc1\n"
"s_mov_b64 exec, -1\n"
"v_mfma_f32_16x16x32_bf16 v[36:39], v[172:175], v[248:251], v[36:39]\n"
"v_mfma_f32_16x16x32_bf16 v[40:43], v[172:175], v[252:255], v[40:43]\n"
"v_mfma_f32_16x16x32_bf16 v[40:43], v[168:171], v[236:239], v[40:43]\n"
"buffer_load_dwordx4 v[144:147], v1, s[24:27], s93 offen offset:3072 sc1\n"
"s_barrier\n"
"v_mfma_f32_16x16x32_bf16 v[44:47], v[176:179], a[0:3], 0\n"
"v_mfma_f32_16x16x32_bf16 v[44:47], v[180:183], a[16:19], v[44:47]\n"
"v_mfma_f32_16x16x32_bf16 v[48:51], v[180:183], a[20:23], 0\n"
"v_mfma_f32_16x16x32_bf16 v[48:51], v[176:179], a[4:7], v[48:51]\n"
"v_mfma_f32_16x16x32_bf16 v[52:55], v[176:179], a[8:11], 0\n"
"v_cvt_pk_bf16_f32 v18, v28, v29\n"
"v_mfma_f32_16x16x32_bf16 v[52:55], v[180:183], a[24:27], v[52:55]\n"
"v_cvt_pk_bf16_f32 v19, v30, v31\n"
"v_mfma_f32_16x16x32_bf16 v[56:59], v[180:183], a[28:31], 0\n"
"v_cvt_pk_bf16_f32 v20, v32, v33\n"
"v_mfma_f32_16x16x32_bf16 v[56:59], v[176:179], a[12:15], v[56:59]\n"
"s_add_u32 s93, s62, 270336\n"
"buffer_load_dwordx4 v[148:151], v1, s[24:27], s93 offen offset:0 sc1\n"
"v_cvt_pk_bf16_f32 v21, v34, v35\n"
"v_mfma_f32_16x16x32_bf16 v[56:59], v[184:187], a[44:47], v[56:59]\n"
"s_add_u32 s65, s63, s67\n"
"buffer_store_dwordx4 v[18:21], v1, s[24:27], s65 offen sc1\n"
"v_mfma_f32_16x16x32_bf16 v[56:59], v[188:191], a[60:63], v[56:59]\n"
"v_cvt_pk_bf16_f32 v22, v36, v37\n"
"v_mfma_f32_16x16x32_bf16 v[52:55], v[188:191], a[56:59], v[52:55]\n"
"v_cvt_pk_bf16_f32 v23, v38, v39\n"
"v_mfma_f32_16x16x32_bf16 v[52:55], v[184:187], a[40:43], v[52:55]\n"
"buffer_load_dwordx4 v[152:155], v1, s[24:27], s93 offen offset:1024 sc1\n"
"v_cvt_pk_bf16_f32 v24, v40, v41\n"
"v_mfma_f32_16x16x32_bf16 v[48:51], v[184:187], a[36:39], v[48:51]\n"
"v_cvt_pk_bf16_f32 v25, v42, v43\n"
"v_mfma_f32_16x16x32_bf16 v[48:51], v[188:191], a[52:55], v[48:51]\n"
"s_add_u32 s65, s65, 36864\n"
"buffer_store_dwordx4 v[22:25], v1, s[24:27], s65 offen sc1\n"
"v_mfma_f32_16x16x32_bf16 v[44:47], v[188:191], a[48:51], v[44:47]\n"
"s_add_u32 s67, s60, 4\n"
"s_and_b32 s67, s67, 7\n"
"s_lshl_b32 s67, s67, 12\n"
"v_mfma_f32_16x16x32_bf16 v[44:47], v[184:187], a[32:35], v[44:47]\n"
"buffer_load_dwordx4 v[156:159], v1, s[24:27], s93 offen offset:2048 sc1\n"
"v_mfma_f32_16x16x32_bf16 v[44:47], v[192:195], a[64:67], v[44:47]\n"
"v_mfma_f32_16x16x32_bf16 v[44:47], v[196:199], a[80:83], v[44:47]\n"
"v_mfma_f32_16x16x32_bf16 v[48:51], v[196:199], a[84:87], v[48:51]\n"
"v_mfma_f32_16x16x32_bf16 v[48:51], v[192:195], a[68:71], v[48:51]\n"
"buffer_load_dwordx4 v[160:163], v1, s[24:27], s93 offen offset:3072 sc1\n"
"v_mfma_f32_16x16x32_bf16 v[52:55], v[192:195], a[72:75], v[52:55]\n"
"v_mfma_f32_16x16x32_bf16 v[52:55], v[196:199], a[88:91], v[52:55]\n"
"v_mfma_f32_16x16x32_bf16 v[56:59], v[196:199], a[92:95], v[56:59]\n"
"v_mfma_f32_16x16x32_bf16 v[56:59], v[192:195], a[76:79], v[56:59]\n"
"s_barrier\n"
"v_mfma_f32_16x16x32_bf16 v[56:59], v[200:203], a[108:111], v[56:59]\n"
"v_mfma_f32_16x16x32_bf16 v[56:59], v[204:207], a[124:127], v[56:59]\n"
"v_mfma_f32_16x16x32_bf16 v[52:55], v[204:207], a[120:123], v[52:55]\n"
"v_mfma_f32_16x16x32_bf16 v[52:55], v[200:203], a[104:107], v[52:55]\n"
"s_add_u32 s93, s62, 274432\n"
"buffer_load_dwordx4 v[164:167], v1, s[24:27], s93 offen offset:0 sc1\n"
"v_mfma_f32_16x16x32_bf16 v[48:51], v[200:203], a[100:103], v[48:51]\n"
"v_mfma_f32_16x16x32_bf16 v[48:51], v[204:207], a[116:119], v[48:51]\n"
"v_mfma_f32_16x16x32_bf16 v[44:47], v[204:207], a[112:115], v[44:47]\n"
"v_mfma_f32_16x16x32_bf16 v[44:47], v[200:203], a[96:99], v[44:47]\n"
"buffer_load_dwordx4 v[168:171], v1, s[24:27], s93 offen offset:1024 sc1\n"
"v_mfma_f32_16x16x32_bf16 v[44:47], v[60:63], a[128:131], v[44:47]\n"
"v_mfma_f32_16x16x32_bf16 v[44:47], v[64:67], a[144:147], v[44:47]\n"
"v_mfma_f32_16x16x32_bf16 v[48:51], v[64:67], a[148:151], v[48:51]\n"
"v_mfma_f32_16x16x32_bf16 v[48:51], v[60:63], a[132:135], v[48:51]\n"
"buffer_load_dwordx4 v[172:175], v1, s[24:27], s93 offen offset:2048 sc1\n"
"v_mfma_f32_16x16x32_bf16 v[52:55], v[60:63], a[136:139], v[52:55]\n"
"v_mfma_f32_16x16x32_bf16 v[52:55], v[64:67], a[152:155], v[52:55]\n"
"v_mfma_f32_16x16x32_bf16 v[56:59], v[64:67], a[156:159], v[56:59]\n"
"v_mfma_f32_16x16x32_bf16 v[56:59], v[60:63], a[140:143], v[56:59]\n"
"v_mfma_f32_16x16x32_bf16 v[56:59], v[68:71], a[172:175], v[56:59]\n"
"v_mfma_f32_16x16x32_bf16 v[56:59], v[72:75], a[188:191], v[56:59]\n"
"v_mfma_f32_16x16x32_bf16 v[52:55], v[72:75], a[184:187], v[52:55]\n"
"v_mfma_f32_16x16x32_bf16 v[52:55], v[68:71], a[168:171], v[52:55]\n"
"buffer_load_dwordx4 v[176:179], v1, s[24:27], s93 offen offset:3072 sc1\n"
"v_mfma_f32_16x16x32_bf16 v[48:51], v[68:71], a[164:167], v[48:51]\n"
"v_mfma_f32_16x16x32_bf16 v[48:51], v[72:75], a[180:183], v[48:51]\n"
"v_mfma_f32_16x16x32_bf16 v[44:47], v[72:75], a[176:179], v[44:47]\n"
"v_mfma_f32_16x16x32_bf16 v[44:47], v[68:71], a[160:163], v[44:47]\n"
"s_add_u32 s93, s62, 278528\n"
"buffer_load_dwordx4 v[180:183], v1, s[24:27], s93 offen offset:0 sc1\n"
"s_barrier\n"
"v_mfma_f32_16x16x32_bf16 v[44:47], v[76:79], a[192:195], v[44:47]\n"
"s_add_u32 s52, s51, 0\n"
"v_min_u32 v7, 1, v0\n"
"v_lshlrev_b32 v7, 3, v7\n"
"buffer_load_dwordx2 v[16:17], v7, s[24:27], s52 offen sc1\n"
"v_mfma_f32_16x16x32_bf16 v[44:47], v[80:83], a[208:211], v[44:47]\n"
"v_mfma_f32_16x16x32_bf16 v[48:51], v[80:83], a[212:215], v[48:51]\n"
"v_mfma_f32_16x16x32_bf16 v[48:51], v[76:79], a[196:199], v[48:51]\n"
"buffer_load_dwordx4 v[184:187], v1, s[24:27], s93 offen offset:1024 sc1\n"
"v_mfma_f32_16x16x32_bf16 v[52:55], v[76:79], a[200:203], v[52:55]\n"
"v_mfma_f32_16x16x32_bf16 v[52:55], v[80:83], a[216:219], v[52:55]\n"
"v_mfma_f32_16x16x32_bf16 v[56:59], v[80:83], a[220:223], v[56:59]\n"
"v_mfma_f32_16x16x32_bf16 v[56:59], v[76:79], a[204:207], v[56:59]\n"
"buffer_load_dwordx4 v[188:191], v1, s[24:27], s93 offen offset:2048 sc1\n"
"v_mfma_f32_16x16x32_bf16 v[56:59], v[84:87], a[236:239], v[56:59]\n"
"v_mfma_f32_16x16x32_bf16 v[56:59], v[88:91], a[252:255], v[56:59]\n"
"v_mfma_f32_16x16x32_bf16 v[52:55], v[88:91], a[248:251], v[52:55]\n"
"v_mfma_f32_16x16x32_bf16 v[52:55], v[84:87], a[232:235], v[52:55]\n"
"v_mfma_f32_16x16x32_bf16 v[48:51], v[84:87], a[228:231], v[48:51]\n"
"v_mfma_f32_16x16x32_bf16 v[48:51], v[88:91], a[244:247], v[48:51]\n"
"v_mfma_f32_16x16x32_bf16 v[44:47], v[88:91], a[240:243], v[44:47]\n"
"v_mfma_f32_16x16x32_bf16 v[44:47], v[84:87], a[224:227], v[44:47]\n"
"buffer_load_dwordx4 v[192:195], v1, s[24:27], s93 offen offset:3072 sc1\n"
"v_mfma_f32_16x16x32_bf16 v[44:47], v[92:95], v[224:227], v[44:47]\n"
"v_mfma_f32_16x16x32_bf16 v[44:47], v[96:99], v[240:243], v[44:47]\n"
"v_mfma_f32_16x16x32_bf16 v[48:51], v[96:99], v[244:247], v[48:51]\n"
"v_mfma_f32_16x16x32_bf16 v[48:51], v[92:95], v[228:231], v[48:51]\n"
"s_add_u32 s93, s62, 282624\n"
"buffer_load_dwordx4 v[196:199], v1, s[24:27], s93 offen offset:0 sc1\n"
"v_mfma_f32_16x16x32_bf16 v[52:55], v[92:95], v[232:235], v[52:55]\n"
"s_add_u32 s33, s60, 3\n"
"s_and_b32 s33, s33, 7\n"
"s_mul_i32 s33, s33, 512\n"
"s_add_u32 s33, s33, s54\n"
"s_waitcnt vmcnt(12)\n"
"s_mov_b64 exec, 1\n"
"buffer_store_dwordx2 v[4:5], off, s[24:27], s33 offset:24 sc1\n"
"s_mov_b64 exec, -1\n"
"v_mfma_f32_16x16x32_bf16 v[52:55], v[96:99], v[248:251], v[52:55]\n"
"v_mfma_f32_16x16x32_bf16 v[56:59], v[96:99], v[252:255], v[56:59]\n"
"v_mfma_f32_16x16x32_bf16 v[56:59], v[92:95], v[236:239], v[56:59]\n"
"buffer_load_dwordx4 v[200:203], v1, s[24:27], s93 offen offset:1024 sc1\n"
"s_barrier\n"
"v_mfma_f32_16x16x32_bf16 v[28:31], v[100:103], a[0:3], 0\n"
"v_mfma_f32_16x16x32_bf16 v[28:31], v[104:107], a[16:19], v[28:31]\n"
"v_mfma_f32_16x16x32_bf16 v[32:35], v[104:107], a[20:23], 0\n"
"v_mfma_f32_16x16x32_bf16 v[32:35], v[100:103], a[4:7], v[32:35]\n"
"v_mfma_f32_16x16x32_bf16 v[36:39], v[100:103], a[8:11], 0\n"
"v_cvt_pk_bf16_f32 v18, v44, v45\n"
"v_mfma_f32_16x16x32_bf16 v[36:39], v[104:107], a[24:27], v[36:39]\n"
"v_cvt_pk_bf16_f32 v19, v46, v47\n"
"v_mfma_f32_16x16x32_bf16 v[40:43], v[104:107], a[28:31], 0\n"
"v_cvt_pk_bf16_f32 v20, v48, v49\n"
"v_mfma_f32_16x16x32_bf16 v[40:43], v[100:103], a[12:15], v[40:43]\n"
"buffer_load_dwordx4 v[204:207], v1, s[24:27], s93 offen offset:2048 sc1\n"
"v_cvt_pk_bf16_f32 v21, v50, v51\n"
"v_mfma_f32_16x16x32_bf16 v[40:43], v[108:111], a[44:47], v[40:43]\n"
"s_add_u32 s65, s63, s67\n"
"buffer_store_dwordx4 v[18:21], v1, s[24:27], s65 offen sc1\n"
"v_mfma_f32_16x16x32_bf16 v[40:43], v[112:115], a[60:63], v[40:43]\n"
"v_cvt_pk_bf16_f32 v22, v52, v53\n"
"v_mfma_f32_16x16x32_bf16 v[36:39], v[112:115], a[56:59], v[36:39]\n"
"v_cvt_pk_bf16_f32 v23, v54, v55\n"
"v_mfma_f32_16x16x32_bf16 v[36:39], v[108:111], a[40:43], v[36:39]\n"
"buffer_load_dwordx4 v[60:63], v1, s[24:27], s93 offen offset:3072 sc1\n"
"v_cvt_pk_bf16_f32 v24, v56, v57\n"
"v_mfma_f32_16x16x32_bf16 v[32:35], v[108:111], a[36:39], v[32:35]\n"
"v_cvt_pk_bf16_f32 v25, v58, v59\n"
"v_mfma_f32_16x16x32_bf16 v[32:35], v[112:115], a[52:55], v[32:35]\n"
"s_add_u32 s65, s65, 36864\n"
"buffer_store_dwordx4 v[22:25], v1, s[24:27], s65 offen sc1\n"
"v_mfma_f32_16x16x32_bf16 v[28:31], v[112:115], a[48:51], v[28:31]\n"
"s_add_u32 s67, s60, 5\n"
"s_and_b32 s67, s67, 7\n"
"s_lshl_b32 s67, s67, 12\n"
"v_mfma_f32_16x16x32_bf16 v[28:31], v[108:111], a[32:35], v[28:31]\n"
"s_add_u32 s93, s62, 286720\n"
"buffer_load_dwordx4 v[64:67], v1, s[24:27], s93 offen offset:0 sc1\n"
"v_mfma_f32_16x16x32_bf16 v[28:31], v[116:119], a[64:67], v[28:31]\n"
"s_waitcnt vmcnt(11)\n"
"v_cmp_ne_u64 vcc, s[96:97], v[16:17]\n"
"s_nop 4\n"
"s_cbranch_vccz 64f\n"
"65:\n"
"buffer_load_dwordx2 v[16:17], v7, s[24:27], s52 offen sc1\n"
"s_waitcnt vmcnt(0)\n"
"v_cmp_ne_u64 vcc, s[96:97], v[16:17]\n"
"s_nop 4\n"
"s_cbranch_vccz 64f\n"
"s_sleep 1\n"
"s_sub_u32 s75, s75, 1\n"
"s_cmp_gt_i32 s75, 0\n"
"s_cbranch_scc1 65b\n"
"64:\n"
"s_add_u32 s33, s60, 7\n"
"s_and_b32 s33, s33, 7\n"
"s_mul_i32 s33, s33, 73728\n"
"s_lshl_b32 s34, s60, 12\n"
"s_add_u32 s33, s33, s34\n"
"s_add_u32 s33, s33, s53\n"
"s_add_u32 s34, s33, 36864\n"
"buffer_load_dwordx4 v[44:47], v1, s[24:27], s33 offen sc1\n"
"buffer_load_dwordx4 v[48:51], v1, s[24:27], s34 offen sc1\n"
"s_add_u32 s33, s60, 6\n"
"s_and_b32 s33, s33, 7\n"
"s_mul_i32 s33, s33, 73728\n"
"s_lshl_b32 s34, s60, 12\n"
"s_add_u32 s33, s33, s34\n"
"s_add_u32 s33, s33, s53\n"
"s_add_u32 s34, s33, 36864\n"
"buffer_load_dwordx4 v[52:55], v1, s[24:27], s33 offen sc1\n"
"buffer_load_dwordx4 v[56:59], v1, s[24:27], s34 offen sc1\n"
"v_mfma_f32_16x16x32_bf16 v[28:31], v[120:123], a[80:83], v[28:31]\n"
"v_mfma_f32_16x16x32_bf16 v[32:35], v[120:123], a[84:87], v[32:35]\n"
"v_mfma_f32_16x16x32_bf16 v[32:35], v[116:119], a[68:71], v[32:35]\n"
"buffer_load_dwordx4 v[68:71], v1, s[24:27], s93 offen offset:1024 sc1\n"
"v_mfma_f32_16x16x32_bf16 v[36:39], v[116:119], a[72:75], v[36:39]\n"
"v_mfma_f32_16x16x32_bf16 v[36:39], v[120:123], a[88:91], v[36:39]\n"
"v_mfma_f32_16x16x32_bf16 v[40:43], v[120:123], a[92:95], v[40:43]\n"
"v_mfma_f32_16x16x32_bf16 v[40:43], v[116:119], a[76:79], v[40:43]\n"
"s_barrier\n"
"v_mfma_f32_16x16x32_bf16 v[40:43], v[124:127], a[108:111], v[40:43]\n"
"v_and_b32 v7, 7, v0\n"
"v_mfma_f32_16x16x32_bf16 v[40:43], v[128:131], a[124:127], v[40:43]\n"
"v_lshlrev_b32 v7, 3, v7\n"
"v_mfma_f32_16x16x32_bf16 v[36:39], v[128:131], a[120:123], v[36:39]\n"
"v_lshrrev_b32 v6, 4, v0\n"
"v_mfma_f32_16x16x32_bf16 v[36:39], v[124:127], a[104:107], v[36:39]\n"
"buffer_load_dwordx4 v[72:75], v1, s[24:27], s93 offen offset:2048 sc1\n"
"v_mul_u32_u24 v6, 73728, v6\n"
"v_mfma_f32_16x16x32_bf16 v[32:35], v[124:127], a[100:103], v[32:35]\n"
".Lp1_2_%=:\n"
"s_cbranch_execz .Lp1_3_%=\n"
"v_bfe_u32 v14, v0, 2, 2\n"
"v_mfma_f32_16x16x32_bf16 v[32:35], v[128:131], a[116:119], v[32:35]\n"
"v_lshl_add_u32 v6, v14, 6, v6\n"
"v_mfma_f32_16x16x32_bf16 v[28:31], v[128:131], a[112:115], v[28:31]\n"
"v_and_b32 v15, 3, v0\n"
"v_mfma_f32_16x16x32_bf16 v[28:31], v[124:127], a[96:99], v[28:31]\n"
"buffer_load_dwordx4 v[76:79], v1, s[24:27], s93 offen offset:3072 sc1\n"
"v_lshl_add_u32 v6, v15, 4, v6\n"
"v_mfma_f32_16x16x32_bf16 v[28:31], v[132:135], a[128:131], v[28:31]\n"
"v_mfma_f32_16x16x32_bf16 v[28:31], v[136:139], a[144:147], v[28:31]\n"
"v_mfma_f32_16x16x32_bf16 v[32:35], v[136:139], a[148:151], v[32:35]\n"
"v_mfma_f32_16x16x32_bf16 v[32:35], v[132:135], a[132:135], v[32:35]\n"
"s_add_u32 s93, s62, 290816\n"
"buffer_load_dwordx4 v[80:83], v1, s[24:27], s93 offen offset:0 sc1\n"
"v_mfma_f32_16x16x32_bf16 v[36:39], v[132:135], a[136:139], v[36:39]\n"
"v_mfma_f32_16x16x32_bf16 v[36:39], v[136:139], a[152:155], v[36:39]\n"
"v_mfma_f32_16x16x32_bf16 v[40:43], v[136:139], a[156:159], v[40:43]\n"
"v_mfma_f32_16x16x32_bf16 v[40:43], v[132:135], a[140:143], v[40:43]\n"
"buffer_load_dwordx4 v[84:87], v1, s[24:27], s93 offen offset:1024 sc1\n"
"v_mfma_f32_16x16x32_bf16 v[40:43], v[140:143], a[172:175], v[40:43]\n"
"v_mfma_f32_16x16x32_bf16 v[40:43], v[144:147], a[188:191], v[40:43]\n"
"v_mfma_f32_16x16x32_bf16 v[36:39], v[144:147], a[184:187], v[36:39]\n"
"v_mfma_f32_16x16x32_bf16 v[36:39], v[140:143], a[168:171], v[36:39]\n"
"v_mfma_f32_16x16x32_bf16 v[32:35], v[140:143], a[164:167], v[32:35]\n"
"v_mfma_f32_16x16x32_bf16 v[32:35], v[144:147], a[180:183], v[32:35]\n"
"v_mfma_f32_16x16x32_bf16 v[28:31], v[144:147], a[176:179], v[28:31]\n"
"v_mfma_f32_16x16x32_bf16 v[28:31], v[140:143], a[160:163], v[28:31]\n"
"buffer_load_dwordx4 v[88:91], v1, s[24:27], s93 offen offset:2048 sc1\n"
"s_barrier\n"
"v_mfma_f32_16x16x32_bf16 v[28:31], v[148:151], a[192:195], v[28:31]\n"
"s_mov_b32 s52, s49\n"
"buffer_load_dwordx2 v[16:17], v7, s[24:27], s52 offen sc1\n"
"v_mfma_f32_16x16x32_bf16 v[28:31], v[152:155], a[208:211], v[28:31]\n"
"v_mfma_f32_16x16x32_bf16 v[32:35], v[152:155], a[212:215], v[32:35]\n"
"v_mfma_f32_16x16x32_bf16 v[32:35], v[148:151], a[196:199], v[32:35]\n"
"buffer_load_dwordx4 v[92:95], v1, s[24:27], s93 offen offset:3072 sc1\n"
"v_mfma_f32_16x16x32_bf16 v[36:39], v[148:151], a[200:203], v[36:39]\n"
"v_mfma_f32_16x16x32_bf16 v[36:39], v[152:155], a[216:219], v[36:39]\n"
"v_mfma_f32_16x16x32_bf16 v[40:43], v[152:155], a[220:223], v[40:43]\n"
"v_mfma_f32_16x16x32_bf16 v[40:43], v[148:151], a[204:207], v[40:43]\n"
"s_add_u32 s93, s62, 294912\n"
"buffer_load_dwordx4 v[96:99], v1, s[24:27], s93 offen offset:0 sc1\n"
"v_mfma_f32_16x16x32_bf16 v[40:43], v[156:159], a[236:239], v[40:43]\n"
"v_mfma_f32_16x16x32_bf16 v[40:43], v[160:163], a[252:255], v[40:43]\n"
"v_mfma_f32_16x16x32_bf16 v[36:39], v[160:163], a[248:251], v[36:39]\n"
"v_mfma_f32_16x16x32_bf16 v[36:39], v[156:159], a[232:235], v[36:39]\n"
"v_mfma_f32_16x16x32_bf16 v[32:35], v[156:159], a[228:231], v[32:35]\n"
"s_waitcnt vmcnt(12)\n"
"v_mfma_f32_16x16x32_bf16 v[208:211], v[10:13], v[44:47], 0\n"
"v_mfma_f32_16x16x32_bf16 v[32:35], v[160:163], a[244:247], v[32:35]\n"
"v_mfma_f32_16x16x32_bf16 v[212:215], v[8:11], v[44:47], 0\n"
"v_mfma_f32_16x16x32_bf16 v[28:31], v[160:163], a[240:243], v[28:31]\n"
"s_waitcnt vmcnt(11)\n"
"v_mfma_f32_16x16x32_bf16 v[216:219], v[10:13], v[48:51], 0\n"
"v_mfma_f32_16x16x32_bf16 v[28:31], v[156:159], a[224:227], v[28:31]\n"
"buffer_load_dwordx4 v[100:103], v1, s[24:27], s93 offen offset:1024 sc1\n"
"v_mfma_f32_16x16x32_bf16 v[220:223], v[8:11], v[48:51], 0\n"
"v_mfma_f32_16x16x32_bf16 v[28:31], v[164:167], v[224:227], v[28:31]\n"
"s_waitcnt vmcnt(11)\n"
"v_mfma_f32_16x16x32_bf16 v[208:211], v[10:13], v[52:55], v[208:211]\n"
"v_mfma_f32_16x16x32_bf16 v[28:31], v[168:171], v[240:243], v[28:31]\n"
"v_mfma_f32_16x16x32_bf16 v[212:215], v[8:11], v[52:55], v[212:215]\n"
"v_mfma_f32_16x16x32_bf16 v[32:35], v[168:171], v[244:247], v[32:35]\n"
"s_waitcnt vmcnt(10)\n"
"v_mfma_f32_16x16x32_bf16 v[216:219], v[10:13], v[56:59], v[216:219]\n"
"v_mfma_f32_16x16x32_bf16 v[32:35], v[164:167], v[228:231], v[32:35]\n"
"buffer_load_dwordx4 v[104:107], v1, s[24:27], s93 offen offset:2048 sc1\n"
"v_mfma_f32_16x16x32_bf16 v[220:223], v[8:11], v[56:59], v[220:223]\n"
"v_mfma_f32_16x16x32_bf16 v[36:39], v[164:167], v[232:235], v[36:39]\n"
"s_add_u32 s33, s60, 4\n"
"s_and_b32 s33, s33, 7\n"
"s_mul_i32 s33, s33, 512\n"
"s_add_u32 s33, s33, s54\n"
"s_mov_b64 exec, 1\n"
"buffer_store_dwordx2 v[4:5], off, s[24:27], s33 offset:32 sc1\n"
"s_mov_b64 exec, -1\n"
"v_mfma_f32_16x16x32_bf16 v[36:39], v[168:171], v[248:251], v[36:39]\n"
"v_mfma_f32_16x16x32_bf16 v[40:43], v[168:171], v[252:255], v[40:43]\n"
"v_mfma_f32_16x16x32_bf16 v[40:43], v[164:167], v[236:239], v[40:43]\n"
"buffer_load_dwordx4 v[108:111], v1, s[24:27], s93 offen offset:3072 sc1\n"
"s_barrier\n"
"v_mfma_f32_16x16x32_bf16 v[44:47], v[172:175], a[0:3], 0\n"
"v_mfma_f32_16x16x32_bf16 v[44:47], v[176:179], a[16:19], v[44:47]\n"
"v_mfma_f32_16x16x32_bf16 v[48:51], v[176:179], a[20:23], 0\n"
"v_mfma_f32_16x16x32_bf16 v[48:51], v[172:175], a[4:7], v[48:51]\n"
"s_add_u32 s93, s62, 299008\n"
"buffer_load_dwordx4 v[112:115], v1, s[24:27], s93 offen offset:0 sc1\n"
"v_mfma_f32_16x16x32_bf16 v[52:55], v[172:175], a[8:11], 0\n"
"v_cvt_pk_bf16_f32 v18, v28, v29\n"
"v_mfma_f32_16x16x32_bf16 v[52:55], v[176:179], a[24:27], v[52:55]\n"
"v_cvt_pk_bf16_f32 v19, v30, v31\n"
"v_mfma_f32_16x16x32_bf16 v[56:59], v[176:179], a[28:31], 0\n"
"v_cvt_pk_bf16_f32 v20, v32, v33\n"
"v_mfma_f32_16x16x32_bf16 v[56:59], v[172:175], a[12:15], v[56:59]\n"
"v_cvt_pk_bf16_f32 v21, v34, v35\n"
"v_mfma_f32_16x16x32_bf16 v[56:59], v[180:183], a[44:47], v[56:59]\n"
"s_add_u32 s65, s63, s67\n"
"buffer_store_dwordx4 v[18:21], v1, s[24:27], s65 offen sc1\n"
"v_mfma_f32_16x16x32_bf16 v[56:59], v[184:187], a[60:63], v[56:59]\n"
"v_cvt_pk_bf16_f32 v22, v36, v37\n"
"v_mfma_f32_16x16x32_bf16 v[52:55], v[184:187], a[56:59], v[52:55]\n"
"v_cvt_pk_bf16_f32 v23, v38, v39\n"
"v_mfma_f32_16x16x32_bf16 v[52:55], v[180:183], a[40:43], v[52:55]\n"
"buffer_load_dwordx4 v[116:119], v1, s[24:27], s93 offen offset:1024 sc1\n"
"v_cvt_pk_bf16_f32 v24, v40, v41\n"
"v_mfma_f32_16x16x32_bf16 v[48:51], v[180:183], a[36:39], v[48:51]\n"
"v_cvt_pk_bf16_f32 v25, v42, v43\n"
"v_mfma_f32_16x16x32_bf16 v[48:51], v[184:187], a[52:55], v[48:51]\n"
"s_add_u32 s65, s65, 36864\n"
"buffer_store_dwordx4 v[22:25], v1, s[24:27], s65 offen sc1\n"
"v_mfma_f32_16x16x32_bf16 v[44:47], v[184:187], a[48:51], v[44:47]\n"
"s_add_u32 s67, s60, 6\n"
"s_and_b32 s67, s67, 7\n"
"s_lshl_b32 s67, s67, 12\n"
"v_mfma_f32_16x16x32_bf16 v[44:47], v[180:183], a[32:35], v[44:47]\n"
"buffer_load_dwordx4 v[120:123], v1, s[24:27], s93 offen offset:2048 sc1\n"
"v_mfma_f32_16x16x32_bf16 v[44:47], v[188:191], a[64:67], v[44:47]\n"
"s_waitcnt vmcnt(11)\n"
"v_cmp_ne_u64 vcc, s[96:97], v[16:17]\n"
"s_nop 4\n"
"s_cbranch_vccz 66f\n"
"67:\n"
"buffer_load_dwordx2 v[16:17], v7, s[24:27], s52 offen sc1\n"
"s_waitcnt vmcnt(0)\n"
"v_cmp_ne_u64 vcc, s[96:97], v[16:17]\n"
"s_nop 4\n"
"s_cbranch_vccz 66f\n"
"s_sleep 1\n"
"s_sub_u32 s75, s75, 1\n"
"s_cmp_gt_i32 s75, 0\n"
"s_cbranch_scc1 67b\n"
"66:\n"
"s_and_b32 s33, s60, 1\n"
"s_mul_i32 s33, s33, 36864\n"
"s_lshr_b32 s34, s60, 1\n"
"s_xor_b32 s34, s34, s61\n"
"s_and_b32 s34, s34, 3\n"
"s_lshl_b32 s34, s34, 8\n"
"s_add_u32 s33, s33, s34\n"
"s_add_u32 s33, s33, s53\n"
"s_add_u32 s33, s33, 32768\n"
"s_add_u32 s34, s33, 294912\n"
"buffer_load_dwordx4 v[28:31], v6, s[24:27], s33 offen sc1\n"
"buffer_load_dwordx4 v[32:35], v6, s[24:27], s34 offen sc1\n"
"v_mfma_f32_16x16x32_bf16 v[44:47], v[192:195], a[80:83], v[44:47]\n"
"v_lshrrev_b32 v6, 4, v0\n"
"v_mfma_f32_16x16x32_bf16 v[48:51], v[192:195], a[84:87], v[48:51]\n"
"v_lshl_add_u32 v6, v6, 2, v15\n"
"v_mfma_f32_16x16x32_bf16 v[48:51], v[188:191], a[68:71], v[48:51]\n"
"buffer_load_dwordx4 v[124:127], v1, s[24:27], s93 offen offset:3072 sc1\n"
"v_mul_u32_u24 v6, 144, v6\n"
"v_mfma_f32_16x16x32_bf16 v[52:55], v[188:191], a[72:75], v[52:55]\n"
"v_lshl_add_u32 v6, v14, 3, v6\n"
"v_mfma_f32_16x16x32_bf16 v[52:55], v[192:195], a[88:91], v[52:55]\n"
"s_lshl_b32 s33, s28, 5\n"
"v_add_u32 v6, s33, v6\n"
"v_mfma_f32_16x16x32_bf16 v[56:59], v[192:195], a[92:95], v[56:59]\n"
"v_and_b32 v14, 1, v0\n"
"v_mfma_f32_16x16x32_bf16 v[56:59], v[188:191], a[76:79], v[56:59]\n"
"v_lshlrev_b32 v14, 4, v14\n"
"s_barrier\n"
"v_mfma_f32_16x16x32_bf16 v[56:59], v[196:199], a[108:111], v[56:59]\n"
"v_mov_b32 v15, 0x3f80\n"
"v_mfma_f32_16x16x32_bf16 v[56:59], v[200:203], a[124:127], v[56:59]\n"
"v_lshlrev_b32 v14, v14, v15\n"
"v_mfma_f32_16x16x32_bf16 v[52:55], v[200:203], a[120:123], v[52:55]\n"
"v_mov_b32 v36, 0\n"
"v_mfma_f32_16x16x32_bf16 v[52:55], v[196:199], a[104:107], v[52:55]\n"
"s_add_u32 s93, s62, 303104\n"
"buffer_load_dwordx4 v[128:131], v1, s[24:27], s93 offen offset:0 sc1\n"
"s_mov_b32 exec_lo, 0x30003\n"
"s_mov_b32 exec_hi, 0x30003\n"
"v_mov_b32 v36, v14\n"
"s_mov_b64 exec, -1\n"
"v_mfma_f32_16x16x32_bf16 v[48:51], v[196:199], a[100:103], v[48:51]\n"
"v_mov_b32 v37, 0\n"
"v_mfma_f32_16x16x32_bf16 v[48:51], v[200:203], a[116:119], v[48:51]\n"
"s_mov_b32 exec_lo, 0xc000c\n"
"s_mov_b32 exec_hi, 0xc000c\n"
"v_mov_b32 v37, v14\n"
"s_mov_b64 exec, -1\n"
"v_mfma_f32_16x16x32_bf16 v[44:47], v[200:203], a[112:115], v[44:47]\n"
"v_mov_b32 v38, 0\n"
"v_mfma_f32_16x16x32_bf16 v[44:47], v[196:199], a[96:99], v[44:47]\n"
"buffer_load_dwordx4 v[132:135], v1, s[24:27], s93 offen offset:1024 sc1\n"
"s_mov_b32 exec_lo, 0x300030\n"
"s_mov_b32 exec_hi, 0x300030\n"
"v_mov_b32 v38, v14\n"
"s_mov_b64 exec, -1\n"
"v_mfma_f32_16x16x32_bf16 v[44:47], v[204:207], a[128:131], v[44:47]\n"
"v_mov_b32 v39, 0\n"
"v_mfma_f32_16x16x32_bf16 v[44:47], v[60:63], a[144:147], v[44:47]\n"
"s_mov_b32 exec_lo, 0xc000c0\n"
"s_mov_b32 exec_hi, 0xc000c0\n"
"v_mov_b32 v39, v14\n"
"s_mov_b64 exec, -1\n"
"v_mfma_f32_16x16x32_bf16 v[48:51], v[60:63], a[148:151], v[48:51]\n"
"v_mfma_f32_16x16x32_bf16 v[48:51], v[204:207], a[132:135], v[48:51]\n"
"buffer_load_dwordx4 v[136:139], v1, s[24:27], s93 offen offset:2048 sc1\n"
"v_mfma_f32_16x16x32_bf16 v[52:55], v[204:207], a[136:139], v[52:55]\n"
"v_mfma_f32_16x16x32_bf16 v[52:55], v[60:63], a[152:155], v[52:55]\n"
"v_mfma_f32_16x16x32_bf16 v[56:59], v[60:63], a[156:159], v[56:59]\n"
"v_mfma_f32_16x16x32_bf16 v[56:59], v[204:207], a[140:143], v[56:59]\n"
"buffer_load_dwordx4 v[140:143], v1, s[24:27], s93 offen offset:3072 sc1\n"
"v_mfma_f32_16x16x32_bf16 v[56:59], v[64:67], a[172:175], v[56:59]\n"
"v_mfma_f32_16x16x32_bf16 v[56:59], v[68:71], a[188:191], v[56:59]\n"
"v_mfma_f32_16x16x32_bf16 v[52:55], v[68:71], a[184:187], v[52:55]\n"
"v_mfma_f32_16x16x32_bf16 v[52:55], v[64:67], a[168:171], v[52:55]\n"
"v_mfma_f32_16x16x32_bf16 v[48:51], v[64:67], a[164:167], v[48:51]\n"
"v_mfma_f32_16x16x32_bf16 v[48:51], v[68:71], a[180:183], v[48:51]\n"
"v_mfma_f32_16x16x32_bf16 v[44:47], v[68:71], a[176:179], v[44:47]\n"
"v_mfma_f32_16x16x32_bf16 v[44:47], v[64:67], a[160:163], v[44:47]\n"
"s_add_u32 s93, s62, 307200\n"
"buffer_load_dwordx4 v[144:147], v1, s[24:27], s93 offen offset:0 sc1\n"
"s_barrier\n"
"v_mfma_f32_16x16x32_bf16 v[44:47], v[72:75], a[192:195], v[44:47]\n"
"v_mfma_f32_16x16x32_bf16 v[44:47], v[76:79], a[208:211], v[44:47]\n"
"v_mfma_f32_16x16x32_bf16 v[48:51], v[76:79], a[212:215], v[48:51]\n"
"v_mfma_f32_16x16x32_bf16 v[48:51], v[72:75], a[196:199], v[48:51]\n"
"buffer_load_dwordx4 v[148:151], v1, s[24:27], s93 offen offset:1024 sc1\n"
"v_mfma_f32_16x16x32_bf16 v[52:55], v[72:75], a[200:203], v[52:55]\n"
"v_mfma_f32_16x16x32_bf16 v[52:55], v[76:79], a[216:219], v[52:55]\n"
"v_mfma_f32_16x16x32_bf16 v[56:59], v[76:79], a[220:223], v[56:59]\n"
"v_mfma_f32_16x16x32_bf16 v[56:59], v[72:75], a[204:207], v[56:59]\n"
"buffer_load_dwordx4 v[152:155], v1, s[24:27], s93 offen offset:2048 sc1\n"
"v_mfma_f32_16x16x32_bf16 v[56:59], v[80:83], a[236:239], v[56:59]\n"
"s_waitcnt vmcnt(9)\n"
"v_mfma_f32_16x16x32_bf16 v[40:43], v[36:39], v[28:31], 0\n"
"v_mfma_f32_16x16x32_bf16 v[56:59], v[84:87], a[252:255], v[56:59]\n"
"s_waitcnt vmcnt(8)\n"
"v_mfma_f32_16x16x32_bf16 v[40:43], v[36:39], v[32:35], v[40:43]\n"
"v_mfma_f32_16x16x32_bf16 v[52:55], v[84:87], a[248:251], v[52:55]\n"
"v_mfma_f32_16x16x32_bf16 v[52:55], v[80:83], a[232:235], v[52:55]\n"
"buffer_load_dwordx4 v[156:159], v1, s[24:27], s93 offen offset:3072 sc1\n"
"v_mfma_f32_16x16x32_bf16 v[48:51], v[80:83], a[228:231], v[48:51]\n"
"v_mfma_f32_16x16x32_bf16 v[48:51], v[84:87], a[244:247], v[48:51]\n"
"v_mfma_f32_16x16x32_bf16 v[44:47], v[84:87], a[240:243], v[44:47]\n"
"v_mfma_f32_16x16x32_bf16 v[44:47], v[80:83], a[224:227], v[44:47]\n"
"v_mfma_f32_16x16x32_bf16 v[44:47], v[88:91], v[224:227], v[44:47]\n"
"v_mfma_f32_16x16x32_bf16 v[44:47], v[92:95], v[240:243], v[44:47]\n"
"v_cvt_pk_bf16_f32 v14, v40, v41\n"
"v_mfma_f32_16x16x32_bf16 v[48:51], v[92:95], v[244:247], v[48:51]\n"
"v_cvt_pk_bf16_f32 v15, v42, v43\n"
"v_mfma_f32_16x16x32_bf16 v[48:51], v[88:91], v[228:231], v[48:51]\n"
"s_add_u32 s93, s62, 311296\n"
"buffer_load_dwordx4 v[160:163], v1, s[24:27], s93 offen offset:0 sc1\n"
"s_mov_b32 exec_hi, 0\n"
"ds_write_b64 v6, v[14:15] offset:9216\n"
"s_mov_b32 exec_hi, -1\n"
"v_mfma_f32_16x16x32_bf16 v[52:55], v[88:91], v[232:235], v[52:55]\n"
"s_add_u32 s33, s60, 5\n"
"s_and_b32 s33, s33, 7\n"
"s_mul_i32 s33, s33, 512\n"
"s_add_u32 s33, s33, s54\n"
"s_mov_b64 exec, 1\n"
"buffer_store_dwordx2 v[4:5], off, s[24:27], s33 offset:40 sc1\n"
"s_mov_b64 exec, -1\n"
"v_mfma_f32_16x16x32_bf16 v[52:55], v[92:95], v[248:251], v[52:55]\n"
"v_mfma_f32_16x16x32_bf16 v[56:59], v[92:95], v[252:255], v[56:59]\n"
"v_mfma_f32_16x16x32_bf16 v[56:59], v[88:91], v[236:239], v[56:59]\n"
"buffer_load_dwordx4 v[164:167], v1, s[24:27], s93 offen offset:1024 sc1\n"
"s_barrier\n"
"v_mfma_f32_16x16x32_bf16 v[28:31], v[96:99], a[0:3], 0\n"
"v_mfma_f32_16x16x32_bf16 v[28:31], v[100:103], a[16:19], v[28:31]\n"
"v_mfma_f32_16x16x32_bf16 v[32:35], v[100:103], a[20:23], 0\n"
"v_mfma_f32_16x16x32_bf16 v[32:35], v[96:99], a[4:7], v[32:35]\n"
"buffer_load_dwordx4 v[168:171], v1, s[24:27], s93 offen offset:2048 sc1\n"
"v_mfma_f32_16x16x32_bf16 v[36:39], v[96:99], a[8:11], 0\n"
"v_cvt_pk_bf16_f32 v18, v44, v45\n"
"v_mfma_f32_16x16x32_bf16 v[36:39], v[100:103], a[24:27], v[36:39]\n"
"v_cvt_pk_bf16_f32 v19, v46, v47\n"
"v_mfma_f32_16x16x32_bf16 v[40:43], v[100:103], a[28:31], 0\n"
"v_cvt_pk_bf16_f32 v20, v48, v49\n"
"v_mfma_f32_16x16x32_bf16 v[40:43], v[96:99], a[12:15], v[40:43]\n"
"v_cvt_pk_bf16_f32 v21, v50, v51\n"
"v_mfma_f32_16x16x32_bf16 v[40:43], v[104:107], a[44:47], v[40:43]\n"
"s_add_u32 s65, s63, s67\n"
"buffer_store_dwordx4 v[18:21], v1, s[24:27], s65 offen sc1\n"
"v_mfma_f32_16x16x32_bf16 v[40:43], v[108:111], a[60:63], v[40:43]\n"
"v_cvt_pk_bf16_f32 v22, v52, v53\n"
"v_mfma_f32_16x16x32_bf16 v[36:39], v[108:111], a[56:59], v[36:39]\n"
"v_cvt_pk_bf16_f32 v23, v54, v55\n"
"v_mfma_f32_16x16x32_bf16 v[36:39], v[104:107], a[40:43], v[36:39]\n"
"buffer_load_dwordx4 v[172:175], v1, s[24:27], s93 offen offset:3072 sc1\n"
"v_cvt_pk_bf16_f32 v24, v56, v57\n"
"v_mfma_f32_16x16x32_bf16 v[32:35], v[104:107], a[36:39], v[32:35]\n"
"v_cvt_pk_bf16_f32 v25, v58, v59\n"
"v_mfma_f32_16x16x32_bf16 v[32:35], v[108:111], a[52:55], v[32:35]\n"
"s_add_u32 s65, s65, 36864\n"
"buffer_store_dwordx4 v[22:25], v1, s[24:27], s65 offen sc1\n"
"v_mfma_f32_16x16x32_bf16 v[28:31], v[108:111], a[48:51], v[28:31]\n"
"s_add_u32 s67, s60, 7\n"
"s_and_b32 s67, s67, 7\n"
"s_lshl_b32 s67, s67, 12\n"
"v_mfma_f32_16x16x32_bf16 v[28:31], v[104:107], a[32:35], v[28:31]\n"
"s_add_u32 s93, s62, 315392\n"
"buffer_load_dwordx4 v[176:179], v1, s[24:27], s93 offen offset:0 sc1\n"
"v_mfma_f32_16x16x32_bf16 v[28:31], v[112:115], a[64:67], v[28:31]\n"
"v_mfma_f32_16x16x32_bf16 v[28:31], v[116:119], a[80:83], v[28:31]\n"
"v_mfma_f32_16x16x32_bf16 v[32:35], v[116:119], a[84:87], v[32:35]\n"
"v_mfma_f32_16x16x32_bf16 v[32:35], v[112:115], a[68:71], v[32:35]\n"
"buffer_load_dwordx4 v[180:183], v1, s[24:27], s93 offen offset:1024 sc1\n"
"v_mfma_f32_16x16x32_bf16 v[36:39], v[112:115], a[72:75], v[36:39]\n"
"v_mfma_f32_16x16x32_bf16 v[36:39], v[116:119], a[88:91], v[36:39]\n"
"v_mfma_f32_16x16x32_bf16 v[40:43], v[116:119], a[92:95], v[40:43]\n"
"v_mfma_f32_16x16x32_bf16 v[40:43], v[112:115], a[76:79], v[40:43]\n"
"buffer_load_dwordx4 v[184:187], v1, s[24:27], s93 offen offset:2048 sc1\n"
"s_barrier\n"
"v_mfma_f32_16x16x32_bf16 v[40:43], v[120:123], a[108:111], v[40:43]\n"
"s_waitcnt vmcnt(18)\n"
"v_mfma_f32_16x16x32_bf16 v[40:43], v[124:127], a[124:127], v[40:43]\n"
"v_mfma_f32_16x16x32_bf16 v[36:39], v[124:127], a[120:123], v[36:39]\n"
"v_mfma_f32_16x16x32_bf16 v[36:39], v[120:123], a[104:107], v[36:39]\n"
"v_mfma_f32_16x16x32_bf16 v[32:35], v[120:123], a[100:103], v[32:35]\n"
"v_mfma_f32_16x16x32_bf16 v[32:35], v[124:127], a[116:119], v[32:35]\n"
"v_mfma_f32_16x16x32_bf16 v[28:31], v[124:127], a[112:115], v[28:31]\n"
"v_mfma_f32_16x16x32_bf16 v[28:31], v[120:123], a[96:99], v[28:31]\n"
"s_waitcnt vmcnt(17)\n"
"buffer_load_dwordx4 v[188:191], v1, s[24:27], s93 offen offset:3072 sc1\n"
"v_mfma_f32_16x16x32_bf16 v[28:31], v[128:131], a[128:131], v[28:31]\n"
"s_waitcnt vmcnt(17)\n"
"v_mfma_f32_16x16x32_bf16 v[28:31], v[132:135], a[144:147], v[28:31]\n"
"v_mfma_f32_16x16x32_bf16 v[32:35], v[132:135], a[148:151], v[32:35]\n"
"v_mfma_f32_16x16x32_bf16 v[32:35], v[128:131], a[132:135], v[32:35]\n"
"s_add_u32 s93, s62, 319488\n"
"buffer_load_dwordx4 v[192:195], v1, s[24:27], s93 offen offset:0 sc1\n"
"v_mfma_f32_16x16x32_bf16 v[36:39], v[128:131], a[136:139], v[36:39]\n"
"v_mfma_f32_16x16x32_bf16 v[36:39], v[132:135], a[152:155], v[36:39]\n"
"v_mfma_f32_16x16x32_bf16 v[40:43], v[132:135], a[156:159], v[40:43]\n"
"v_mfma_f32_16x16x32_bf16 v[40:43], v[128:131], a[140:143], v[40:43]\n"
"s_waitcnt vmcnt(17)\n"
"buffer_load_dwordx4 v[196:199], v1, s[24:27], s93 offen offset:1024 sc1\n"
"v_mfma_f32_16x16x32_bf16 v[40:43], v[136:139], a[172:175], v[40:43]\n"
"s_waitcnt vmcnt(17)\n"
"v_mfma_f32_16x16x32_bf16 v[40:43], v[140:143], a[188:191], v[40:43]\n"
"v_mfma_f32_16x16x32_bf16 v[36:39], v[140:143], a[184:187], v[36:39]\n"
"v_mfma_f32_16x16x32_bf16 v[36:39], v[136:139], a[168:171], v[36:39]\n"
"buffer_load_dwordx4 v[200:203], v1, s[24:27], s93 offen offset:2048 sc1\n"
"v_mfma_f32_16x16x32_bf16 v[32:35], v[136:139], a[164:167], v[32:35]\n"
"v_mfma_f32_16x16x32_bf16 v[32:35], v[140:143], a[180:183], v[32:35]\n"
"v_mfma_f32_16x16x32_bf16 v[28:31], v[140:143], a[176:179], v[28:31]\n"
"v_mfma_f32_16x16x32_bf16 v[28:31], v[136:139], a[160:163], v[28:31]\n"
"s_waitcnt vmcnt(17)\n"
"s_barrier\n"
"v_mfma_f32_16x16x32_bf16 v[28:31], v[144:147], a[192:195], v[28:31]\n"
"s_waitcnt vmcnt(16)\n"
"s_add_u32 s52, s51, 24\n"
"v_min_u32 v7, 1, v0\n"
"v_lshlrev_b32 v7, 3, v7\n"
"buffer_load_dwordx2 v[16:17], v7, s[24:27], s52 offen sc1\n"
"v_mfma_f32_16x16x32_bf16 v[28:31], v[148:151], a[208:211], v[28:31]\n"
"v_mfma_f32_16x16x32_bf16 v[32:35], v[148:151], a[212:215], v[32:35]\n"
"v_mfma_f32_16x16x32_bf16 v[32:35], v[144:147], a[196:199], v[32:35]\n"
"buffer_load_dwordx4 v[204:207], v1, s[24:27], s93 offen offset:3072 sc1\n"
"v_mfma_f32_16x16x32_bf16 v[36:39], v[144:147], a[200:203], v[36:39]\n"
"v_mfma_f32_16x16x32_bf16 v[36:39], v[148:151], a[216:219], v[36:39]\n"
"v_mfma_f32_16x16x32_bf16 v[40:43], v[148:151], a[220:223], v[40:43]\n"
"v_mfma_f32_16x16x32_bf16 v[40:43], v[144:147], a[204:207], v[40:43]\n"
"s_waitcnt vmcnt(17)\n"
"s_add_u32 s93, s62, 323584\n"
"buffer_load_dwordx4 v[60:63], v1, s[24:27], s93 offen offset:0 sc1\n"
"v_mfma_f32_16x16x32_bf16 v[40:43], v[152:155], a[236:239], v[40:43]\n"
"s_waitcnt vmcnt(17)\n"
"v_mfma_f32_16x16x32_bf16 v[40:43], v[156:159], a[252:255], v[40:43]\n"
"v_mfma_f32_16x16x32_bf16 v[36:39], v[156:159], a[248:251], v[36:39]\n"
"v_mfma_f32_16x16x32_bf16 v[36:39], v[152:155], a[232:235], v[36:39]\n"
"buffer_load_dwordx4 v[64:67], v1, s[24:27], s93 offen offset:1024 sc1\n"
"v_mfma_f32_16x16x32_bf16 v[32:35], v[152:155], a[228:231], v[32:35]\n"
"v_mfma_f32_16x16x32_bf16 v[32:35], v[156:159], a[244:247], v[32:35]\n"
"v_mfma_f32_16x16x32_bf16 v[28:31], v[156:159], a[240:243], v[28:31]\n"
"v_mfma_f32_16x16x32_bf16 v[28:31], v[152:155], a[224:227], v[28:31]\n"
"s_waitcnt vmcnt(17)\n"
"v_mfma_f32_16x16x32_bf16 v[28:31], v[160:163], v[224:227], v[28:31]\n"
"s_waitcnt vmcnt(15)\n"
"v_mfma_f32_16x16x32_bf16 v[28:31], v[164:167], v[240:243], v[28:31]\n"
"v_mfma_f32_16x16x32_bf16 v[32:35], v[164:167], v[244:247], v[32:35]\n"
"v_mfma_f32_16x16x32_bf16 v[32:35], v[160:163], v[228:231], v[32:35]\n"
"buffer_load_dwordx4 v[68:71], v1, s[24:27], s93 offen offset:2048 sc1\n"
"v_mfma_f32_16x16x32_bf16 v[36:39], v[160:163], v[232:235], v[36:39]\n"
"s_add_u32 s33, s60, 6\n"
"s_and_b32 s33, s33, 7\n"
"s_mul_i32 s33, s33, 512\n"
"s_add_u32 s33, s33, s54\n"
"s_waitcnt vmcnt(12)\n"
"s_mov_b64 exec, 1\n"
"buffer_store_dwordx2 v[4:5], off, s[24:27], s33 offset:48 sc1\n"
"s_mov_b64 exec, -1\n"
"v_mfma_f32_16x16x32_bf16 v[36:39], v[164:167], v[248:251], v[36:39]\n"
"v_mfma_f32_16x16x32_bf16 v[40:43], v[164:167], v[252:255], v[40:43]\n"
"v_mfma_f32_16x16x32_bf16 v[40:43], v[160:163], v[236:239], v[40:43]\n"
"buffer_load_dwordx4 v[72:75], v1, s[24:27], s93 offen offset:3072 sc1\n"
"s_cmp_lg_u32 s30, 0\n"
"s_cbranch_scc1 2f\n"
"s_barrier\n"
"v_mfma_f32_16x16x32_bf16 v[208:211], v[168:171], a[0:3], v[208:211]\n"
"v_mfma_f32_16x16x32_bf16 v[208:211], v[172:175], a[16:19], v[208:211]\n"
"v_mfma_f32_16x16x32_bf16 v[212:215], v[172:175], a[20:23], v[212:215]\n"
"v_mfma_f32_16x16x32_bf16 v[212:215], v[168:171], a[4:7], v[212:215]\n"
"s_add_u32 s93, s62, 327680\n"
"buffer_load_dwordx4 v[76:79], v1, s[24:27], s93 offen offset:0 sc1\n"
"v_mfma_f32_16x16x32_bf16 v[216:219], v[168:171], a[8:11], v[216:219]\n"
"v_cvt_pk_bf16_f32 v18, v28, v29\n"
"v_mfma_f32_16x16x32_bf16 v[216:219], v[172:175], a[24:27], v[216:219]\n"
"v_cvt_pk_bf16_f32 v19, v30, v31\n"
"v_mfma_f32_16x16x32_bf16 v[220:223], v[172:175], a[28:31], v[220:223]\n"
"v_cvt_pk_bf16_f32 v20, v32, v33\n"
"v_mfma_f32_16x16x32_bf16 v[220:223], v[168:171], a[12:15], v[220:223]\n"
"s_waitcnt vmcnt(14)\n"
"buffer_load_dwordx4 v[80:83], v1, s[24:27], s93 offen offset:1024 sc1\n"
"v_cvt_pk_bf16_f32 v21, v34, v35\n"
"v_mfma_f32_16x16x32_bf16 v[220:223], v[176:179], a[44:47], v[220:223]\n"
"s_waitcnt vmcnt(14)\n"
"s_add_u32 s65, s63, s67\n"
"buffer_store_dwordx4 v[18:21], v1, s[24:27], s65 offen sc1\n"
"v_mfma_f32_16x16x32_bf16 v[220:223], v[180:183], a[60:63], v[220:223]\n"
"v_cvt_pk_bf16_f32 v22, v36, v37\n"
"v_mfma_f32_16x16x32_bf16 v[216:219], v[180:183], a[56:59], v[216:219]\n"
"v_cvt_pk_bf16_f32 v23, v38, v39\n"
"v_mfma_f32_16x16x32_bf16 v[216:219], v[176:179], a[40:43], v[216:219]\n"
"buffer_load_dwordx4 v[84:87], v1, s[24:27], s93 offen offset:2048 sc1\n"
"v_cvt_pk_bf16_f32 v24, v40, v41\n"
"v_mfma_f32_16x16x32_bf16 v[212:215], v[176:179], a[36:39], v[212:215]\n"
"v_cvt_pk_bf16_f32 v25, v42, v43\n"
"v_mfma_f32_16x16x32_bf16 v[212:215], v[180:183], a[52:55], v[212:215]\n"
"s_add_u32 s65, s65, 36864\n"
"buffer_store_dwordx4 v[22:25], v1, s[24:27], s65 offen sc1\n"
"v_mfma_f32_16x16x32_bf16 v[208:211], v[180:183], a[48:51], v[208:211]\n"
"s_add_u32 s67, s60, 8\n"
"s_and_b32 s67, s67, 7\n"
"s_lshl_b32 s67, s67, 12\n"
"v_mfma_f32_16x16x32_bf16 v[208:211], v[176:179], a[32:35], v[208:211]\n"
"s_waitcnt vmcnt(16)\n"
"buffer_load_dwordx4 v[88:91], v1, s[24:27], s93 offen offset:3072 sc1\n"
"v_mfma_f32_16x16x32_bf16 v[208:211], v[184:187], a[64:67], v[208:211]\n"
"s_waitcnt vmcnt(16)\n"
"s_waitcnt vmcnt(12)\n"
"v_cmp_ne_u64 vcc, s[96:97], v[16:17]\n"
"s_nop 4\n"
"s_cbranch_vccz 68f\n"
"69:\n"
"buffer_load_dwordx2 v[16:17], v7, s[24:27], s52 offen sc1\n"
"s_waitcnt vmcnt(0)\n"
"v_cmp_ne_u64 vcc, s[96:97], v[16:17]\n"
"s_nop 4\n"
"s_cbranch_vccz 68f\n"
"s_sleep 1\n"
"s_sub_u32 s75, s75, 1\n"
"s_cmp_gt_i32 s75, 0\n"
"s_cbranch_scc1 69b\n"
"68:\n"
"s_add_u32 s33, s60, 5\n"
"s_and_b32 s33, s33, 7\n"
"s_mul_i32 s33, s33, 73728\n"
"s_lshl_b32 s34, s60, 12\n"
"s_add_u32 s33, s33, s34\n"
"s_add_u32 s33, s33, s53\n"
"s_add_u32 s34, s33, 36864\n"
"buffer_load_dwordx4 v[28:31], v1, s[24:27], s33 offen sc1\n"
"buffer_load_dwordx4 v[32:35], v1, s[24:27], s34 offen sc1\n"
"s_add_u32 s33, s60, 4\n"
"s_and_b32 s33, s33, 7\n"
"s_mul_i32 s33, s33, 73728\n"
"s_lshl_b32 s34, s60, 12\n"
"s_add_u32 s33, s33, s34\n"
"s_add_u32 s33, s33, s53\n"
"s_add_u32 s34, s33, 36864\n"
"buffer_load_dwordx4 v[36:39], v1, s[24:27], s33 offen sc1\n"
"buffer_load_dwordx4 v[40:43], v1, s[24:27], s34 offen sc1\n"
"v_mfma_f32_16x16x32_bf16 v[208:211], v[188:191], a[80:83], v[208:211]\n"
"v_mfma_f32_16x16x32_bf16 v[212:215], v[188:191], a[84:87], v[212:215]\n"
"v_mfma_f32_16x16x32_bf16 v[212:215], v[184:187], a[68:71], v[212:215]\n"
"s_bitcmp1_b32 s28, 0\n"
"s_cselect_b64 vcc, -1, 0\n"
"v_cndmask_b32 v14, v2, v3, vcc\n"
"s_cmp_lt_u32 s28, 2\n"
"s_cbranch_scc0 70f\n"
"s_lshl_b32 s81, s28, 10\n"
"s_add_u32 m0, s81, 91136\n"
"s_add_u32 s81, s83, 1024\n"
"buffer_load_dwordx4 v14, s[68:71], s81 offen lds\n"
"70:\n"
"v_mfma_f32_16x16x32_bf16 v[216:219], v[184:187], a[72:75], v[216:219]\n"
"v_mfma_f32_16x16x32_bf16 v[216:219], v[188:191], a[88:91], v[216:219]\n"
"v_mfma_f32_16x16x32_bf16 v[220:223], v[188:191], a[92:95], v[220:223]\n"
"v_mfma_f32_16x16x32_bf16 v[220:223], v[184:187], a[76:79], v[220:223]\n"
"s_lshl_b32 s81, s28, 10\n"
"s_add_u32 m0, s81, 74752\n"
"s_add_u32 s81, s83, 0\n"
"buffer_load_dwordx4 v14, s[68:71], s81 offen lds\n"
"s_barrier\n"
"v_mfma_f32_16x16x32_bf16 v[220:223], v[192:195], a[108:111], v[220:223]\n"
"v_mfma_f32_16x16x32_bf16 v[220:223], v[196:199], a[124:127], v[220:223]\n"
"v_mfma_f32_16x16x32_bf16 v[216:219], v[196:199], a[120:123], v[216:219]\n"
"v_mfma_f32_16x16x32_bf16 v[216:219], v[192:195], a[104:107], v[216:219]\n"
"s_lshl_b32 s81, s28, 10\n"
"s_add_u32 m0, s81, 78848\n"
"s_add_u32 s81, s83, 256\n"
"buffer_load_dwordx4 v14, s[68:71], s81 offen lds\n"
"v_mfma_f32_16x16x32_bf16 v[212:215], v[192:195], a[100:103], v[212:215]\n"
"v_mfma_f32_16x16x32_bf16 v[212:215], v[196:199], a[116:119], v[212:215]\n"
"v_mfma_f32_16x16x32_bf16 v[208:211], v[196:199], a[112:115], v[208:211]\n"
"v_mfma_f32_16x16x32_bf16 v[208:211], v[192:195], a[96:99], v[208:211]\n"
"s_lshl_b32 s81, s28, 10\n"
"s_add_u32 m0, s81, 82944\n"
"s_add_u32 s81, s83, 512\n"
"buffer_load_dwordx4 v14, s[68:71], s81 offen lds\n"
"v_mfma_f32_16x16x32_bf16 v[208:211], v[200:203], a[128:131], v[208:211]\n"
"s_waitcnt vmcnt(18)\n"
"v_mfma_f32_16x16x32_bf16 v[208:211], v[204:207], a[144:147], v[208:211]\n"
"v_mfma_f32_16x16x32_bf16 v[212:215], v[204:207], a[148:151], v[212:215]\n"
"v_mfma_f32_16x16x32_bf16 v[212:215], v[200:203], a[132:135], v[212:215]\n"
"s_lshl_b32 s81, s28, 10\n"
"s_add_u32 m0, s81, 87040\n"
"s_add_u32 s81, s83, 768\n"
"buffer_load_dwordx4 v14, s[68:71], s81 offen lds\n"
"v_mfma_f32_16x16x32_bf16 v[216:219], v[200:203], a[136:139], v[216:219]\n"
"v_mfma_f32_16x16x32_bf16 v[216:219], v[204:207], a[152:155], v[216:219]\n"
"v_mfma_f32_16x16x32_bf16 v[220:223], v[204:207], a[156:159], v[220:223]\n"
"v_mfma_f32_16x16x32_bf16 v[220:223], v[200:203], a[140:143], v[220:223]\n"
"s_waitcnt vmcnt(18)\n"
"s_add_u32 m0, s79, 0\n"
"s_add_u32 s81, s78, 0\n"
"buffer_load_dwordx4 v2, s[68:71], s81 offen lds\n"
"v_mfma_f32_16x16x32_bf16 v[220:223], v[60:63], a[172:175], v[220:223]\n"
"s_waitcnt vmcnt(18)\n"
"v_mfma_f32_16x16x32_bf16 v[220:223], v[64:67], a[188:191], v[220:223]\n"
"v_mfma_f32_16x16x32_bf16 v[216:219], v[64:67], a[184:187], v[216:219]\n"
"v_mfma_f32_16x16x32_bf16 v[216:219], v[60:63], a[168:171], v[216:219]\n"
"v_mfma_f32_16x16x32_bf16 v[212:215], v[60:63], a[164:167], v[212:215]\n"
"s_add_u32 s33, s60, 7\n"
"s_and_b32 s33, s33, 7\n"
"s_mul_i32 s33, s33, 512\n"
"s_add_u32 s33, s33, s54\n"
"s_waitcnt vmcnt(10)\n"
"s_mov_b64 exec, 1\n"
"buffer_store_dwordx2 v[4:5], off, s[24:27], s33 offset:56 sc1\n"
"s_mov_b64 exec, -1\n"
"v_mfma_f32_16x16x32_bf16 v[212:215], v[64:67], a[180:183], v[212:215]\n"
"v_mfma_f32_16x16x32_bf16 v[208:211], v[64:67], a[176:179], v[208:211]\n"
"v_mfma_f32_16x16x32_bf16 v[208:211], v[60:63], a[160:163], v[208:211]\n"
"s_barrier\n"
"v_mfma_f32_16x16x32_bf16 v[208:211], v[68:71], a[192:195], v[208:211]\n"
"v_mfma_f32_16x16x32_bf16 v[208:211], v[72:75], a[208:211], v[208:211]\n"
"v_mfma_f32_16x16x32_bf16 v[212:215], v[72:75], a[212:215], v[212:215]\n"
"v_mfma_f32_16x16x32_bf16 v[212:215], v[68:71], a[196:199], v[212:215]\n"
"v_mfma_f32_16x16x32_bf16 v[216:219], v[68:71], a[200:203], v[216:219]\n"
"v_mfma_f32_16x16x32_bf16 v[216:219], v[72:75], a[216:219], v[216:219]\n"
"v_mfma_f32_16x16x32_bf16 v[220:223], v[72:75], a[220:223], v[220:223]\n"
"v_mfma_f32_16x16x32_bf16 v[220:223], v[68:71], a[204:207], v[220:223]\n"
"v_mfma_f32_16x16x32_bf16 v[220:223], v[76:79], a[236:239], v[220:223]\n"
"v_mfma_f32_16x16x32_bf16 v[220:223], v[80:83], a[252:255], v[220:223]\n"
"v_mfma_f32_16x16x32_bf16 v[216:219], v[80:83], a[248:251], v[216:219]\n"
"v_mfma_f32_16x16x32_bf16 v[216:219], v[76:79], a[232:235], v[216:219]\n"
"v_mfma_f32_16x16x32_bf16 v[212:215], v[76:79], a[228:231], v[212:215]\n"
"s_waitcnt vmcnt(9)\n"
"v_mfma_f32_16x16x32_bf16 v[208:211], v[10:13], v[28:31], v[208:211]\n"
"v_mfma_f32_16x16x32_bf16 v[212:215], v[80:83], a[244:247], v[212:215]\n"
"v_mfma_f32_16x16x32_bf16 v[212:215], v[8:11], v[28:31], v[212:215]\n"
"v_mfma_f32_16x16x32_bf16 v[208:211], v[80:83], a[240:243], v[208:211]\n"
"s_waitcnt vmcnt(8)\n"
"v_mfma_f32_16x16x32_bf16 v[216:219], v[10:13], v[32:35], v[216:219]\n"
"v_mfma_f32_16x16x32_bf16 v[208:211], v[76:79], a[224:227], v[208:211]\n"
"v_mfma_f32_16x16x32_bf16 v[220:223], v[8:11], v[32:35], v[220:223]\n"
"v_mfma_f32_16x16x32_bf16 v[208:211], v[84:87], v[224:227], v[208:211]\n"
"s_waitcnt vmcnt(7)\n"
"v_mfma_f32_16x16x32_bf16 v[208:211], v[10:13], v[36:39], v[208:211]\n"
"v_mfma_f32_16x16x32_bf16 v[208:211], v[88:91], v[240:243], v[208:211]\n"
"v_mfma_f32_16x16x32_bf16 v[212:215], v[8:11], v[36:39], v[212:215]\n"
"v_mfma_f32_16x16x32_bf16 v[212:215], v[88:91], v[244:247], v[212:215]\n"
"s_waitcnt vmcnt(6)\n"
"v_mfma_f32_16x16x32_bf16 v[216:219], v[10:13], v[40:43], v[216:219]\n"
"v_mfma_f32_16x16x32_bf16 v[212:215], v[84:87], v[228:231], v[212:215]\n"
"v_mfma_f32_16x16x32_bf16 v[220:223], v[8:11], v[40:43], v[220:223]\n"
"v_mfma_f32_16x16x32_bf16 v[216:219], v[84:87], v[232:235], v[216:219]\n"
"v_mfma_f32_16x16x32_bf16 v[216:219], v[88:91], v[248:251], v[216:219]\n"
"v_mfma_f32_16x16x32_bf16 v[220:223], v[88:91], v[252:255], v[220:223]\n"
"v_mfma_f32_16x16x32_bf16 v[220:223], v[84:87], v[236:239], v[220:223]\n"
"v_and_b32 v4, 15, v0\n"
"v_lshrrev_b32 v5, 4, v0\n"
"v_mul_u32_u24 v18, 144, v4\n"
"v_lshl_add_u32 v18, v5, 3, v18\n"
"s_lshl_b32 s33, s28, 5\n"
"v_add_u32 v18, s33, v18\n"
"s_add_u32 s52, s51, 40\n"
"v_min_u32 v7, 2, v0\n"
"v_lshlrev_b32 v7, 3, v7\n"
"72:\n"
"buffer_load_dwordx2 v[16:17], v7, s[24:27], s52 offen sc1\n"
"s_waitcnt vmcnt(0)\n"
"v_cmp_ne_u64 vcc, s[96:97], v[16:17]\n"
"s_nop 4\n"
"s_cbranch_vccz 71f\n"
"s_sleep 1\n"
"s_sub_u32 s75, s75, 1\n"
"s_cmp_gt_i32 s75, 0\n"
"s_cbranch_scc1 72b\n"
"71:\n"
"s_add_u32 s33, s60, 3\n"
"s_and_b32 s33, s33, 7\n"
"s_mul_i32 s33, s33, 73728\n"
"s_lshl_b32 s34, s60, 12\n"
"s_add_u32 s33, s33, s34\n"
"s_add_u32 s33, s33, s53\n"
"s_add_u32 s34, s33, 36864\n"
"buffer_load_dwordx4 v[36:39], v1, s[24:27], s33 offen sc1\n"
"buffer_load_dwordx4 v[40:43], v1, s[24:27], s34 offen sc1\n"
"s_add_u32 s33, s60, 2\n"
"s_and_b32 s33, s33, 7\n"
"s_mul_i32 s33, s33, 73728\n"
".Lp2_2_%=:\n"
"s_cbranch_execz .Lp2_3_%=\n"
"s_lshl_b32 s34, s60, 12\n"
"s_add_u32 s33, s33, s34\n"
"s_add_u32 s33, s33, s53\n"
"s_add_u32 s34, s33, 36864\n"
"buffer_load_dwordx4 v[44:47], v1, s[24:27], s33 offen sc1\n"
"buffer_load_dwordx4 v[48:51], v1, s[24:27], s34 offen sc1\n"
"s_add_u32 s33, s60, 1\n"
"s_and_b32 s33, s33, 7\n"
"s_mul_i32 s33, s33, 73728\n"
"s_lshl_b32 s34, s60, 12\n"
"s_add_u32 s33, s33, s34\n"
"s_add_u32 s33, s33, s53\n"
"s_add_u32 s34, s33, 36864\n"
"buffer_load_dwordx4 v[52:55], v1, s[24:27], s33 offen sc1\n"
"buffer_load_dwordx4 v[56:59], v1, s[24:27], s34 offen sc1\n"
"s_add_u32 m0, s79, 1024\n"
"s_add_u32 s81, s78, 0\n"
"buffer_load_dwordx4 v3, s[68:71], s81 offen lds\n"
"s_add_u32 m0, s79, 2048\n"
"s_add_u32 s81, s78, 128\n"
"buffer_load_dwordx4 v2, s[68:71], s81 offen lds\n"
"s_add_u32 m0, s79, 3072\n"
"s_add_u32 s81, s78, 128\n"
"buffer_load_dwordx4 v3, s[68:71], s81 offen lds\n"
"s_add_u32 m0, s79, 4096\n"
"s_add_u32 s81, s78, 256\n"
"buffer_load_dwordx4 v2, s[68:71], s81 offen lds\n"
"s_add_u32 m0, s79, 5120\n"
"s_add_u32 s81, s78, 256\n"
"buffer_load_dwordx4 v3, s[68:71], s81 offen lds\n"
"s_add_u32 m0, s79, 6144\n"
"s_add_u32 s81, s78, 384\n"
"buffer_load_dwordx4 v2, s[68:71], s81 offen lds\n"
"v_lshrrev_b32 v19, 3, v0\n"
"v_and_b32 v20, 7, v0\n"
"v_lshlrev_b32 v20, 4, v20\n"
"v_mul_u32_u24 v21, 144, v19\n"
"v_add_u32 v21, v21, v20\n"
"s_mul_i32 s33, s28, 1152\n"
"v_add_u32 v21, s33, v21\n"
"v_mul_u32_u24 v22, 1152, v19\n"
"v_add_u32 v22, v22, v20\n"
"v_lshrrev_b32 v23, 5, v0\n"
"v_mul_u32_u24 v23, 13824, v23\n"
"v_add_u32 v23, v23, v22\n"
"s_mul_i32 s35, s61, 73728\n"
"s_add_u32 s35, s35, 39632896\n"
"s_and_b32 s33, s60, 1\n"
"s_lshl_b32 s33, s33, 3\n"
"s_lshr_b32 s34, s60, 1\n"
"s_xor_b32 s34, s34, s61\n"
"s_and_b32 s34, s34, 3\n"
"s_add_u32 s33, s33, s34\n"
"s_mul_i32 s33, s33, 4608\n"
"s_add_u32 s31, s33, s35\n"
"s_lshl_b32 s33, s60, 7\n"
"s_add_u32 s35, s35, s33\n"
"s_mul_i32 s33, s28, 9216\n"
"s_add_u32 s35, s35, s33\n"
"s_add_u32 s85, s35, 36864\n"
"s_waitcnt vmcnt(11)\n"
"v_mfma_f32_16x16x32_bf16 v[208:211], v[10:13], v[36:39], v[208:211]\n"
"v_mfma_f32_16x16x32_bf16 v[212:215], v[8:11], v[36:39], v[212:215]\n"
"s_waitcnt vmcnt(10)\n"
"v_mfma_f32_16x16x32_bf16 v[216:219], v[10:13], v[40:43], v[216:219]\n"
"v_mfma_f32_16x16x32_bf16 v[220:223], v[8:11], v[40:43], v[220:223]\n"
"s_waitcnt vmcnt(9)\n"
"v_mfma_f32_16x16x32_bf16 v[208:211], v[10:13], v[44:47], v[208:211]\n"
"v_mfma_f32_16x16x32_bf16 v[212:215], v[8:11], v[44:47], v[212:215]\n"
"s_waitcnt vmcnt(8)\n"
"v_mfma_f32_16x16x32_bf16 v[216:219], v[10:13], v[48:51], v[216:219]\n"
"v_mfma_f32_16x16x32_bf16 v[220:223], v[8:11], v[48:51], v[220:223]\n"
"s_waitcnt vmcnt(7)\n"
"v_mfma_f32_16x16x32_bf16 v[208:211], v[10:13], v[52:55], v[208:211]\n"
"v_mfma_f32_16x16x32_bf16 v[212:215], v[8:11], v[52:55], v[212:215]\n"
"s_waitcnt vmcnt(6)\n"
"v_mfma_f32_16x16x32_bf16 v[216:219], v[10:13], v[56:59], v[216:219]\n"
"v_mfma_f32_16x16x32_bf16 v[220:223], v[8:11], v[56:59], v[220:223]\n"
"s_nop 3\n"
"v_cvt_pk_bf16_f32 v208, v208, v209\n"
"v_cvt_pk_bf16_f32 v209, v210, v211\n"
"ds_write_b64 v18, v[208:209] offset:0\n"
"v_cvt_pk_bf16_f32 v212, v212, v213\n"
"v_cvt_pk_bf16_f32 v213, v214, v215\n"
"ds_write_b64 v18, v[212:213] offset:2304\n"
"v_cvt_pk_bf16_f32 v216, v216, v217\n"
"v_cvt_pk_bf16_f32 v217, v218, v219\n"
"ds_write_b64 v18, v[216:217] offset:4608\n"
"v_cvt_pk_bf16_f32 v220, v220, v221\n"
"v_cvt_pk_bf16_f32 v221, v222, v223\n"
"ds_write_b64 v18, v[220:221] offset:6912\n"
"s_waitcnt lgkmcnt(0)\n"
"s_barrier\n"
"ds_read_b128 v[28:31], v21 offset:0\n"
"ds_read_b128 v[32:35], v21 offset:4608\n"
"ds_read_b128 v[36:39], v21 offset:9216\n"
"s_waitcnt lgkmcnt(2)\n"
"buffer_store_dwordx4 v[28:31], v22, s[24:27], s35 offen sc1\n"
"s_waitcnt lgkmcnt(1)\n"
"buffer_store_dwordx4 v[32:35], v22, s[24:27], s85 offen sc1\n"
"s_waitcnt lgkmcnt(0)\n"
"s_cmp_eq_u32 s28, 0\n"
"s_cbranch_scc0 73f\n"
"buffer_store_dwordx4 v[36:39], v23, s[24:27], s31 offen offset:1024 sc1\n"
"73:\n"
"s_add_u32 m0, s79, 7168\n"
"s_add_u32 s81, s78, 384\n"
"buffer_load_dwordx4 v3, s[68:71], s81 offen lds\n"
"s_add_u32 m0, s79, 8192\n"
"s_add_u32 s81, s78, 512\n"
"buffer_load_dwordx4 v2, s[68:71], s81 offen lds\n"
"s_lshl_b32 s33, s61, 4\n"
"s_add_u32 s33, s33, s60\n"
"s_lshl_b32 s33, s33, 3\n"
"s_add_u32 s35, s33, 12288\n"
"v_and_b32 v144, 7, v0\n"
"v_lshlrev_b32 v144, 3, v144\n"
"s_waitcnt vmcnt(2)\n"
"s_barrier\n"
"v_mov_b32 v146, s96\n"
"v_mov_b32 v147, s97\n"
"s_cmp_eq_u32 s28, 0\n"
"s_cbranch_scc0 74f\n"
"s_mov_b64 exec, 1\n"
"buffer_store_dwordx2 v[146:147], v144, s[24:27], s35 offen sc1\n"
"s_mov_b64 exec, -1\n"
"74:\n"
"s_add_u32 m0, s79, 9216\n"
"s_add_u32 s81, s78, 512\n"
"buffer_load_dwordx4 v3, s[68:71], s81 offen lds\n"
"s_add_u32 m0, s79, 10240\n"
"s_add_u32 s81, s78, 640\n"
"buffer_load_dwordx4 v2, s[68:71], s81 offen lds\n"
"s_add_u32 m0, s79, 11264\n"
"s_add_u32 s81, s78, 640\n"
"buffer_load_dwordx4 v3, s[68:71], s81 offen lds\n"
"s_add_u32 m0, s79, 12288\n"
"s_add_u32 s81, s78, 768\n"
"buffer_load_dwordx4 v2, s[68:71], s81 offen lds\n"
"s_add_u32 m0, s79, 13312\n"
"s_add_u32 s81, s78, 768\n"
"buffer_load_dwordx4 v3, s[68:71], s81 offen lds\n"
"s_add_u32 m0, s79, 14336\n"
"s_add_u32 s81, s78, 896\n"
"buffer_load_dwordx4 v2, s[68:71], s81 offen lds\n"
"s_add_u32 m0, s79, 15360\n"
"s_add_u32 s81, s78, 896\n"
"buffer_load_dwordx4 v3, s[68:71], s81 offen lds\n"
"s_mul_i32 s33, s28, 192\n"
"s_add_u32 s33, s33, 158720\n"
"v_lshl_add_u32 v14, v4, 2, s33\n"
"ds_read_b32 v140, v14 offset:0\n"
"ds_read_b32 v141, v14 offset:64\n"
"ds_read_b32 v142, v14 offset:128\n"
"v_lshrrev_b32 v7, 1, v4\n"
"v_xor_b32 v7, v7, v5\n"
"v_lshlrev_b32 v7, 4, v7\n"
"v_lshl_add_u32 v7, v4, 7, v7\n"
"v_mov_b32 v8, v7\n"
"v_xor_b32 v9, 64, v7\n"
"v_add_u32 v10, 55296, v8\n"
"v_add_u32 v11, 55296, v9\n"
"s_mul_i32 s33, s28, 18432\n"
"v_add_u32 v12, s33, v8\n"
"v_add_u32 v13, s33, v9\n"
"v_add_u32 v6, s79, v7\n"
"v_xor_b32 v7, 64, v6\n"
"v_xor_b32 v58, v4, v5\n"
"v_lshlrev_b32 v58, 4, v58\n"
"v_lshl_add_u32 v58, v5, 12, v58\n"
"s_lshl_b32 s33, s58, 7\n"
"v_xor_b32 v58, s33, v58\n"
"v_xor_b32 v59, 64, v58\n"
"s_and_b32 s33, s77, 2\n"
"s_lshr_b32 s35, s76, 5\n"
"s_add_u32 s33, s33, s35\n"
"s_lshl_b32 s33, s33, 7\n"
"s_bfe_u32 s35, s76, 0x30002\n"
"s_add_u32 s33, s33, s35\n"
"s_mul_i32 s66, s33, 9216\n"
"s_add_u32 s66, s66, 16039936\n"
"s_lshl2_add_u32 s35, s57, s28\n"
"s_mul_i32 s35, s35, 73728\n"
"s_add_u32 s35, s35, s66\n"
"s_and_b32 s33, s76, 3\n"
"s_add_u32 s33, s33, 0\n"
"s_xor_b32 s33, s33, s28\n"
"s_lshl_b32 s33, s33, 1\n"
"s_addc_u32 s33, s33, 0\n"
"s_lshl_b32 s33, s33, 10\n"
"s_add_u32 s62, s33, s35\n"
"s_and_b32 s33, s76, 3\n"
"s_add_u32 s33, s33, 1\n"
"s_xor_b32 s33, s33, s28\n"
"s_lshl_b32 s33, s33, 1\n"
"s_addc_u32 s33, s33, 0\n"
"s_lshl_b32 s33, s33, 10\n"
"s_add_u32 s63, s33, s35\n"
"v_add_u32 v14, 74752, v8\n"
"v_add_u32 v15, 74752, v9\n"
"v_mov_b32 v16, 0x3f803f80\n"
"v_mov_b32 v17, 0x3f803f80\n"
"v_mov_b32 v18, 0x3f803f80\n"
"v_mov_b32 v19, 0x3f803f80\n"
"ds_read_b128 v[60:63], v6 offset:0\n"
"ds_read_b128 v[64:67], v7 offset:0\n"
"ds_read_b128 v[68:71], v6 offset:2048\n"
"ds_read_b128 v[72:75], v7 offset:2048\n"
"s_waitcnt lgkmcnt(2)\n"
"s_add_u32 m0, s79, 0\n"
"s_add_u32 s81, s78, 1024\n"
"buffer_load_dwordx4 v2, s[68:71], s81 offen lds\n"
"s_add_u32 m0, s79, 1024\n"
"s_add_u32 s81, s78, 1024\n"
"buffer_load_dwordx4 v3, s[68:71], s81 offen lds\n"
"ds_read_b128 v[76:79], v6 offset:4096\n"
"ds_read_b128 v[80:83], v7 offset:4096\n"
"s_waitcnt lgkmcnt(2)\n"
"s_add_u32 m0, s79, 2048\n"
"s_add_u32 s81, s78, 18432\n"
"buffer_load_dwordx4 v2, s[68:71], s81 offen lds\n"
"s_add_u32 m0, s79, 3072\n"
"s_add_u32 s81, s78, 18432\n"
"buffer_load_dwordx4 v3, s[68:71], s81 offen lds\n"
"s_waitcnt vmcnt(12)\n"
"ds_read_b128 v[84:87], v6 offset:6144\n"
"ds_read_b128 v[88:91], v7 offset:6144\n"
"s_waitcnt lgkmcnt(2)\n"
"s_add_u32 m0, s79, 4096\n"
"s_add_u32 s81, s78, 18560\n"
"buffer_load_dwordx4 v2, s[68:71], s81 offen lds\n"
"s_add_u32 m0, s79, 5120\n"
"s_add_u32 s81, s78, 18560\n"
"buffer_load_dwordx4 v3, s[68:71], s81 offen lds\n"
"s_waitcnt vmcnt(12)\n"
"ds_read_b128 v[92:95], v6 offset:8192\n"
"ds_read_b128 v[96:99], v7 offset:8192\n"
"s_waitcnt lgkmcnt(2)\n"
"s_add_u32 m0, s79, 6144\n"
"s_add_u32 s81, s78, 18688\n"
"buffer_load_dwordx4 v2, s[68:71], s81 offen lds\n"
"s_add_u32 m0, s79, 7168\n"
"s_add_u32 s81, s78, 18688\n"
"buffer_load_dwordx4 v3, s[68:71], s81 offen lds\n"
"s_waitcnt vmcnt(12)\n"
"ds_read_b128 v[100:103], v6 offset:10240\n"
"ds_read_b128 v[104:107], v7 offset:10240\n"
"s_waitcnt lgkmcnt(2)\n"
"s_add_u32 m0, s79, 8192\n"
"s_add_u32 s81, s78, 18816\n"
"buffer_load_dwordx4 v2, s[68:71], s81 offen lds\n"
"s_add_u32 m0, s79, 9216\n"
"s_add_u32 s81, s78, 18816\n"
"buffer_load_dwordx4 v3, s[68:71], s81 offen lds\n"
"s_waitcnt vmcnt(12)\n"
"ds_read_b128 v[108:111], v6 offset:12288\n"
"ds_read_b128 v[112:115], v7 offset:12288\n"
"s_waitcnt lgkmcnt(2)\n"
"s_add_u32 m0, s79, 10240\n"
"s_add_u32 s81, s78, 18944\n"
"buffer_load_dwordx4 v2, s[68:71], s81 offen lds\n"
"s_add_u32 m0, s79, 11264\n"
"s_add_u32 s81, s78, 18944\n"
"buffer_load_dwordx4 v3, s[68:71], s81 offen lds\n"
"s_waitcnt vmcnt(12)\n"
"ds_read_b128 v[116:119], v6 offset:14336\n"
"ds_read_b128 v[120:123], v7 offset:14336\n"
"s_waitcnt lgkmcnt(2)\n"
"s_add_u32 m0, s79, 12288\n"
"s_add_u32 s81, s78, 19072\n"
"buffer_load_dwordx4 v2, s[68:71], s81 offen lds\n"
"s_add_u32 m0, s79, 13312\n"
"s_add_u32 s81, s78, 19072\n"
"buffer_load_dwordx4 v3, s[68:71], s81 offen lds\n"
"s_waitcnt vmcnt(12)\n"
"ds_read_b128 v[124:127], v6 offset:0\n"
"ds_read_b128 v[128:131], v7 offset:0\n"
"s_waitcnt lgkmcnt(2)\n"
"s_add_u32 m0, s79, 14336\n"
"s_add_u32 s81, s78, 19200\n"
"buffer_load_dwordx4 v2, s[68:71], s81 offen lds\n"
"s_add_u32 m0, s79, 15360\n"
"s_add_u32 s81, s78, 19200\n"
"buffer_load_dwordx4 v3, s[68:71], s81 offen lds\n"
"v_bfe_u32 v56, v0, 3, 2\n"
"v_lshrrev_b32 v57, 5, v0\n"
"v_lshl_add_u32 v56, v57, 3, v56\n"
"v_mul_u32_u24 v56, 1152, v56\n"
"v_and_b32 v20, 7, v0\n"
"v_xor_b32 v57, v20, v5\n"
"v_lshl_add_u32 v56, v57, 4, v56\n"
"v_xor_b32 v57, 64, v56\n"
"v_add_u32 v57, 18432, v57\n"
"s_lshr_b32 s33, s28, 1\n"
"s_add_u32 s33, s33, s76\n"
"s_lshl_b32 s33, s33, 3\n"
"s_and_b32 s34, s28, 1\n"
"s_add_u32 s33, s33, s34\n"
"s_mul_i32 s33, s33, 4608\n"
"s_mul_i32 s34, s28, 18432\n"
"s_add_u32 s65, s33, 39632896\n"
"v_add_u32 v21, s34, v1\n"
"s_lshl_b32 s33, s76, 6\n"
"s_add_u32 s33, s33, 12288\n"
"s_cmp_lg_u32 s28, 0\n"
"s_cbranch_scc1 75f\n"
"76:\n"
"buffer_load_dwordx2 v[146:147], v144, s[24:27], s33 offen sc1\n"
"s_waitcnt vmcnt(0)\n"
"v_cmp_ne_u64 vcc, s[96:97], v[146:147]\n"
"s_nop 4\n"
"s_cbranch_vccz 75f\n"
"s_sleep 1\n"
"s_sub_u32 s75, s75, 1\n"
"s_cmp_gt_i32 s75, 0\n"
"s_cbranch_scc1 76b\n"
"75:\n"
"s_barrier\n"
"buffer_load_dwordx4 a[144:147], v56, s[24:27], s65 offen offset:0 sc1\n"
"buffer_load_dwordx4 a[148:151], v57, s[24:27], s65 offen offset:0 sc1\n"
"buffer_load_dwordx4 a[152:155], v56, s[24:27], s65 offen offset:128 sc1\n"
"buffer_load_dwordx4 a[156:159], v57, s[24:27], s65 offen offset:128 sc1\n"
"buffer_load_dwordx4 a[160:163], v56, s[24:27], s65 offen offset:256 sc1\n"
"buffer_load_dwordx4 a[164:167], v57, s[24:27], s65 offen offset:256 sc1\n"
"buffer_load_dwordx4 a[168:171], v56, s[24:27], s65 offen offset:384 sc1\n"
"buffer_load_dwordx4 a[172:175], v57, s[24:27], s65 offen offset:384 sc1\n"
"buffer_load_dwordx4 a[176:179], v56, s[24:27], s65 offen offset:512 sc1\n"
"buffer_load_dwordx4 a[180:183], v57, s[24:27], s65 offen offset:512 sc1\n"
"buffer_load_dwordx4 a[184:187], v56, s[24:27], s65 offen offset:640 sc1\n"
"buffer_load_dwordx4 a[188:191], v57, s[24:27], s65 offen offset:640 sc1\n"
"buffer_load_dwordx4 a[192:195], v56, s[24:27], s65 offen offset:768 sc1\n"
"buffer_load_dwordx4 a[196:199], v57, s[24:27], s65 offen offset:768 sc1\n"
"buffer_load_dwordx4 a[200:203], v56, s[24:27], s65 offen offset:896 sc1\n"
"buffer_load_dwordx4 a[204:207], v57, s[24:27], s65 offen offset:896 sc1\n"
"buffer_load_dwordx4 a[208:211], v56, s[24:27], s65 offen offset:1024 sc1\n"
"buffer_load_dwordx4 a[212:215], v57, s[24:27], s65 offen offset:1024 sc1\n"
"s_waitcnt vmcnt(17)\n"
"ds_write_b128 v21, a[144:147] offset:0\n"
"s_waitcnt vmcnt(16)\n"
"ds_write_b128 v21, a[148:151] offset:1024\n"
"ds_read_b128 a[0:3], v12 offset:0\n"
"ds_read_b128 a[4:7], v14 offset:0\n"
"ds_read_b128 a[8:11], v13 offset:0\n"
"ds_read_b128 a[12:15], v15 offset:0\n"
"s_waitcnt vmcnt(15)\n"
"ds_write_b128 v21, a[152:155] offset:2048\n"
"s_waitcnt vmcnt(14)\n"
"ds_write_b128 v21, a[156:159] offset:3072\n"
"ds_read_b128 a[16:19], v12 offset:2048\n"
"ds_read_b128 a[20:23], v14 offset:2048\n"
"ds_read_b128 a[24:27], v13 offset:2048\n"
"ds_read_b128 a[28:31], v15 offset:2048\n"
"s_waitcnt vmcnt(13)\n"
"ds_write_b128 v21, a[160:163] offset:4096\n"
"s_waitcnt vmcnt(12)\n"
"s_waitcnt lgkmcnt(14)\n"
"ds_write_b128 v21, a[164:167] offset:5120\n"
"s_waitcnt lgkmcnt(11)\n"
"v_mfma_f32_16x16x32_bf16 v[196:199], v[16:19], a[0:3], 0\n"
"ds_read_b128 a[32:35], v12 offset:4096\n"
"v_mfma_f32_16x16x32_bf16 v[200:203], a[0:3], a[0:3], 0\n"
"ds_read_b128 a[36:39], v14 offset:4096\n"
"s_waitcnt lgkmcnt(12)\n"
"v_mfma_f32_16x16x32_bf16 v[212:215], a[0:3], a[4:7], 0\n"
"ds_read_b128 a[40:43], v13 offset:4096\n"
"s_waitcnt lgkmcnt(12)\n"
"v_mfma_f32_16x16x32_bf16 v[196:199], v[16:19], a[8:11], v[196:199]\n"
"ds_read_b128 a[44:47], v15 offset:4096\n"
"v_mfma_f32_16x16x32_bf16 v[200:203], a[8:11], a[8:11], v[200:203]\n"
"s_waitcnt lgkmcnt(12)\n"
"v_mfma_f32_16x16x32_bf16 v[212:215], a[8:11], a[12:15], v[212:215]\n"
"s_waitcnt vmcnt(11)\n"
"ds_write_b128 v21, a[168:171] offset:6144\n"
"s_waitcnt vmcnt(10)\n"
"ds_write_b128 v21, a[172:175] offset:7168\n"
"s_waitcnt lgkmcnt(11)\n"
"v_mfma_f32_16x16x32_bf16 v[196:199], v[16:19], a[16:19], v[196:199]\n"
"ds_read_b128 a[48:51], v12 offset:6144\n"
"v_mfma_f32_16x16x32_bf16 v[200:203], a[16:19], a[16:19], v[200:203]\n"
"ds_read_b128 a[52:55], v14 offset:6144\n"
"s_waitcnt lgkmcnt(12)\n"
"v_mfma_f32_16x16x32_bf16 v[212:215], a[16:19], a[20:23], v[212:215]\n"
"ds_read_b128 a[56:59], v13 offset:6144\n"
"s_waitcnt lgkmcnt(12)\n"
"v_mfma_f32_16x16x32_bf16 v[196:199], v[16:19], a[24:27], v[196:199]\n"
"ds_read_b128 a[60:63], v15 offset:6144\n"
"v_mfma_f32_16x16x32_bf16 v[200:203], a[24:27], a[24:27], v[200:203]\n"
"s_waitcnt lgkmcnt(12)\n"
"v_mfma_f32_16x16x32_bf16 v[212:215], a[24:27], a[28:31], v[212:215]\n"
"s_add_u32 m0, s79, 0\n"
"s_add_u32 s81, s78, 19328\n"
"buffer_load_dwordx4 v2, s[68:71], s81 offen lds\n"
"s_add_u32 m0, s79, 1024\n"
"s_add_u32 s81, s78, 19328\n"
"buffer_load_dwordx4 v3, s[68:71], s81 offen lds\n"
"s_waitcnt vmcnt(11)\n"
"ds_write_b128 v21, a[176:179] offset:8192\n"
"s_waitcnt vmcnt(10)\n"
"ds_write_b128 v21, a[180:183] offset:9216\n"
"s_waitcnt lgkmcnt(11)\n"
"v_mfma_f32_16x16x32_bf16 v[196:199], v[16:19], a[32:35], v[196:199]\n"
"ds_read_b128 a[64:67], v12 offset:8192\n"
"v_mfma_f32_16x16x32_bf16 v[200:203], a[32:35], a[32:35], v[200:203]\n"
"ds_read_b128 a[68:71], v14 offset:8192\n"
"s_waitcnt lgkmcnt(12)\n"
"v_mfma_f32_16x16x32_bf16 v[212:215], a[32:35], a[36:39], v[212:215]\n"
"ds_read_b128 a[72:75], v13 offset:8192\n"
"s_waitcnt lgkmcnt(12)\n"
"v_mfma_f32_16x16x32_bf16 v[196:199], v[16:19], a[40:43], v[196:199]\n"
"ds_read_b128 a[76:79], v15 offset:8192\n"
"v_mfma_f32_16x16x32_bf16 v[200:203], a[40:43], a[40:43], v[200:203]\n"
"s_waitcnt lgkmcnt(12)\n"
"v_mfma_f32_16x16x32_bf16 v[212:215], a[40:43], a[44:47], v[212:215]\n"
"ds_read_b128 v[132:135], v6 offset:2048\n"
"ds_read_b128 v[136:139], v7 offset:2048\n"
"s_waitcnt lgkmcnt(0)\n"
"s_add_u32 m0, s79, 2048\n"
"s_add_u32 s81, s78, 19456\n"
"buffer_load_dwordx4 v2, s[68:71], s81 offen lds\n"
"s_add_u32 m0, s79, 3072\n"
"s_add_u32 s81, s78, 19456\n"
"buffer_load_dwordx4 v3, s[68:71], s81 offen lds\n"
"s_waitcnt vmcnt(11)\n"
"ds_write_b128 v21, a[184:187] offset:10240\n"
"s_waitcnt vmcnt(10)\n"
"ds_write_b128 v21, a[188:191] offset:11264\n"
"v_mfma_f32_16x16x32_bf16 v[196:199], v[16:19], a[48:51], v[196:199]\n"
"ds_read_b128 a[80:83], v12 offset:10240\n"
"v_mfma_f32_16x16x32_bf16 v[200:203], a[48:51], a[48:51], v[200:203]\n"
"ds_read_b128 a[84:87], v14 offset:10240\n"
"v_mfma_f32_16x16x32_bf16 v[212:215], a[48:51], a[52:55], v[212:215]\n"
"ds_read_b128 a[88:91], v13 offset:10240\n"
"v_mfma_f32_16x16x32_bf16 v[196:199], v[16:19], a[56:59], v[196:199]\n"
"ds_read_b128 a[92:95], v15 offset:10240\n"
"v_mfma_f32_16x16x32_bf16 v[200:203], a[56:59], a[56:59], v[200:203]\n"
"v_mfma_f32_16x16x32_bf16 v[212:215], a[56:59], a[60:63], v[212:215]\n"
"s_waitcnt vmcnt(9)\n"
"ds_write_b128 v21, a[192:195] offset:12288\n"
"s_waitcnt vmcnt(8)\n"
"ds_write_b128 v21, a[196:199] offset:13312\n"
"v_mfma_f32_16x16x32_bf16 v[196:199], v[16:19], a[64:67], v[196:199]\n"
"ds_read_b128 a[96:99], v12 offset:12288\n"
"v_mfma_f32_16x16x32_bf16 v[200:203], a[64:67], a[64:67], v[200:203]\n"
"ds_read_b128 a[100:103], v14 offset:12288\n"
"v_mfma_f32_16x16x32_bf16 v[212:215], a[64:67], a[68:71], v[212:215]\n"
"ds_read_b128 a[104:107], v13 offset:12288\n"
"v_mfma_f32_16x16x32_bf16 v[196:199], v[16:19], a[72:75], v[196:199]\n"
"ds_read_b128 a[108:111], v15 offset:12288\n"
"v_mfma_f32_16x16x32_bf16 v[200:203], a[72:75], a[72:75], v[200:203]\n"
"v_mfma_f32_16x16x32_bf16 v[212:215], a[72:75], a[76:79], v[212:215]\n"
"s_waitcnt vmcnt(7)\n"
"ds_write_b128 v21, a[200:203] offset:14336\n"
"s_waitcnt vmcnt(6)\n"
"ds_write_b128 v21, a[204:207] offset:15360\n"
"s_waitcnt lgkmcnt(11)\n"
"v_mfma_f32_16x16x32_bf16 v[196:199], v[16:19], a[80:83], v[196:199]\n"
"ds_read_b128 a[112:115], v12 offset:14336\n"
"v_mfma_f32_16x16x32_bf16 v[200:203], a[80:83], a[80:83], v[200:203]\n"
"ds_read_b128 a[116:119], v14 offset:14336\n"
"s_waitcnt lgkmcnt(12)\n"
"v_mfma_f32_16x16x32_bf16 v[212:215], a[80:83], a[84:87], v[212:215]\n"
"ds_read_b128 a[120:123], v13 offset:14336\n"
"s_waitcnt lgkmcnt(12)\n"
"v_mfma_f32_16x16x32_bf16 v[196:199], v[16:19], a[88:91], v[196:199]\n"
"ds_read_b128 a[124:127], v15 offset:14336\n"
"v_mfma_f32_16x16x32_bf16 v[200:203], a[88:91], a[88:91], v[200:203]\n"
"s_waitcnt lgkmcnt(12)\n"
"v_mfma_f32_16x16x32_bf16 v[212:215], a[88:91], a[92:95], v[212:215]\n"
"s_waitcnt vmcnt(5)\n"
"ds_write_b128 v21, a[208:211] offset:16384\n"
"s_waitcnt vmcnt(4)\n"
"ds_write_b128 v21, a[212:215] offset:17408\n"
"s_waitcnt lgkmcnt(11)\n"
"v_mfma_f32_16x16x32_bf16 v[196:199], v[16:19], a[96:99], v[196:199]\n"
"ds_read_b128 a[128:131], v12 offset:16384\n"
"v_mfma_f32_16x16x32_bf16 v[200:203], a[96:99], a[96:99], v[200:203]\n"
"ds_read_b128 a[132:135], v14 offset:16384\n"
"s_waitcnt lgkmcnt(12)\n"
"v_mfma_f32_16x16x32_bf16 v[212:215], a[96:99], a[100:103], v[212:215]\n"
"ds_read_b128 a[136:139], v13 offset:16384\n"
"s_waitcnt lgkmcnt(12)\n"
"v_mfma_f32_16x16x32_bf16 v[196:199], v[16:19], a[104:107], v[196:199]\n"
"ds_read_b128 a[140:143], v15 offset:16384\n"
"v_mfma_f32_16x16x32_bf16 v[200:203], a[104:107], a[104:107], v[200:203]\n"
"s_waitcnt lgkmcnt(12)\n"
"v_mfma_f32_16x16x32_bf16 v[212:215], a[104:107], a[108:111], v[212:215]\n"
"s_waitcnt lgkmcnt(4)\n"
"s_barrier\n"
"ds_read_b128 a[0:3], v8 offset:0\n"
"ds_read_b128 a[4:7], v8 offset:18432\n"
"ds_read_b128 a[8:11], v8 offset:36864\n"
"ds_read_b128 a[12:15], v10 offset:0\n"
"ds_read_b128 a[16:19], v9 offset:0\n"
"ds_read_b128 a[20:23], v9 offset:18432\n"
"ds_read_b128 a[24:27], v9 offset:36864\n"
"ds_read_b128 a[28:31], v11 offset:0\n"
"ds_read_b128 a[32:35], v8 offset:2048\n"
"ds_read_b128 a[36:39], v8 offset:20480\n"
"ds_read_b128 a[40:43], v8 offset:38912\n"
"s_waitcnt lgkmcnt(14)\n"
"ds_read_b128 a[44:47], v10 offset:2048\n"
"v_mfma_f32_16x16x32_bf16 v[196:199], v[16:19], a[112:115], v[196:199]\n"
"v_mfma_f32_16x16x32_bf16 v[200:203], a[112:115], a[112:115], v[200:203]\n"
"v_mfma_f32_16x16x32_bf16 v[212:215], a[112:115], a[116:119], v[212:215]\n"
"v_mfma_f32_16x16x32_bf16 v[196:199], v[16:19], a[120:123], v[196:199]\n"
"v_mfma_f32_16x16x32_bf16 v[200:203], a[120:123], a[120:123], v[200:203]\n"
"v_mfma_f32_16x16x32_bf16 v[212:215], a[120:123], a[124:127], v[212:215]\n"
"v_mfma_f32_16x16x32_bf16 v[196:199], v[16:19], a[128:131], v[196:199]\n"
"v_mfma_f32_16x16x32_bf16 v[200:203], a[128:131], a[128:131], v[200:203]\n"
"s_waitcnt lgkmcnt(14)\n"
"v_mfma_f32_16x16x32_bf16 v[212:215], a[128:131], a[132:135], v[212:215]\n"
"s_waitcnt lgkmcnt(13)\n"
"v_mfma_f32_16x16x32_bf16 v[196:199], v[16:19], a[136:139], v[196:199]\n"
"v_mfma_f32_16x16x32_bf16 v[200:203], a[136:139], a[136:139], v[200:203]\n"
"s_waitcnt lgkmcnt(12)\n"
"v_mfma_f32_16x16x32_bf16 v[212:215], a[136:139], a[140:143], v[212:215]\n"
"s_nop 4\n"
"v_mov_b32 v20, v196\n"
"v_mov_b32 v21, v200\n"
"s_mov_b32 exec_lo, 0x200002\n"
"s_mov_b32 exec_hi, 0x20000200\n"
"v_mov_b32 v21, v201\n"
"s_mov_b32 exec_lo, 0x400004\n"
"s_mov_b32 exec_hi, 0x40000400\n"
"v_mov_b32 v21, v202\n"
"s_mov_b32 exec_lo, 0x800008\n"
"s_mov_b32 exec_hi, 0x80000800\n"
"v_mov_b32 v21, v203\n"
"s_mov_b64 exec, -1\n"
"v_mul_f32 v20, 0.001736111111111111, v20\n"
"v_mul_f32 v22, v20, v20\n"
"v_mul_f32 v21, 0.001736111111111111, v21\n"
"v_sub_f32 v21, v21, v22\n"
"v_max_f32 v21, 0, v21\n"
"v_add_f32 v21, 0x3727c5ac, v21\n"
"v_rsq_f32 v21, v21\n"
"s_nop 0\n"
"v_mul_f32 v20, -v20, v21\n"
"s_lshl_b32 s33, s77, 30\n"
"v_xor_b32 v20, s33, v20\n"
"v_xor_b32 v21, s33, v21\n"
"s_lshl_b32 s33, s28, 7\n"
"s_add_u32 s33, s33, 73728\n"
"v_lshl_add_u32 v22, v4, 3, s33\n"
"s_mov_b32 exec_lo, 0xf0000f\n"
"s_mov_b32 exec_hi, 0xf0000f00\n"
"ds_write_b64 v22, v[20:21]\n"
"s_mov_b64 exec, -1\n"
"v_lshl_add_u32 v23, v5, 5, s33\n"
"v_lshlrev_b32 v22, 5, v5\n"
"v_add_u32 v22, 73728, v22\n"
"ds_read_b128 a[48:51], v9 offset:2048\n"
"ds_read_b128 a[52:55], v9 offset:20480\n"
"s_waitcnt lgkmcnt(14)\n"
"ds_read_b128 a[56:59], v9 offset:38912\n"
"s_waitcnt lgkmcnt(14)\n"
"ds_read_b128 a[60:63], v11 offset:2048\n"
"v_mfma_f32_16x16x32_bf16 v[156:159], a[0:3], v[60:63], 0\n"
"s_waitcnt lgkmcnt(12)\n"
"ds_read_b128 a[64:67], v8 offset:4096\n"
"ds_read_b128 a[68:71], v8 offset:22528\n"
"v_mfma_f32_16x16x32_bf16 v[156:159], a[16:19], v[64:67], v[156:159]\n"
"s_waitcnt lgkmcnt(13)\n"
"ds_read_b128 a[72:75], v8 offset:40960\n"
"ds_read_b128 a[76:79], v10 offset:4096\n"
"v_mfma_f32_16x16x32_bf16 v[160:163], a[20:23], v[64:67], 0\n"
"s_waitcnt lgkmcnt(14)\n"
"ds_read_b128 a[80:83], v9 offset:4096\n"
"s_waitcnt lgkmcnt(14)\n"
"ds_read_b128 a[84:87], v9 offset:22528\n"
"v_mfma_f32_16x16x32_bf16 v[160:163], a[4:7], v[60:63], v[160:163]\n"
"s_waitcnt lgkmcnt(14)\n"
"ds_read_b128 a[88:91], v9 offset:40960\n"
"s_waitcnt lgkmcnt(14)\n"
"ds_read_b128 a[92:95], v11 offset:4096\n"
"v_mfma_f32_16x16x32_bf16 v[164:167], a[8:11], v[60:63], 0\n"
"v_mfma_f32_16x16x32_bf16 v[164:167], a[24:27], v[64:67], v[164:167]\n"
"v_mfma_f32_16x16x32_bf16 v[168:171], a[28:31], v[64:67], 0\n"
"v_mfma_f32_16x16x32_bf16 v[168:171], a[12:15], v[60:63], v[168:171]\n"
"s_waitcnt lgkmcnt(13)\n"
"v_mfma_f32_16x16x32_bf16 v[168:171], a[44:47], v[68:71], v[168:171]\n"
"s_waitcnt lgkmcnt(8)\n"
"ds_read_b128 a[96:99], v8 offset:6144\n"
"ds_read_b128 a[100:103], v8 offset:24576\n"
"v_mfma_f32_16x16x32_bf16 v[168:171], a[60:63], v[72:75], v[168:171]\n"
"ds_read_b128 a[104:107], v8 offset:43008\n"
"ds_read_b128 a[108:111], v10 offset:6144\n"
"v_mfma_f32_16x16x32_bf16 v[164:167], a[56:59], v[72:75], v[164:167]\n"
"ds_read_b128 a[112:115], v9 offset:6144\n"
"ds_read_b128 a[116:119], v9 offset:24576\n"
"v_mfma_f32_16x16x32_bf16 v[164:167], a[40:43], v[68:71], v[164:167]\n"
"ds_read_b128 a[120:123], v9 offset:43008\n"
"s_waitcnt lgkmcnt(14)\n"
"ds_read_b128 a[124:127], v11 offset:6144\n"
"v_mfma_f32_16x16x32_bf16 v[160:163], a[36:39], v[68:71], v[160:163]\n"
"v_mfma_f32_16x16x32_bf16 v[160:163], a[52:55], v[72:75], v[160:163]\n"
"v_mfma_f32_16x16x32_bf16 v[156:159], a[48:51], v[72:75], v[156:159]\n"
"v_mfma_f32_16x16x32_bf16 v[156:159], a[32:35], v[68:71], v[156:159]\n"
"v_mfma_f32_16x16x32_bf16 v[156:159], a[64:67], v[76:79], v[156:159]\n"
"s_waitcnt lgkmcnt(11)\n"
"ds_read_b128 a[128:131], v8 offset:8192\n"
"ds_read_b128 a[132:135], v8 offset:26624\n"
"v_mfma_f32_16x16x32_bf16 v[156:159], a[80:83], v[80:83], v[156:159]\n"
"s_waitcnt lgkmcnt(12)\n"
"ds_read_b128 a[136:139], v8 offset:45056\n"
"ds_read_b128 a[140:143], v10 offset:8192\n"
"v_mfma_f32_16x16x32_bf16 v[160:163], a[84:87], v[80:83], v[160:163]\n"
"ds_read_b128 a[144:147], v9 offset:8192\n"
"s_waitcnt lgkmcnt(14)\n"
"ds_read_b128 a[148:151], v9 offset:26624\n"
"v_mfma_f32_16x16x32_bf16 v[160:163], a[68:71], v[76:79], v[160:163]\n"
"s_waitcnt lgkmcnt(14)\n"
"ds_read_b128 a[152:155], v9 offset:45056\n"
"s_waitcnt lgkmcnt(14)\n"
"ds_read_b128 a[156:159], v11 offset:8192\n"
"v_mfma_f32_16x16x32_bf16 v[164:167], a[72:75], v[76:79], v[164:167]\n"
".Lp0_3_%=:\n"
"s_cbranch_execz .Lpret%=\n"
"v_mfma_f32_16x16x32_bf16 v[164:167], a[88:91], v[80:83], v[164:167]\n"
"v_mfma_f32_16x16x32_bf16 v[168:171], a[92:95], v[80:83], v[168:171]\n"
"v_mfma_f32_16x16x32_bf16 v[168:171], a[76:79], v[76:79], v[168:171]\n"
"s_waitcnt lgkmcnt(12)\n"
"s_barrier\n"
"v_mfma_f32_16x16x32_bf16 v[168:171], a[108:111], v[84:87], v[168:171]\n"
"s_waitcnt lgkmcnt(8)\n"
"ds_read_b128 a[160:163], v8 offset:10240\n"
"ds_read_b128 a[164:167], v8 offset:28672\n"
"v_mfma_f32_16x16x32_bf16 v[168:171], a[124:127], v[88:91], v[168:171]\n"
"ds_read_b128 a[168:171], v8 offset:47104\n"
"ds_read_b128 a[172:175], v10 offset:10240\n"
"v_mfma_f32_16x16x32_bf16 v[164:167], a[120:123], v[88:91], v[164:167]\n"
"ds_read_b128 a[176:179], v9 offset:10240\n"
"ds_read_b128 a[180:183], v9 offset:28672\n"
"v_mfma_f32_16x16x32_bf16 v[164:167], a[104:107], v[84:87], v[164:167]\n"
"ds_read_b128 a[184:187], v9 offset:47104\n"
"s_waitcnt lgkmcnt(14)\n"
"ds_read_b128 a[188:191], v11 offset:10240\n"
"v_mfma_f32_16x16x32_bf16 v[160:163], a[100:103], v[84:87], v[160:163]\n"
"s_waitcnt lgkmcnt(14)\n"
"ds_read_b128 v[196:199], v23 offset:0\n"
"s_waitcnt lgkmcnt(14)\n"
"ds_read_b128 v[200:203], v23 offset:16\n"
"v_mfma_f32_16x16x32_bf16 v[160:163], a[116:119], v[88:91], v[160:163]\n"
"s_waitcnt lgkmcnt(14)\n"
"ds_read_b128 v[24:27], v22 offset:0\n"
"s_waitcnt lgkmcnt(14)\n"
"ds_read_b128 v[28:31], v22 offset:16\n"
"v_mfma_f32_16x16x32_bf16 v[156:159], a[112:115], v[88:91], v[156:159]\n"
"s_waitcnt lgkmcnt(14)\n"
"ds_read_b128 v[32:35], v22 offset:128\n"
"s_waitcnt lgkmcnt(14)\n"
"ds_read_b128 v[36:39], v22 offset:144\n"
"v_mfma_f32_16x16x32_bf16 v[156:159], a[96:99], v[84:87], v[156:159]\n"
"s_waitcnt lgkmcnt(14)\n"
"ds_read_b128 v[40:43], v22 offset:256\n"
"s_waitcnt lgkmcnt(14)\n"
"ds_read_b128 v[44:47], v22 offset:272\n"
"v_mfma_f32_16x16x32_bf16 v[156:159], a[128:131], v[92:95], v[156:159]\n"
"s_waitcnt lgkmcnt(14)\n"
"ds_read_b128 a[192:195], v8 offset:12288\n"
"s_waitcnt lgkmcnt(14)\n"
"ds_read_b128 a[196:199], v8 offset:30720\n"
"v_mfma_f32_16x16x32_bf16 v[156:159], a[144:147], v[96:99], v[156:159]\n"
"s_waitcnt lgkmcnt(14)\n"
"ds_read_b128 a[200:203], v8 offset:49152\n"
"s_waitcnt lgkmcnt(14)\n"
"ds_read_b128 a[204:207], v10 offset:12288\n"
"v_mfma_f32_16x16x32_bf16 v[160:163], a[148:151], v[96:99], v[160:163]\n"
"s_waitcnt lgkmcnt(14)\n"
"ds_read_b128 a[208:211], v9 offset:12288\n"
"s_waitcnt lgkmcnt(14)\n"
"ds_read_b128 a[212:215], v9 offset:30720\n"
"v_mfma_f32_16x16x32_bf16 v[160:163], a[132:135], v[92:95], v[160:163]\n"
"s_waitcnt lgkmcnt(14)\n"
"ds_read_b128 a[216:219], v9 offset:49152\n"
"s_waitcnt lgkmcnt(14)\n"
"ds_read_b128 a[220:223], v11 offset:12288\n"
"v_mfma_f32_16x16x32_bf16 v[164:167], a[136:139], v[92:95], v[164:167]\n"
"s_waitcnt lgkmcnt(14)\n"
"ds_read_b128 v[48:51], v22 offset:384\n"
"s_waitcnt lgkmcnt(14)\n"
"ds_read_b128 v[52:55], v22 offset:400\n"
"v_mfma_f32_16x16x32_bf16 v[164:167], a[152:155], v[96:99], v[164:167]\n"
"v_mul_f32 v196, v196, v142\n"
"v_mul_f32 v198, v198, v142\n"
"v_mfma_f32_16x16x32_bf16 v[168:171], a[156:159], v[96:99], v[168:171]\n"
"v_mul_f32 v200, v200, v142\n"
"v_mul_f32 v202, v202, v142\n"
"v_mfma_f32_16x16x32_bf16 v[168:171], a[140:143], v[92:95], v[168:171]\n"
"v_fmac_f32 v196, v197, v212\n"
"v_fmac_f32 v198, v199, v213\n"
"v_mfma_f32_16x16x32_bf16 v[168:171], a[172:175], v[100:103], v[168:171]\n"
"s_waitcnt lgkmcnt(14)\n"
"ds_read_b128 a[224:227], v8 offset:14336\n"
"s_waitcnt lgkmcnt(14)\n"
"ds_read_b128 a[228:231], v8 offset:32768\n"
"v_mfma_f32_16x16x32_bf16 v[168:171], a[188:191], v[104:107], v[168:171]\n"
"s_waitcnt lgkmcnt(14)\n"
"ds_read_b128 a[232:235], v8 offset:51200\n"
"s_waitcnt lgkmcnt(14)\n"
"ds_read_b128 a[236:239], v10 offset:14336\n"
"v_mfma_f32_16x16x32_bf16 v[164:167], a[184:187], v[104:107], v[164:167]\n"
"s_waitcnt lgkmcnt(14)\n"
"ds_read_b128 a[240:243], v9 offset:14336\n"
"s_waitcnt lgkmcnt(14)\n"
"ds_read_b128 a[244:247], v9 offset:32768\n"
"v_mfma_f32_16x16x32_bf16 v[164:167], a[168:171], v[100:103], v[164:167]\n"
"s_waitcnt lgkmcnt(14)\n"
"ds_read_b128 a[248:251], v9 offset:51200\n"
"s_waitcnt lgkmcnt(14)\n"
"ds_read_b128 a[252:255], v11 offset:14336\n"
"v_mfma_f32_16x16x32_bf16 v[160:163], a[164:167], v[100:103], v[160:163]\n"
"v_fmac_f32 v200, v201, v214\n"
"v_fmac_f32 v202, v203, v215\n"
"v_mfma_f32_16x16x32_bf16 v[160:163], a[180:183], v[104:107], v[160:163]\n"
"v_cvt_pk_bf16_f32 v220, v196, v198\n"
"v_cvt_pk_bf16_f32 v221, v200, v202\n"
"v_mfma_f32_16x16x32_bf16 v[156:159], a[176:179], v[104:107], v[156:159]\n"
"s_add_u32 s33, s76, s59\n"
"s_and_b32 s33, s33, 3\n"
"s_lshl2_add_u32 s33, s57, s33\n"
"s_mul_i32 s33, s33, 73728\n"
"s_add_u32 s33, s33, s66\n"
"s_lshl3_add_u32 s33, s58, s33\n"
"buffer_store_dwordx2 v[220:221], v1, s[24:27], s33 offen offset:2048 sc1\n"
"v_mfma_f32_16x16x32_bf16 v[156:159], a[160:163], v[100:103], v[156:159]\n"
"v_mfma_f32_16x16x32_bf16 v[156:159], a[192:195], v[108:111], v[156:159]\n"
"s_waitcnt lgkmcnt(13)\n"
"ds_read_b128 v[224:227], v8 offset:16384\n"
"ds_read_b128 v[228:231], v8 offset:34816\n"
"v_mfma_f32_16x16x32_bf16 v[156:159], a[208:211], v[112:115], v[156:159]\n"
"s_waitcnt lgkmcnt(14)\n"
"ds_read_b128 v[232:235], v8 offset:53248\n"
"s_waitcnt lgkmcnt(14)\n"
"ds_read_b128 v[236:239], v10 offset:16384\n"
"v_mfma_f32_16x16x32_bf16 v[160:163], a[212:215], v[112:115], v[160:163]\n"
"s_waitcnt lgkmcnt(14)\n"
"ds_read_b128 v[240:243], v9 offset:16384\n"
"s_waitcnt lgkmcnt(14)\n"
"ds_read_b128 v[244:247], v9 offset:34816\n"
"v_mfma_f32_16x16x32_bf16 v[160:163], a[196:199], v[108:111], v[160:163]\n"
"s_waitcnt lgkmcnt(14)\n"
"ds_read_b128 v[248:251], v9 offset:53248\n"
"s_waitcnt lgkmcnt(14)\n"
"ds_read_b128 v[252:255], v11 offset:16384\n"
"v_mfma_f32_16x16x32_bf16 v[164:167], a[200:203], v[108:111], v[164:167]\n"
"v_mfma_f32_16x16x32_bf16 v[164:167], a[216:219], v[112:115], v[164:167]\n"
"v_mfma_f32_16x16x32_bf16 v[168:171], a[220:223], v[112:115], v[168:171]\n"
"v_mfma_f32_16x16x32_bf16 v[168:171], a[204:207], v[108:111], v[168:171]\n"
"s_waitcnt lgkmcnt(12)\n"
"v_mfma_f32_16x16x32_bf16 v[168:171], a[236:239], v[116:119], v[168:171]\n"
"s_waitcnt lgkmcnt(8)\n"
"v_mfma_f32_16x16x32_bf16 v[168:171], a[252:255], v[120:123], v[168:171]\n"
"v_mfma_f32_16x16x32_bf16 v[164:167], a[248:251], v[120:123], v[164:167]\n"
"v_mfma_f32_16x16x32_bf16 v[164:167], a[232:235], v[116:119], v[164:167]\n"
"v_mfma_f32_16x16x32_bf16 v[160:163], a[228:231], v[116:119], v[160:163]\n"
"v_mfma_f32_16x16x32_bf16 v[160:163], a[244:247], v[120:123], v[160:163]\n"
"v_mfma_f32_16x16x32_bf16 v[156:159], a[240:243], v[120:123], v[156:159]\n"
"v_mfma_f32_16x16x32_bf16 v[156:159], a[224:227], v[116:119], v[156:159]\n"
"s_waitcnt lgkmcnt(7)\n"
"v_mfma_f32_16x16x32_bf16 v[156:159], v[224:227], v[124:127], v[156:159]\n"
"s_waitcnt lgkmcnt(3)\n"
"ds_read_b128 v[60:63], v6 offset:4096\n"
"ds_read_b128 v[64:67], v7 offset:4096\n"
"v_mfma_f32_16x16x32_bf16 v[156:159], v[240:243], v[128:131], v[156:159]\n"
"s_waitcnt lgkmcnt(4)\n"
"v_mfma_f32_16x16x32_bf16 v[160:163], v[244:247], v[128:131], v[160:163]\n"
"v_mfma_f32_16x16x32_bf16 v[160:163], v[228:231], v[124:127], v[160:163]\n"
"v_mfma_f32_16x16x32_bf16 v[164:167], v[232:235], v[124:127], v[164:167]\n"
"s_waitcnt lgkmcnt(3)\n"
"v_mfma_f32_16x16x32_bf16 v[164:167], v[248:251], v[128:131], v[164:167]\n"
"s_waitcnt lgkmcnt(2)\n"
"v_mfma_f32_16x16x32_bf16 v[168:171], v[252:255], v[128:131], v[168:171]\n"
"v_mfma_f32_16x16x32_bf16 v[168:171], v[236:239], v[124:127], v[168:171]\n"
"v_mfma_f32_16x16x32_bf16 v[172:175], a[0:3], v[132:135], 0\n"
"ds_read_b128 v[68:71], v6 offset:6144\n"
"ds_read_b128 v[72:75], v7 offset:6144\n"
"v_mfma_f32_16x16x32_bf16 v[172:175], a[16:19], v[136:139], v[172:175]\n"
"v_mfma_f32_16x16x32_bf16 v[176:179], a[20:23], v[136:139], 0\n"
"v_mfma_f32_16x16x32_bf16 v[176:179], a[4:7], v[132:135], v[176:179]\n"
"v_mfma_f32_16x16x32_bf16 v[180:183], a[8:11], v[132:135], 0\n"
"v_mul_f32 v196, v24, v140\n"
"v_mul_f32 v197, v26, v140\n"
"v_mfma_f32_16x16x32_bf16 v[180:183], a[24:27], v[136:139], v[180:183]\n"
"v_fmac_f32 v196, v25, v156\n"
"v_fmac_f32 v197, v27, v157\n"
"v_mfma_f32_16x16x32_bf16 v[184:187], a[28:31], v[136:139], 0\n"
"v_mul_f32 v198, v28, v140\n"
"v_mul_f32 v199, v30, v140\n"
"v_mfma_f32_16x16x32_bf16 v[184:187], a[12:15], v[132:135], v[184:187]\n"
"s_waitcnt lgkmcnt(3)\n"
"v_fmac_f32 v198, v29, v158\n"
"v_fmac_f32 v199, v31, v159\n"
"s_barrier\n"
"v_mfma_f32_16x16x32_bf16 v[184:187], a[44:47], v[60:63], v[184:187]\n"
"s_waitcnt lgkmcnt(2)\n"
"ds_read_b128 v[76:79], v6 offset:8192\n"
"ds_read_b128 v[80:83], v7 offset:8192\n"
"v_mfma_f32_16x16x32_bf16 v[184:187], a[60:63], v[64:67], v[184:187]\n"
"s_add_u32 m0, s46, 0\n"
"s_xor_b32 s47, s44, 262144\n"
"buffer_load_dwordx4 v58, s[36:39], s47 offen  lds\n"
"v_mfma_f32_16x16x32_bf16 v[180:183], a[56:59], v[64:67], v[180:183]\n"
"s_add_u32 m0, s46, 1024\n"
"s_xor_b32 s47, s44, 278528\n"
"buffer_load_dwordx4 v59, s[36:39], s47 offen  lds\n"
"v_mfma_f32_16x16x32_bf16 v[180:183], a[40:43], v[60:63], v[180:183]\n"
"v_mul_f32 v200, v32, v140\n"
"v_mul_f32 v201, v34, v140\n"
"v_mfma_f32_16x16x32_bf16 v[176:179], a[36:39], v[60:63], v[176:179]\n"
"v_fmac_f32 v200, v33, v160\n"
"v_fmac_f32 v201, v35, v161\n"
"v_mfma_f32_16x16x32_bf16 v[176:179], a[52:55], v[64:67], v[176:179]\n"
"v_mul_f32 v202, v36, v140\n"
"v_mul_f32 v203, v38, v140\n"
"v_mfma_f32_16x16x32_bf16 v[172:175], a[48:51], v[64:67], v[172:175]\n"
"v_fmac_f32 v202, v37, v162\n"
"v_fmac_f32 v203, v39, v163\n"
"v_mfma_f32_16x16x32_bf16 v[172:175], a[32:35], v[60:63], v[172:175]\n"
"s_waitcnt lgkmcnt(3)\n"
"v_cvt_pk_bf16_f32 v204, v196, v197\n"
"v_cvt_pk_bf16_f32 v205, v198, v199\n"
"v_mfma_f32_16x16x32_bf16 v[172:175], a[64:67], v[68:71], v[172:175]\n"
"s_waitcnt lgkmcnt(2)\n"
"ds_read_b128 v[84:87], v6 offset:10240\n"
"ds_read_b128 v[88:91], v7 offset:10240\n"
"v_mfma_f32_16x16x32_bf16 v[172:175], a[80:83], v[72:75], v[172:175]\n"
"s_add_u32 m0, s46, 8192\n"
"s_xor_b32 s47, s44, 393216\n"
"buffer_load_dwordx4 v58, s[36:39], s47 offen  lds\n"
"v_mfma_f32_16x16x32_bf16 v[176:179], a[84:87], v[72:75], v[176:179]\n"
"s_add_u32 m0, s46, 9216\n"
"s_xor_b32 s47, s44, 409600\n"
"buffer_load_dwordx4 v59, s[36:39], s47 offen  lds\n"
"v_mfma_f32_16x16x32_bf16 v[176:179], a[68:71], v[68:71], v[176:179]\n"
"v_cvt_pk_bf16_f32 v206, v200, v201\n"
"v_cvt_pk_bf16_f32 v207, v202, v203\n"
"v_mfma_f32_16x16x32_bf16 v[180:183], a[72:75], v[68:71], v[180:183]\n"
"buffer_store_dwordx4 v[204:207], v1, s[24:27], s62 offen offset:0 sc1\n"
"v_mfma_f32_16x16x32_bf16 v[180:183], a[88:91], v[72:75], v[180:183]\n"
"v_mul_f32 v196, v40, v140\n"
"v_mul_f32 v197, v42, v140\n"
"v_mfma_f32_16x16x32_bf16 v[184:187], a[92:95], v[72:75], v[184:187]\n"
"v_fmac_f32 v196, v41, v164\n"
"v_fmac_f32 v197, v43, v165\n"
"v_mfma_f32_16x16x32_bf16 v[184:187], a[76:79], v[68:71], v[184:187]\n"
"s_waitcnt lgkmcnt(3)\n"
"v_mul_f32 v198, v44, v140\n"
"v_mul_f32 v199, v46, v140\n"
"v_mfma_f32_16x16x32_bf16 v[184:187], a[108:111], v[76:79], v[184:187]\n"
"s_waitcnt lgkmcnt(2)\n"
"ds_read_b128 v[92:95], v6 offset:12288\n"
"ds_read_b128 v[96:99], v7 offset:12288\n"
"v_mfma_f32_16x16x32_bf16 v[184:187], a[124:127], v[80:83], v[184:187]\n"
"s_add_u32 m0, s46, 16384\n"
"s_xor_b32 s47, s44, 262400\n"
"buffer_load_dwordx4 v58, s[36:39], s47 offen  lds\n"
"v_mfma_f32_16x16x32_bf16 v[180:183], a[120:123], v[80:83], v[180:183]\n"
"s_add_u32 m0, s46, 17408\n"
"s_xor_b32 s47, s44, 278784\n"
"buffer_load_dwordx4 v59, s[36:39], s47 offen  lds\n"
"v_mfma_f32_16x16x32_bf16 v[180:183], a[104:107], v[76:79], v[180:183]\n"
"v_fmac_f32 v198, v45, v166\n"
"v_fmac_f32 v199, v47, v167\n"
"v_mfma_f32_16x16x32_bf16 v[176:179], a[100:103], v[76:79], v[176:179]\n"
"v_mul_f32 v200, v48, v140\n"
"v_mul_f32 v201, v50, v140\n"
"v_mfma_f32_16x16x32_bf16 v[176:179], a[116:119], v[80:83], v[176:179]\n"
"v_fmac_f32 v200, v49, v168\n"
"v_fmac_f32 v201, v51, v169\n"
"v_mfma_f32_16x16x32_bf16 v[172:175], a[112:115], v[80:83], v[172:175]\n"
"v_mul_f32 v202, v52, v140\n"
"v_mul_f32 v203, v54, v140\n"
"v_mfma_f32_16x16x32_bf16 v[172:175], a[96:99], v[76:79], v[172:175]\n"
"s_waitcnt lgkmcnt(3)\n"
"v_fmac_f32 v202, v53, v170\n"
"v_fmac_f32 v203, v55, v171\n"
"v_mfma_f32_16x16x32_bf16 v[172:175], a[128:131], v[84:87], v[172:175]\n"
"s_waitcnt lgkmcnt(2)\n"
"ds_read_b128 v[100:103], v6 offset:14336\n"
"ds_read_b128 v[104:107], v7 offset:14336\n"
"v_mfma_f32_16x16x32_bf16 v[172:175], a[144:147], v[88:91], v[172:175]\n"
"s_add_u32 m0, s46, 24576\n"
"s_xor_b32 s47, s44, 393472\n"
"buffer_load_dwordx4 v58, s[36:39], s47 offen  lds\n"
"v_mfma_f32_16x16x32_bf16 v[176:179], a[148:151], v[88:91], v[176:179]\n"
"s_add_u32 m0, s46, 25600\n"
"s_xor_b32 s47, s44, 409856\n"
"buffer_load_dwordx4 v59, s[36:39], s47 offen  lds\n"
"v_mfma_f32_16x16x32_bf16 v[176:179], a[132:135], v[84:87], v[176:179]\n"
"v_cvt_pk_bf16_f32 v208, v196, v197\n"
"v_cvt_pk_bf16_f32 v209, v198, v199\n"
"v_mfma_f32_16x16x32_bf16 v[180:183], a[136:139], v[84:87], v[180:183]\n"
"v_cvt_pk_bf16_f32 v210, v200, v201\n"
"v_cvt_pk_bf16_f32 v211, v202, v203\n"
"v_mfma_f32_16x16x32_bf16 v[180:183], a[152:155], v[88:91], v[180:183]\n"
"buffer_store_dwordx4 v[208:211], v1, s[24:27], s63 offen offset:0 sc1\n"
"v_mfma_f32_16x16x32_bf16 v[184:187], a[156:159], v[88:91], v[184:187]\n"
"v_mul_f32 v24, v24, v141\n"
"v_mul_f32 v26, v26, v141\n"
"v_mfma_f32_16x16x32_bf16 v[184:187], a[140:143], v[84:87], v[184:187]\n"
"s_waitcnt lgkmcnt(3)\n"
"v_mul_f32 v28, v28, v141\n"
"v_mul_f32 v30, v30, v141\n"
"v_mfma_f32_16x16x32_bf16 v[184:187], a[172:175], v[92:95], v[184:187]\n"
"s_waitcnt lgkmcnt(2)\n"
"s_waitcnt vmcnt(13)\n"
"ds_read_b128 v[108:111], v6 offset:0\n"
"ds_read_b128 v[112:115], v7 offset:0\n"
"v_mfma_f32_16x16x32_bf16 v[184:187], a[188:191], v[96:99], v[184:187]\n"
"s_add_u32 m0, s46, 32768\n"
"s_xor_b32 s47, s44, 262656\n"
"buffer_load_dwordx4 v58, s[36:39], s47 offen  lds\n"
"v_mfma_f32_16x16x32_bf16 v[180:183], a[184:187], v[96:99], v[180:183]\n"
"s_add_u32 m0, s46, 33792\n"
"s_xor_b32 s47, s44, 279040\n"
"buffer_load_dwordx4 v59, s[36:39], s47 offen  lds\n"
"v_mfma_f32_16x16x32_bf16 v[180:183], a[168:171], v[92:95], v[180:183]\n"
"v_mul_f32 v32, v32, v141\n"
"v_mul_f32 v34, v34, v141\n"
"v_mfma_f32_16x16x32_bf16 v[176:179], a[164:167], v[92:95], v[176:179]\n"
"v_mul_f32 v36, v36, v141\n"
"v_mul_f32 v38, v38, v141\n"
"v_mfma_f32_16x16x32_bf16 v[176:179], a[180:183], v[96:99], v[176:179]\n"
"v_mul_f32 v40, v40, v141\n"
"v_mul_f32 v42, v42, v141\n"
"v_mfma_f32_16x16x32_bf16 v[172:175], a[176:179], v[96:99], v[172:175]\n"
"v_mul_f32 v44, v44, v141\n"
"v_mul_f32 v46, v46, v141\n"
"v_mfma_f32_16x16x32_bf16 v[172:175], a[160:163], v[92:95], v[172:175]\n"
"s_waitcnt lgkmcnt(3)\n"
"v_mul_f32 v48, v48, v141\n"
"v_mul_f32 v50, v50, v141\n"
"v_mfma_f32_16x16x32_bf16 v[172:175], a[192:195], v[100:103], v[172:175]\n"
"s_waitcnt lgkmcnt(2)\n"
"s_waitcnt vmcnt(13)\n"
"ds_read_b128 v[116:119], v6 offset:2048\n"
"ds_read_b128 v[120:123], v7 offset:2048\n"
"v_mfma_f32_16x16x32_bf16 v[172:175], a[208:211], v[104:107], v[172:175]\n"
"s_add_u32 m0, s46, 40960\n"
"s_xor_b32 s47, s44, 393728\n"
"buffer_load_dwordx4 v58, s[36:39], s47 offen  lds\n"
"v_mfma_f32_16x16x32_bf16 v[176:179], a[212:215], v[104:107], v[176:179]\n"
"s_add_u32 m0, s46, 41984\n"
"s_xor_b32 s47, s44, 410112\n"
"buffer_load_dwordx4 v59, s[36:39], s47 offen  lds\n"
"v_mfma_f32_16x16x32_bf16 v[176:179], a[196:199], v[100:103], v[176:179]\n"
"v_mul_f32 v52, v52, v141\n"
"v_mul_f32 v54, v54, v141\n"
"v_mfma_f32_16x16x32_bf16 v[180:183], a[200:203], v[100:103], v[180:183]\n"
"v_mfma_f32_16x16x32_bf16 v[180:183], a[216:219], v[104:107], v[180:183]\n"
"v_mfma_f32_16x16x32_bf16 v[184:187], a[220:223], v[104:107], v[184:187]\n"
"v_mfma_f32_16x16x32_bf16 v[184:187], a[204:207], v[100:103], v[184:187]\n"
"s_waitcnt lgkmcnt(3)\n"
"v_mfma_f32_16x16x32_bf16 v[184:187], a[236:239], v[108:111], v[184:187]\n"
"s_waitcnt lgkmcnt(2)\n"
"v_mfma_f32_16x16x32_bf16 v[184:187], a[252:255], v[112:115], v[184:187]\n"
"v_mfma_f32_16x16x32_bf16 v[180:183], a[248:251], v[112:115], v[180:183]\n"
"v_mfma_f32_16x16x32_bf16 v[180:183], a[232:235], v[108:111], v[180:183]\n"
"v_mfma_f32_16x16x32_bf16 v[176:179], a[228:231], v[108:111], v[176:179]\n"
"v_mfma_f32_16x16x32_bf16 v[176:179], a[244:247], v[112:115], v[176:179]\n"
"v_mfma_f32_16x16x32_bf16 v[172:175], a[240:243], v[112:115], v[172:175]\n"
"v_mfma_f32_16x16x32_bf16 v[172:175], a[224:227], v[108:111], v[172:175]\n"
"s_waitcnt lgkmcnt(1)\n"
"v_mfma_f32_16x16x32_bf16 v[172:175], v[224:227], v[116:119], v[172:175]\n"
"s_waitcnt lgkmcnt(0)\n"
"v_mfma_f32_16x16x32_bf16 v[172:175], v[240:243], v[120:123], v[172:175]\n"
"v_mfma_f32_16x16x32_bf16 v[176:179], v[244:247], v[120:123], v[176:179]\n"
"v_mfma_f32_16x16x32_bf16 v[176:179], v[228:231], v[116:119], v[176:179]\n"
"v_mfma_f32_16x16x32_bf16 v[180:183], v[232:235], v[116:119], v[180:183]\n"
"v_mfma_f32_16x16x32_bf16 v[180:183], v[248:251], v[120:123], v[180:183]\n"
"v_mfma_f32_16x16x32_bf16 v[184:187], v[252:255], v[120:123], v[184:187]\n"
"v_mfma_f32_16x16x32_bf16 v[184:187], v[236:239], v[116:119], v[184:187]\n"
"s_nop 1\n"
"v_fmac_f32 v24, v25, v172\n"
"v_fmac_f32 v26, v27, v173\n"
"v_fmac_f32 v28, v29, v174\n"
"v_fmac_f32 v30, v31, v175\n"
"v_fmac_f32 v32, v33, v176\n"
"v_fmac_f32 v34, v35, v177\n"
"v_fmac_f32 v36, v37, v178\n"
"v_fmac_f32 v38, v39, v179\n"
"v_cvt_pk_bf16_f32 v204, v24, v26\n"
"v_cvt_pk_bf16_f32 v205, v28, v30\n"
"v_cvt_pk_bf16_f32 v206, v32, v34\n"
"v_cvt_pk_bf16_f32 v207, v36, v38\n"
"buffer_store_dwordx4 v[204:207], v1, s[24:27], s62 offen offset:1024 sc1\n"
"v_fmac_f32 v40, v41, v180\n"
"v_fmac_f32 v42, v43, v181\n"
"v_fmac_f32 v44, v45, v182\n"
"v_fmac_f32 v46, v47, v183\n"
"v_fmac_f32 v48, v49, v184\n"
"v_fmac_f32 v50, v51, v185\n"
"v_fmac_f32 v52, v53, v186\n"
"v_fmac_f32 v54, v55, v187\n"
"v_cvt_pk_bf16_f32 v208, v40, v42\n"
"v_cvt_pk_bf16_f32 v209, v44, v46\n"
"v_cvt_pk_bf16_f32 v210, v48, v50\n"
"v_cvt_pk_bf16_f32 v211, v52, v54\n"
"buffer_store_dwordx4 v[208:211], v1, s[24:27], s63 offen offset:1024 sc1\n"
"s_waitcnt lgkmcnt(0)\n"
"s_barrier\n"
"s_mov_b32 s30, 1\n"
"s_branch 1b\n"
"2:\n"
".Lp1_3_%=:\n"
"s_cbranch_execz .Lpret%=\n"
"s_nop 3\n"
"s_barrier\n"
"v_mfma_f32_16x16x32_bf16 v[208:211], v[168:171], a[0:3], v[208:211]\n"
"v_mfma_f32_16x16x32_bf16 v[208:211], v[172:175], a[16:19], v[208:211]\n"
"v_mfma_f32_16x16x32_bf16 v[212:215], v[172:175], a[20:23], v[212:215]\n"
"v_mfma_f32_16x16x32_bf16 v[212:215], v[168:171], a[4:7], v[212:215]\n"
"s_add_u32 s93, s62, 327680\n"
"buffer_load_dwordx4 v[76:79], v1, s[24:27], s93 offen offset:0 sc1\n"
"v_mfma_f32_16x16x32_bf16 v[216:219], v[168:171], a[8:11], v[216:219]\n"
"v_cvt_pk_bf16_f32 v18, v28, v29\n"
"v_mfma_f32_16x16x32_bf16 v[216:219], v[172:175], a[24:27], v[216:219]\n"
"v_cvt_pk_bf16_f32 v19, v30, v31\n"
"v_mfma_f32_16x16x32_bf16 v[220:223], v[172:175], a[28:31], v[220:223]\n"
"v_cvt_pk_bf16_f32 v20, v32, v33\n"
"v_mfma_f32_16x16x32_bf16 v[220:223], v[168:171], a[12:15], v[220:223]\n"
"s_waitcnt vmcnt(14)\n"
"buffer_load_dwordx4 v[80:83], v1, s[24:27], s93 offen offset:1024 sc1\n"
"v_cvt_pk_bf16_f32 v21, v34, v35\n"
"v_mfma_f32_16x16x32_bf16 v[220:223], v[176:179], a[44:47], v[220:223]\n"
"s_waitcnt vmcnt(14)\n"
"s_add_u32 s65, s63, s67\n"
"buffer_store_dwordx4 v[18:21], v1, s[24:27], s65 offen sc1\n"
"v_mfma_f32_16x16x32_bf16 v[220:223], v[180:183], a[60:63], v[220:223]\n"
"v_cvt_pk_bf16_f32 v22, v36, v37\n"
"v_mfma_f32_16x16x32_bf16 v[216:219], v[180:183], a[56:59], v[216:219]\n"
"v_cvt_pk_bf16_f32 v23, v38, v39\n"
"v_mfma_f32_16x16x32_bf16 v[216:219], v[176:179], a[40:43], v[216:219]\n"
"buffer_load_dwordx4 v[84:87], v1, s[24:27], s93 offen offset:2048 sc1\n"
"v_cvt_pk_bf16_f32 v24, v40, v41\n"
"v_mfma_f32_16x16x32_bf16 v[212:215], v[176:179], a[36:39], v[212:215]\n"
"v_cvt_pk_bf16_f32 v25, v42, v43\n"
"v_mfma_f32_16x16x32_bf16 v[212:215], v[180:183], a[52:55], v[212:215]\n"
"s_add_u32 s65, s65, 36864\n"
"buffer_store_dwordx4 v[22:25], v1, s[24:27], s65 offen sc1\n"
"v_mfma_f32_16x16x32_bf16 v[208:211], v[180:183], a[48:51], v[208:211]\n"
"s_add_u32 s67, s60, 8\n"
"s_and_b32 s67, s67, 7\n"
"s_lshl_b32 s67, s67, 12\n"
"v_mfma_f32_16x16x32_bf16 v[208:211], v[176:179], a[32:35], v[208:211]\n"
"s_waitcnt vmcnt(16)\n"
"buffer_load_dwordx4 v[88:91], v1, s[24:27], s93 offen offset:3072 sc1\n"
"v_mfma_f32_16x16x32_bf16 v[208:211], v[184:187], a[64:67], v[208:211]\n"
"s_waitcnt vmcnt(16)\n"
"s_waitcnt vmcnt(12)\n"
"v_cmp_ne_u64 vcc, s[96:97], v[16:17]\n"
"s_nop 4\n"
"s_cbranch_vccz 77f\n"
"78:\n"
"buffer_load_dwordx2 v[16:17], v7, s[24:27], s52 offen sc1\n"
"s_waitcnt vmcnt(0)\n"
"v_cmp_ne_u64 vcc, s[96:97], v[16:17]\n"
"s_nop 4\n"
"s_cbranch_vccz 77f\n"
"s_sleep 1\n"
"s_sub_u32 s75, s75, 1\n"
"s_cmp_gt_i32 s75, 0\n"
"s_cbranch_scc1 78b\n"
"77:\n"
"s_add_u32 s33, s60, 5\n"
"s_and_b32 s33, s33, 7\n"
"s_mul_i32 s33, s33, 73728\n"
"s_lshl_b32 s34, s60, 12\n"
"s_add_u32 s33, s33, s34\n"
"s_add_u32 s33, s33, s53\n"
"s_add_u32 s34, s33, 36864\n"
"buffer_load_dwordx4 v[28:31], v1, s[24:27], s33 offen sc1\n"
"buffer_load_dwordx4 v[32:35], v1, s[24:27], s34 offen sc1\n"
"s_add_u32 s33, s60, 4\n"
"s_and_b32 s33, s33, 7\n"
"s_mul_i32 s33, s33, 73728\n"
"s_lshl_b32 s34, s60, 12\n"
"s_add_u32 s33, s33, s34\n"
"s_add_u32 s33, s33, s53\n"
"s_add_u32 s34, s33, 36864\n"
"buffer_load_dwordx4 v[36:39], v1, s[24:27], s33 offen sc1\n"
"buffer_load_dwordx4 v[40:43], v1, s[24:27], s34 offen sc1\n"
"v_mfma_f32_16x16x32_bf16 v[208:211], v[188:191], a[80:83], v[208:211]\n"
"v_mfma_f32_16x16x32_bf16 v[212:215], v[188:191], a[84:87], v[212:215]\n"
"v_mfma_f32_16x16x32_bf16 v[212:215], v[184:187], a[68:71], v[212:215]\n"
"v_mfma_f32_16x16x32_bf16 v[216:219], v[184:187], a[72:75], v[216:219]\n"
"v_mfma_f32_16x16x32_bf16 v[216:219], v[188:191], a[88:91], v[216:219]\n"
"v_mfma_f32_16x16x32_bf16 v[220:223], v[188:191], a[92:95], v[220:223]\n"
"v_mfma_f32_16x16x32_bf16 v[220:223], v[184:187], a[76:79], v[220:223]\n"
"s_barrier\n"
"v_mfma_f32_16x16x32_bf16 v[220:223], v[192:195], a[108:111], v[220:223]\n"
"v_mfma_f32_16x16x32_bf16 v[220:223], v[196:199], a[124:127], v[220:223]\n"
"v_mfma_f32_16x16x32_bf16 v[216:219], v[196:199], a[120:123], v[216:219]\n"
"v_mfma_f32_16x16x32_bf16 v[216:219], v[192:195], a[104:107], v[216:219]\n"
"v_mfma_f32_16x16x32_bf16 v[212:215], v[192:195], a[100:103], v[212:215]\n"
"v_mfma_f32_16x16x32_bf16 v[212:215], v[196:199], a[116:119], v[212:215]\n"
"v_mfma_f32_16x16x32_bf16 v[208:211], v[196:199], a[112:115], v[208:211]\n"
"v_mfma_f32_16x16x32_bf16 v[208:211], v[192:195], a[96:99], v[208:211]\n"
"v_mfma_f32_16x16x32_bf16 v[208:211], v[200:203], a[128:131], v[208:211]\n"
"s_waitcnt vmcnt(15)\n"
"v_mfma_f32_16x16x32_bf16 v[208:211], v[204:207], a[144:147], v[208:211]\n"
"v_mfma_f32_16x16x32_bf16 v[212:215], v[204:207], a[148:151], v[212:215]\n"
"v_mfma_f32_16x16x32_bf16 v[212:215], v[200:203], a[132:135], v[212:215]\n"
"v_mfma_f32_16x16x32_bf16 v[216:219], v[200:203], a[136:139], v[216:219]\n"
"v_mfma_f32_16x16x32_bf16 v[216:219], v[204:207], a[152:155], v[216:219]\n"
"v_mfma_f32_16x16x32_bf16 v[220:223], v[204:207], a[156:159], v[220:223]\n"
"v_mfma_f32_16x16x32_bf16 v[220:223], v[200:203], a[140:143], v[220:223]\n"
"s_waitcnt vmcnt(14)\n"
"v_mfma_f32_16x16x32_bf16 v[220:223], v[60:63], a[172:175], v[220:223]\n"
"s_waitcnt vmcnt(13)\n"
"v_mfma_f32_16x16x32_bf16 v[220:223], v[64:67], a[188:191], v[220:223]\n"
"v_mfma_f32_16x16x32_bf16 v[216:219], v[64:67], a[184:187], v[216:219]\n"
"v_mfma_f32_16x16x32_bf16 v[216:219], v[60:63], a[168:171], v[216:219]\n"
"v_mfma_f32_16x16x32_bf16 v[212:215], v[60:63], a[164:167], v[212:215]\n"
"s_add_u32 s33, s60, 7\n"
"s_and_b32 s33, s33, 7\n"
"s_mul_i32 s33, s33, 512\n"
"s_add_u32 s33, s33, s54\n"
"s_waitcnt vmcnt(5)\n"
"s_mov_b64 exec, 1\n"
"buffer_store_dwordx2 v[4:5], off, s[24:27], s33 offset:56 sc1\n"
"s_mov_b64 exec, -1\n"
"v_mfma_f32_16x16x32_bf16 v[212:215], v[64:67], a[180:183], v[212:215]\n"
"v_mfma_f32_16x16x32_bf16 v[208:211], v[64:67], a[176:179], v[208:211]\n"
"v_mfma_f32_16x16x32_bf16 v[208:211], v[60:63], a[160:163], v[208:211]\n"
"s_barrier\n"
"v_mfma_f32_16x16x32_bf16 v[208:211], v[68:71], a[192:195], v[208:211]\n"
"v_mfma_f32_16x16x32_bf16 v[208:211], v[72:75], a[208:211], v[208:211]\n"
"v_mfma_f32_16x16x32_bf16 v[212:215], v[72:75], a[212:215], v[212:215]\n"
"v_mfma_f32_16x16x32_bf16 v[212:215], v[68:71], a[196:199], v[212:215]\n"
"v_mfma_f32_16x16x32_bf16 v[216:219], v[68:71], a[200:203], v[216:219]\n"
"v_mfma_f32_16x16x32_bf16 v[216:219], v[72:75], a[216:219], v[216:219]\n"
"v_mfma_f32_16x16x32_bf16 v[220:223], v[72:75], a[220:223], v[220:223]\n"
"v_mfma_f32_16x16x32_bf16 v[220:223], v[68:71], a[204:207], v[220:223]\n"
"v_mfma_f32_16x16x32_bf16 v[220:223], v[76:79], a[236:239], v[220:223]\n"
"v_mfma_f32_16x16x32_bf16 v[220:223], v[80:83], a[252:255], v[220:223]\n"
"v_mfma_f32_16x16x32_bf16 v[216:219], v[80:83], a[248:251], v[216:219]\n"
"v_mfma_f32_16x16x32_bf16 v[216:219], v[76:79], a[232:235], v[216:219]\n"
"v_mfma_f32_16x16x32_bf16 v[212:215], v[76:79], a[228:231], v[212:215]\n"
"s_waitcnt vmcnt(4)\n"
"v_mfma_f32_16x16x32_bf16 v[208:211], v[10:13], v[28:31], v[208:211]\n"
"v_mfma_f32_16x16x32_bf16 v[212:215], v[80:83], a[244:247], v[212:215]\n"
"v_mfma_f32_16x16x32_bf16 v[212:215], v[8:11], v[28:31], v[212:215]\n"
"v_mfma_f32_16x16x32_bf16 v[208:211], v[80:83], a[240:243], v[208:211]\n"
"s_waitcnt vmcnt(3)\n"
"v_mfma_f32_16x16x32_bf16 v[216:219], v[10:13], v[32:35], v[216:219]\n"
"v_mfma_f32_16x16x32_bf16 v[208:211], v[76:79], a[224:227], v[208:211]\n"
"v_mfma_f32_16x16x32_bf16 v[220:223], v[8:11], v[32:35], v[220:223]\n"
"v_mfma_f32_16x16x32_bf16 v[208:211], v[84:87], v[224:227], v[208:211]\n"
"s_waitcnt vmcnt(2)\n"
"v_mfma_f32_16x16x32_bf16 v[208:211], v[10:13], v[36:39], v[208:211]\n"
"v_mfma_f32_16x16x32_bf16 v[208:211], v[88:91], v[240:243], v[208:211]\n"
"v_mfma_f32_16x16x32_bf16 v[212:215], v[8:11], v[36:39], v[212:215]\n"
"v_mfma_f32_16x16x32_bf16 v[212:215], v[88:91], v[244:247], v[212:215]\n"
"s_waitcnt vmcnt(1)\n"
"v_mfma_f32_16x16x32_bf16 v[216:219], v[10:13], v[40:43], v[216:219]\n"
"v_mfma_f32_16x16x32_bf16 v[212:215], v[84:87], v[228:231], v[212:215]\n"
"v_mfma_f32_16x16x32_bf16 v[220:223], v[8:11], v[40:43], v[220:223]\n"
"v_mfma_f32_16x16x32_bf16 v[216:219], v[84:87], v[232:235], v[216:219]\n"
"v_mfma_f32_16x16x32_bf16 v[216:219], v[88:91], v[248:251], v[216:219]\n"
"v_mfma_f32_16x16x32_bf16 v[220:223], v[88:91], v[252:255], v[220:223]\n"
"v_mfma_f32_16x16x32_bf16 v[220:223], v[84:87], v[236:239], v[220:223]\n"
"v_and_b32 v4, 15, v0\n"
"v_lshrrev_b32 v5, 4, v0\n"
"v_mul_u32_u24 v18, 144, v4\n"
"v_lshl_add_u32 v18, v5, 3, v18\n"
"s_lshl_b32 s33, s28, 5\n"
"v_add_u32 v18, s33, v18\n"
"s_add_u32 s52, s51, 40\n"
"v_min_u32 v7, 2, v0\n"
"v_lshlrev_b32 v7, 3, v7\n"
"80:\n"
"buffer_load_dwordx2 v[16:17], v7, s[24:27], s52 offen sc1\n"
"s_waitcnt vmcnt(0)\n"
"v_cmp_ne_u64 vcc, s[96:97], v[16:17]\n"
"s_nop 4\n"
"s_cbranch_vccz 79f\n"
"s_sleep 1\n"
"s_sub_u32 s75, s75, 1\n"
"s_cmp_gt_i32 s75, 0\n"
"s_cbranch_scc1 80b\n"
"79:\n"
"s_add_u32 s33, s60, 3\n"
"s_and_b32 s33, s33, 7\n"
"s_mul_i32 s33, s33, 73728\n"
"s_lshl_b32 s34, s60, 12\n"
"s_add_u32 s33, s33, s34\n"
"s_add_u32 s33, s33, s53\n"
"s_add_u32 s34, s33, 36864\n"
"buffer_load_dwordx4 v[36:39], v1, s[24:27], s33 offen sc1\n"
"buffer_load_dwordx4 v[40:43], v1, s[24:27], s34 offen sc1\n"
"s_add_u32 s33, s60, 2\n"
"s_and_b32 s33, s33, 7\n"
"s_mul_i32 s33, s33, 73728\n"
"s_lshl_b32 s34, s60, 12\n"
"s_add_u32 s33, s33, s34\n"
"s_add_u32 s33, s33, s53\n"
"s_add_u32 s34, s33, 36864\n"
"buffer_load_dwordx4 v[44:47], v1, s[24:27], s33 offen sc1\n"
"buffer_load_dwordx4 v[48:51], v1, s[24:27], s34 offen sc1\n"
"s_add_u32 s33, s60, 1\n"
"s_and_b32 s33, s33, 7\n"
"s_mul_i32 s33, s33, 73728\n"
"s_lshl_b32 s34, s60, 12\n"
"s_add_u32 s33, s33, s34\n"
"s_add_u32 s33, s33, s53\n"
"s_add_u32 s34, s33, 36864\n"
"buffer_load_dwordx4 v[52:55], v1, s[24:27], s33 offen sc1\n"
"buffer_load_dwordx4 v[56:59], v1, s[24:27], s34 offen sc1\n"
"v_lshrrev_b32 v19, 3, v0\n"
"v_and_b32 v20, 7, v0\n"
"v_lshlrev_b32 v20, 4, v20\n"
"v_mul_u32_u24 v21, 144, v19\n"
"v_add_u32 v21, v21, v20\n"
"s_mul_i32 s33, s28, 1152\n"
"v_add_u32 v21, s33, v21\n"
"v_mul_u32_u24 v22, 1152, v19\n"
"v_add_u32 v22, v22, v20\n"
"v_lshrrev_b32 v23, 5, v0\n"
"v_mul_u32_u24 v23, 13824, v23\n"
"v_add_u32 v23, v23, v22\n"
"s_mov_b64 s[40:41], s[16:17]\n"
"s_mov_b32 s42, 2359296\n"
"s_mov_b32 s43, 0x20000\n"
"s_mul_i32 s35, s61, 73728\n"
"s_and_b32 s33, s60, 1\n"
"s_lshl_b32 s33, s33, 3\n"
"s_lshr_b32 s34, s60, 1\n"
"s_xor_b32 s34, s34, s61\n"
"s_and_b32 s34, s34, 3\n"
"s_add_u32 s33, s33, s34\n"
"s_mul_i32 s33, s33, 4608\n"
"s_add_u32 s31, s33, s35\n"
"s_lshl_b32 s33, s60, 7\n"
"s_add_u32 s35, s35, s33\n"
"s_mul_i32 s33, s28, 9216\n"
"s_add_u32 s35, s35, s33\n"
"s_add_u32 s85, s35, 36864\n"
"s_waitcnt vmcnt(5)\n"
"v_mfma_f32_16x16x32_bf16 v[208:211], v[10:13], v[36:39], v[208:211]\n"
"v_mfma_f32_16x16x32_bf16 v[212:215], v[8:11], v[36:39], v[212:215]\n"
"s_waitcnt vmcnt(4)\n"
"v_mfma_f32_16x16x32_bf16 v[216:219], v[10:13], v[40:43], v[216:219]\n"
"v_mfma_f32_16x16x32_bf16 v[220:223], v[8:11], v[40:43], v[220:223]\n"
"s_waitcnt vmcnt(3)\n"
"v_mfma_f32_16x16x32_bf16 v[208:211], v[10:13], v[44:47], v[208:211]\n"
"v_mfma_f32_16x16x32_bf16 v[212:215], v[8:11], v[44:47], v[212:215]\n"
"s_waitcnt vmcnt(2)\n"
"v_mfma_f32_16x16x32_bf16 v[216:219], v[10:13], v[48:51], v[216:219]\n"
"v_mfma_f32_16x16x32_bf16 v[220:223], v[8:11], v[48:51], v[220:223]\n"
"s_waitcnt vmcnt(1)\n"
"v_mfma_f32_16x16x32_bf16 v[208:211], v[10:13], v[52:55], v[208:211]\n"
"v_mfma_f32_16x16x32_bf16 v[212:215], v[8:11], v[52:55], v[212:215]\n"
"s_waitcnt vmcnt(0)\n"
"v_mfma_f32_16x16x32_bf16 v[216:219], v[10:13], v[56:59], v[216:219]\n"
"v_mfma_f32_16x16x32_bf16 v[220:223], v[8:11], v[56:59], v[220:223]\n"
"s_nop 3\n"
"v_cvt_pk_bf16_f32 v208, v208, v209\n"
"v_cvt_pk_bf16_f32 v209, v210, v211\n"
"ds_write_b64 v18, v[208:209] offset:0\n"
"v_cvt_pk_bf16_f32 v212, v212, v213\n"
"v_cvt_pk_bf16_f32 v213, v214, v215\n"
".Lp2_3_%=:\n"
"s_cbranch_execz .Lpret%=\n"
"ds_write_b64 v18, v[212:213] offset:2304\n"
"v_cvt_pk_bf16_f32 v216, v216, v217\n"
"v_cvt_pk_bf16_f32 v217, v218, v219\n"
"ds_write_b64 v18, v[216:217] offset:4608\n"
"v_cvt_pk_bf16_f32 v220, v220, v221\n"
"v_cvt_pk_bf16_f32 v221, v222, v223\n"
"ds_write_b64 v18, v[220:221] offset:6912\n"
"s_waitcnt lgkmcnt(0)\n"
"s_barrier\n"
"ds_read_b128 v[28:31], v21 offset:0\n"
"ds_read_b128 v[32:35], v21 offset:4608\n"
"ds_read_b128 v[36:39], v21 offset:9216\n"
"s_waitcnt lgkmcnt(2)\n"
"buffer_store_dwordx4 v[28:31], v22, s[40:43], s35 offen \n"
"s_waitcnt lgkmcnt(1)\n"
"buffer_store_dwordx4 v[32:35], v22, s[40:43], s85 offen \n"
"s_waitcnt lgkmcnt(0)\n"
"s_cmp_eq_u32 s28, 0\n"
"s_cbranch_scc0 81f\n"
"buffer_store_dwordx4 v[36:39], v23, s[40:43], s31 offen offset:1024 \n"
"81:\n"
".Lend%=:\n"
    : "+v"(tid) : "s"(blockIdx.x), "s"(__builtin_amdgcn_kernarg_segment_ptr())
    : "v1","v2","v3","v4","v5","v6","v7","v8","v9","v10","v11","v12","v13","v14","v15","v16","v17","v18","v19","v20","v21","v22","v23","v24","v25","v26","v27","v28","v29","v30","v31","v32","v33","v34","v35","v36","v37","v38","v39","v40","v41","v42","v43","v44","v45","v46","v47","v48","v49","v50","v51","v52","v53","v54","v55","v56","v57","v58","v59","v60","v61","v62","v63","v64","v65","v66","v67","v68","v69","v70","v71","v72","v73","v74","v75","v76","v77","v78","v79","v80","v81","v82","v83","v84","v85","v86","v87","v88","v89","v90","v91","v92","v93","v94","v95","v96","v97","v98","v99","v100","v101","v102","v103","v104","v105","v106","v107","v108","v109","v110","v111","v112","v113","v114","v115","v116","v117","v118","v119","v120","v121","v122","v123","v124","v125","v126","v127","v128","v129","v130","v131","v132","v133","v134","v135","v136","v137","v138","v139","v140","v141","v142","v143","v144","v145","v146","v147","v148","v149","v150","v151","v152","v153","v154","v155","v156","v157","v158","v159","v160","v161","v162","v163","v164","v165","v166","v167","v168","v169","v170","v171","v172","v173","v174","v175","v176","v177","v178","v179","v180","v181","v182","v183","v184","v185","v186","v187","v188","v189","v190","v191","v192","v193","v194","v195","v196","v197","v198","v199","v200","v201","v202","v203","v204","v205","v206","v207","v208","v209","v210","v211","v212","v213","v214","v215","v216","v217","v218","v219","v220","v221","v222","v223","v224","v225","v226","v227","v228","v229","v230","v231","v232","v233","v234","v235","v236","v237","v238","v239","v240","v241","v242","v243","v244","v245","v246","v247","v248","v249","v250","v251","v252","v253","v254","v255",
      "a0","a1","a2","a3","a4","a5","a6","a7","a8","a9","a10","a11","a12","a13","a14","a15","a16","a17","a18","a19","a20","a21","a22","a23","a24","a25","a26","a27","a28","a29","a30","a31","a32","a33","a34","a35","a36","a37","a38","a39","a40","a41","a42","a43","a44","a45","a46","a47","a48","a49","a50","a51","a52","a53","a54","a55","a56","a57","a58","a59","a60","a61","a62","a63","a64","a65","a66","a67","a68","a69","a70","a71","a72","a73","a74","a75","a76","a77","a78","a79","a80","a81","a82","a83","a84","a85","a86","a87","a88","a89","a90","a91","a92","a93","a94","a95","a96","a97","a98","a99","a100","a101","a102","a103","a104","a105","a106","a107","a108","a109","a110","a111","a112","a113","a114","a115","a116","a117","a118","a119","a120","a121","a122","a123","a124","a125","a126","a127","a128","a129","a130","a131","a132","a133","a134","a135","a136","a137","a138","a139","a140","a141","a142","a143","a144","a145","a146","a147","a148","a149","a150","a151","a152","a153","a154","a155","a156","a157","a158","a159","a160","a161","a162","a163","a164","a165","a166","a167","a168","a169","a170","a171","a172","a173","a174","a175","a176","a177","a178","a179","a180","a181","a182","a183","a184","a185","a186","a187","a188","a189","a190","a191","a192","a193","a194","a195","a196","a197","a198","a199","a200","a201","a202","a203","a204","a205","a206","a207","a208","a209","a210","a211","a212","a213","a214","a215","a216","a217","a218","a219","a220","a221","a222","a223","a224","a225","a226","a227","a228","a229","a230","a231","a232","a233","a234","a235","a236","a237","a238","a239","a240","a241","a242","a243","a244","a245","a246","a247","a248","a249","a250","a251","a252","a253","a254","a255",
      "s4","s5","s6","s7","s8","s9","s10","s11","s12","s13","s14","s15","s16","s17","s18","s19","s20","s21","s22","s23","s24","s25","s26","s27","s28","s29","s30","s31","s33","s34","s35","s36","s37","s38","s39","s40","s41","s42","s43","s44","s45","s46","s47","s48","s49","s50","s51","s52","s53","s54","s55","s56","s57","s58","s59","s60","s61","s62","s63","s64","s65","s66","s67","s68","s69","s70","s71","s72","s73","s74","s75","s76","s77","s78","s79","s80","s81","s82","s83","s84","s85","s86","s87","s88","s89","s90","s91","s92","s93","s94","s95","s96","s97","s98","s99", "vcc", "scc", "memory");
}

}

extern "C" void kernel_launch(void* const* d_in, const int* in_sizes, int n_in, void* d_out, int out_size, void* d_ws,
                              size_t ws_size, hipStream_t stream) {
  using namespace fk;
  const u16* x0 = (const u16*)d_in[0]; const u16* Cr = (const u16*)d_in[1]; const u16* Ci = (const u16*)d_in[2];
  const u16* Wr = (const u16*)d_in[3]; const u16* Wi = (const u16*)d_in[4];
  const u16* w1 = (const u16*)d_in[6]; const u16* w2 = (const u16*)d_in[7];
  char* ws = (char*)d_ws;
  constexpr int LDS = 163840;
  (void)hipFuncSetAttribute((const void*)k_main, hipFuncAttributeMaxDynamicSharedMemorySize, LDS);
  k_main<<<256, 256, LDS, stream>>>(ws, x0, Cr, Ci, Wr, Wi, (u16*)d_out, w1, w2);
}
```
